```python
import torch

def _softmax(x, axis=-1):
    return torch.softmax(x, dim=axis)
def _gelu(x):
    return torch.nn.functional.gelu(x, approximate="tanh")
def _silu(x):
    return torch.nn.functional.silu(x)
def _sigmoid(x):
    return torch.sigmoid(x)
def _layernorm(x, axis=-1, eps=1e-5):
    m = x.mean(dim=axis, keepdim=True)
    v = x.var(dim=axis, keepdim=True, unbiased=False)
    return (x - m) / torch.sqrt(v + eps)
def _scatter_add(tgt, idx, src, axis=0):
    return tgt.clone().index_add_(axis, idx, src)
def _segment_sum(src, seg, n):
    out = torch.zeros((n,) + src.shape[1:], dtype=src.dtype)
    return out.index_add_(0, seg, src)
def _pad(x, pw):
    flat = tuple(v for pair in reversed(pw) for v in pair)
    return torch.nn.functional.pad(x, flat)

kernel_name = 'parallel_sum__spectral_gfnet__dense_activation_mish__prompt'

def setup_inputs():

    _w = (lambda a: (lambda t: t.to(torch.bfloat16) if t.is_floating_point() else t)(torch.as_tensor(a)))
    def _A_setup():
        B, S, D = 1, 2048, 1024
        Df = D // 2 + 1
        return {
            "x": _w((lambda *s: torch.randn(*s, dtype=torch.bfloat16) * 0.05)(B, S, D)),
            "w_r": _w((lambda *s: torch.randn(*s, dtype=torch.bfloat16) * 0.8)(S, Df) * 0.02),
            "w_i": _w((lambda *s: torch.randn(*s, dtype=torch.bfloat16) * 0.8)(S, Df) * 0.02),
        }
    _A_in = _A_setup()

    def _B_setup():
        B, S, D, F = 1, 2048, 1024, 3584
        return {
            "x": _w((lambda *s: torch.randn(*s, dtype=torch.bfloat16) * 0.05)(B, S, D)),
            "w1": _w((lambda *s: torch.randn(*s, dtype=torch.bfloat16) * 0.8)(D, F) * (D ** -0.5)),
            "b1": _w((lambda *s: torch.randn(*s, dtype=torch.bfloat16) * 0.2)(F)),
            "w2": _w((lambda *s: torch.randn(*s, dtype=torch.bfloat16) * 0.1)(F, D) * (F ** -0.5)),
        }
    _B_in = _B_setup()

    return {
        "A_x": _A_in["x"],
        "A_w_r": _A_in["w_r"],
        "A_w_i": _A_in["w_i"],
        "B_x": _B_in["x"],
        "B_w1": _B_in["w1"],
        "B_b1": _B_in["b1"],
        "B_w2": _B_in["w2"],
    }

def reference(A_x, A_w_r, A_w_i, B_x, B_w1, B_b1, B_w2):
    def _A_ref(x, w_r, w_i):
        B, S, D = x.shape
        X = (lambda x, axes=(-2,-1): torch.fft.rfft2(x.float(), dim=axes))(x, axes=(-2, -1))
        W = w_r + 1j * w_i
        X = X * W
        y = (lambda x, s, axes=(-2,-1): torch.fft.irfft2(x, s=s, dim=axes).to(torch.bfloat16))(X, (S, D), axes=(-2, -1))
        return y

    def _B_ref(x, w1, b1, w2):
        h = torch.einsum("bsd,df->bsf", x, w1) + b1
        sp = torch.log1p(torch.exp(-torch.abs(h))) + torch.where(h > 0, h, torch.zeros_like(h))
        act = h * torch.tanh(sp)
        return torch.einsum("bsf,fd->bsd", act, w2)

    x = A_x
    xn = _layernorm(x, axis=-1)
    return x + _A_ref(xn, A_w_r, A_w_i) + _B_ref(xn, B_w1, B_b1, B_w2)
```

```cpp
#include <hip/hip_runtime.h>
#include <hip/hip_bf16.h>
#include <stdint.h>
typedef unsigned short u16;
#define S_ 2048
#define D_ 1024
#define F_ 3584
#define DF_ 513
__device__ __forceinline__ float bf2f(u16 v){ return __uint_as_float(((unsigned)v)<<16); }
__device__ __forceinline__ u16 f2bf(float f){ unsigned u=__float_as_uint(f); u += 0x7fffu + ((u>>16)&1u); return (u16)(u>>16);}
__device__ __forceinline__ float rbf(float f){ return bf2f(f2bf(f)); }

__global__ void k_ln(const u16* x, u16* xn){
  int r = blockIdx.x; int t = threadIdx.x;
  __shared__ float red[256];
  float v[4]; float s=0;
  for(int i=0;i<4;i++){ v[i]=bf2f(x[r*D_+t*4+i]); s+=v[i]; }
  red[t]=s; __syncthreads();
  for(int o=128;o>0;o>>=1){ if(t<o) red[t]+=red[t+o]; __syncthreads(); }
  float mean=red[0]/D_; __syncthreads();
  float q=0; for(int i=0;i<4;i++){ float d=v[i]-mean; q+=d*d; }
  red[t]=q; __syncthreads();
  for(int o=128;o>0;o>>=1){ if(t<o) red[t]+=red[t+o]; __syncthreads(); }
  float var=red[0]/D_;
  float m=rbf(mean), vv=rbf(var);
  float sd=rbf(sqrtf(rbf(vv+1e-5f)));
  for(int i=0;i<4;i++){ xn[r*D_+t*4+i]=f2bf(rbf(v[i]-m)/sd); }
}
__global__ void k_rowdft(const u16* xn, float2* X){
  int s=blockIdx.x; __shared__ float row[D_]; __shared__ float2 tw[D_];
  for(int i=threadIdx.x;i<D_;i+=blockDim.x){ row[i]=bf2f(xn[s*D_+i]); float sn,cs; sincospif(-2.0f*i/D_,&sn,&cs); tw[i]=make_float2(cs,sn);}
  __syncthreads();
  for(int k=threadIdx.x;k<DF_;k+=blockDim.x){ float re=0,im=0; for(int d=0;d<D_;d++){ float2 w=tw[(k*d)&(D_-1)]; re+=row[d]*w.x; im+=row[d]*w.y;} X[s*DF_+k]=make_float2(re,im);}
}
__global__ void k_col(const float2* X, const u16* wr, const u16* wi, float2* Y){
  int k=blockIdx.x; __shared__ float2 col[S_]; __shared__ float2 tw[S_]; __shared__ float2 col2[S_];
  for(int i=threadIdx.x;i<S_;i+=blockDim.x){ col[i]=X[i*DF_+k]; float sn,cs; sincospif(-2.0f*i/S_,&sn,&cs); tw[i]=make_float2(cs,sn);}
  __syncthreads();
  for(int j=threadIdx.x;j<S_;j+=blockDim.x){ float re=0,im=0; for(int s=0;s<S_;s++){ float2 w=tw[(j*s)&(S_-1)]; float2 c=col[s]; re+=c.x*w.x-c.y*w.y; im+=c.x*w.y+c.y*w.x;}
    float a=bf2f(wr[j*DF_+k]), b=bf2f(wi[j*DF_+k]); col2[j]=make_float2(re*a-im*b, re*b+im*a);}
  __syncthreads();
  for(int s=threadIdx.x;s<S_;s+=blockDim.x){ float re=0,im=0; for(int j=0;j<S_;j++){ float2 w=tw[(j*s)&(S_-1)]; float2 c=col2[j]; re+=c.x*w.x+c.y*w.y; im+=-c.x*w.y+c.y*w.x;}
    Y[s*DF_+k]=make_float2(re/S_,im/S_);}
}
__global__ void k_irow(const float2* Y, float* y){
  int s=blockIdx.x; __shared__ float2 row[DF_]; __shared__ float2 tw[D_];
  for(int i=threadIdx.x;i<D_;i+=blockDim.x){ if(i<DF_) row[i]=Y[s*DF_+i]; float sn,cs; sincospif(2.0f*i/D_,&sn,&cs); tw[i]=make_float2(cs,sn);}
  __syncthreads();
  for(int d=threadIdx.x;d<D_;d+=blockDim.x){ float acc=row[0].x + ((d&1)?-row[512].x:row[512].x); for(int k=1;k<512;k++){ float2 w=tw[(k*d)&(D_-1)]; acc+=2.f*(row[k].x*w.x-row[k].y*w.y);} y[s*D_+d]=acc/D_;}
}
__global__ void k_g1(const u16* xn, const u16* w1, const u16* b1, u16* h){
  int s=blockIdx.y; int f=blockIdx.x*256+threadIdx.x; __shared__ float row[D_];
  for(int i=threadIdx.x;i<D_;i+=256) row[i]=bf2f(xn[s*D_+i]);
  __syncthreads();
  float acc=0; for(int d=0;d<D_;d++) acc+=row[d]*bf2f(w1[d*F_+f]);
  float hh=rbf(rbf(acc)+bf2f(b1[f]));
  float sp=log1pf(expf(-fabsf(hh)))+fmaxf(hh,0.f);
  h[s*F_+f]=f2bf(hh*tanhf(sp));
}
__global__ void k_g2(const u16* h, const u16* w2, const u16* x, const float* y, u16* out){
  int s=blockIdx.y; int d=blockIdx.x*256+threadIdx.x; __shared__ float row[F_];
  for(int i=threadIdx.x;i<F_;i+=256) row[i]=bf2f(h[s*F_+i]);
  __syncthreads();
  float acc=0; for(int f=0;f<F_;f++) acc+=row[f]*bf2f(w2[f*D_+d]);
  out[s*D_+d]=f2bf(rbf(bf2f(x[s*D_+d])+rbf(y[s*D_+d]))+rbf(acc));
}
extern "C" void kernel_launch(void* const* d_in, const int* in_sizes, int n_in, void* d_out, int out_size, void* d_ws, size_t ws_size, hipStream_t stream){
  const u16* x=(const u16*)d_in[0]; const u16* wr=(const u16*)d_in[1]; const u16* wi=(const u16*)d_in[2];
  const u16* w1=(const u16*)d_in[4]; const u16* b1=(const u16*)d_in[5]; const u16* w2=(const u16*)d_in[6];
  char* ws=(char*)d_ws;
  u16* xn=(u16*)ws; float2* X=(float2*)(ws+(8<<20)); float2* Y=(float2*)(ws+(32<<20)); float* y=(float*)(ws+(64<<20)); u16* h=(u16*)(ws+(96<<20));
  k_ln<<<S_,256,0,stream>>>(x,xn);
  k_rowdft<<<S_,256,0,stream>>>(xn,X);
  k_col<<<DF_,1024,0,stream>>>(X,wr,wi,Y);
  k_irow<<<S_,256,0,stream>>>(Y,y);
  k_g1<<<dim3(F_/256,S_),256,0,stream>>>(xn,w1,b1,h);
  k_g2<<<dim3(D_/256,S_),256,0,stream>>>(h,w2,x,y,(u16*)d_out);
}
```

```cpp
#include <hip/hip_runtime.h>
#include <hip/hip_bf16.h>
#include <stdint.h>
typedef unsigned short u16;
typedef unsigned long long u64;
#define LAS __attribute__((address_space(3)))
typedef short s16x4 __attribute__((ext_vector_type(4)));
typedef __bf16 bf16x8 __attribute__((ext_vector_type(8)));
typedef __bf16 bf16x2 __attribute__((ext_vector_type(2)));
typedef float f32x2 __attribute__((ext_vector_type(2)));
typedef float f32x4 __attribute__((ext_vector_type(4)));
typedef float f32x16 __attribute__((ext_vector_type(16)));
typedef unsigned u32x4 __attribute__((ext_vector_type(4)));
typedef unsigned u32x2 __attribute__((ext_vector_type(2)));
typedef int i32x4 __attribute__((ext_vector_type(4)));
#define DI __device__ __forceinline__

#define S_ 2048
#define D_ 1024
#define F_ 3584
#define DF_ 513

constexpr size_t WS_H    = 0;
constexpr size_t WS_ZW   = 16u<<20;
constexpr size_t WS_Z2   = 24u<<20;
constexpr size_t WS_PART = 28u<<20;
constexpr size_t WS_CS   = 36u<<20;
constexpr size_t WS_STAT = 37u<<20;

DI void dma16(i32x4 rsrc, unsigned voff, unsigned soff, unsigned ldsdst) {
  asm volatile("s_mov_b32 m0, %3\n\ts_nop 0\n\tbuffer_load_dwordx4 %0, %1, %2 offen lds" :: "v"(voff), "s"(rsrc), "s"(soff), "s"(ldsdst) : "memory");
}
DI i32x4 mkrsrc(const void* p, unsigned bytes) {
  u64 a = (u64)p; i32x4 r; r[0] = __builtin_amdgcn_readfirstlane((unsigned)a); r[1] = __builtin_amdgcn_readfirstlane((unsigned)(a>>32)) & 0xffff; r[2] = bytes; r[3] = 0x00020000; return r;
}
DI unsigned cvtpk(float lo, float hi) { f32x2 v = {lo, hi}; return __builtin_bit_cast(unsigned, __builtin_convertvector(v, bf16x2)); }
DI float bflo(unsigned u) { return __uint_as_float(u << 16); }
DI float bfhi(unsigned u) { return __uint_as_float(u & 0xffff0000u); }
#define WAITV(n) asm volatile("s_waitcnt vmcnt(%0)" :: "n"(n) : "memory")
#define BAR() do { asm volatile("" ::: "memory"); __builtin_amdgcn_s_barrier(); asm volatile("" ::: "memory"); } while(0)
#define SB() __builtin_amdgcn_sched_barrier(0)
#define WAITL0() __builtin_amdgcn_s_waitcnt(0xC07F)
#define LBAR() do { WAITL0(); BAR(); } while(0)
DI s16x4 trrd(unsigned a) { return __builtin_amdgcn_ds_read_tr16_b64_v4i16((LAS s16x4*)(size_t)a); }
DI bf16x8 cat8(s16x4 lo, s16x4 hi) { return __builtin_bit_cast(bf16x8, __builtin_shufflevector(lo, hi, 0,1,2,3,4,5,6,7)); }
DI f32x16 mfma32(bf16x8 a, bf16x8 b, f32x16 c) { return __builtin_amdgcn_mfma_f32_32x32x16_bf16(a, b, c, 0,0,0); }
DI float cosr(float r) { return __builtin_amdgcn_cosf(r); }
DI float sinr(float r) { return __builtin_amdgcn_sinf(r); }
DI int crow(int r, int h) { return (r & 3) + 8*(r >> 2) + 4*h; }
DI bf16x8 pack8(const float* v) { u32x4 p = { cvtpk(v[0],v[1]), cvtpk(v[2],v[3]), cvtpk(v[4],v[5]), cvtpk(v[6],v[7]) }; return __builtin_bit_cast(bf16x8, p); }
DI f32x16 zero16() { f32x16 z;
  #pragma unroll
  for (int i=0;i<16;i++) z[i]=0.f; return z; }
DI bf16x8 neg8(bf16x8 v) { u32x4 p = __builtin_bit_cast(u32x4, v); p ^= 0x80008000u; return __builtin_bit_cast(bf16x8, p); }

template<int CTRL> DI float dppadd(float v) { return v + __builtin_bit_cast(float, __builtin_amdgcn_update_dpp(0, __builtin_bit_cast(int, v), CTRL, 0xf, 0xf, true)); }
DI float wave_sum(float v) {
  v = dppadd<0xB1>(v);
  v = dppadd<0x4E>(v);
  v = dppadd<0x141>(v);
  v = dppadd<0x140>(v);
  { auto r = __builtin_amdgcn_permlane16_swap(__float_as_uint(v), __float_as_uint(v), false, false); v = __uint_as_float(r[0]) + __uint_as_float(r[1]); }
  { auto r = __builtin_amdgcn_permlane32_swap(__float_as_uint(v), __float_as_uint(v), false, false); v = __uint_as_float(r[0]) + __uint_as_float(r[1]); }
  return v;
}
template<int N> DI void dft_frag(int idx, int kbase, float phase, bf16x8& c, bf16x8& s) {
  float cv[8], sv[8];
  #pragma unroll
  for (int j=0;j<8;j++) { int k = kbase + 8*(j>>2) + (j&3); float r = (float)((idx*k) & (N-1)) * (1.0f/N) + phase; cv[j] = cosr(r); sv[j] = sinr(r); }
  c = pack8(cv); s = pack8(sv);
}

constexpr int A_XR = 0, A_WR = 16384, A_WI = A_WR + 9216, A_CS = A_WI + 9216, A_TOT = A_CS + 28672;
__global__ __launch_bounds__(512, 2) void k_a(const u16* __restrict__ x, const u16* __restrict__ wr, const u16* __restrict__ wi, const u16* __restrict__ w1, char* __restrict__ ws) {
  extern __shared__ __attribute__((aligned(16))) char smem[];
  const int wid = __builtin_amdgcn_readfirstlane(threadIdx.x >> 6), lane = threadIdx.x & 63;
  const int b = blockIdx.x, xcd = b & 7, idx = b >> 3;
  const int tm = (xcd >> 1)*4 + (idx & 3), tn = (xcd & 1)*8 + (idx >> 2);
  const int gi = lane >> 4, q = (lane & 15) >> 2, p = lane & 3, h = lane >> 5, l31 = lane & 31;
  char* zw = ws + WS_ZW + (size_t)b*32768;
  if (wid >= 4) {
    const int w = wid - 4;
    i32x4 rx = mkrsrc(x, S_*D_*2), rr = mkrsrc(wr, S_*DF_*2), ri = mkrsrc(wi, S_*DF_*2), rb = mkrsrc(w1, D_*F_*2);
    #pragma unroll
    for (int i=0;i<4;i++) dma16(rx, lane*16, b*16384 + (w*4+i)*1024, A_XR + (w*4+i)*1024);
    #pragma unroll
    for (int i=0;i<5;i++) { int pc = w + 4*i; if (pc < 18) { if (pc < 9) dma16(rr, lane*16, b*8208 + pc*1024, A_WR + pc*1024); else dma16(ri, lane*16, b*8208 + (pc-9)*1024, A_WI + (pc-9)*1024); } }
    #pragma unroll
    for (int i=0;i<7;i++) { int pc = w + 4*i; int ci = pc*64 + lane; int r = (ci*9363)>>18, c = ci - r*28; c ^= ((r>>3)&1)<<1;
      dma16(rb, (unsigned)(r*F_*2 + tn*448 + c*16), tm*64*F_*2, A_CS + pc*1024); }
    WAITV(0);
    BAR();
    const int t = w*64 + lane;
    #pragma unroll
    for (int i=0;i<4;i++) {
      int id = t + 256*i, cw = id >> 2, col = (id >> 1) & 1, hf = id & 1;
      int k = 2*cw + col; if (k == 0) k = 512;
      u32x4 v;
      #pragma unroll
      for (int e=0;e<4;e++) { unsigned off = ((hf*4+e)*DF_ + k)*2; unsigned a = *(LAS u16*)(size_t)(A_WR + off), c = *(LAS u16*)(size_t)(A_WI + off); v[e] = a | (c << 16); }
      *(u32x4*)(zw + cw*128 + 64 + (id&3)*16) = v;
    }
    const u32x4 onesu = {0x3f803f80u,0x3f803f80u,0x3f803f80u,0x3f803f80u};
    const bf16x8 ones = __builtin_bit_cast(bf16x8, onesu);
    float* cs = (float*)(ws + WS_CS) + tm*F_ + tn*224;
    #pragma unroll
    for (int i=0;i<4;i++) { int u = w + 4*i; if (u < 14) {
      f32x4 a = {0,0,0,0};
      #pragma unroll
      for (int sub=0; sub<2; sub++) { unsigned base = A_CS + (32*sub + 8*gi + q)*448 + ((u ^ (gi&1))*32) + 8*p;
        a = __builtin_amdgcn_mfma_f32_16x16x32_bf16(cat8(trrd(base), trrd(base + 1792)), ones, a, 0,0,0); }
      if ((lane & 15) == 0) *(f32x4*)(cs + u*16 + 4*gi) = a; } }
    BAR();
    BAR();
  } else {
    bf16x8 Cp[2], Sp[2], M1[2], M2[2];
    #pragma unroll
    for (int s=0;s<2;s++) { dft_frag<32>(l31, 16*s + 4*h, 0.f, Cp[s], Sp[s]); Sp[s] = neg8(Sp[s]); dft_frag<32>(l31 & 15, 16*s + 4*h, (l31 >= 16) ? 0.25f : 0.f, M1[s], M2[s]); }
    float twc[16], tws[16];
    #pragma unroll
    for (int r=0;r<16;r++) { float a = (float)(crow(r,h)*l31) * (1.0f/1024); twc[r] = cosr(a); tws[r] = sinr(a); }
    BAR();
    f32x16 X[2];
    bf16x8 xf[2][2];
    #pragma unroll
    for (int rr=0; rr<2; rr++)
      #pragma unroll
      for (int s=0;s<2;s++) { unsigned base = A_XR + (wid*2+rr)*2048 + (16*s + 4*(gi>>1) + q)*64 + (gi&1)*32 + 8*p; xf[rr][s] = cat8(trrd(base), trrd(base + 512)); }
    LBAR();
    #pragma unroll
    for (int rr=0; rr<2; rr++) {
      f32x16 Pr = zero16(), Pi = zero16();
      #pragma unroll
      for (int s=0;s<2;s++) { Pr = mfma32(xf[rr][s], Cp[s], Pr); Pi = mfma32(xf[rr][s], Sp[s], Pi); }
      float s1 = 0.f, s2 = 0.f;
      const bf16x2 one2 = __builtin_bit_cast(bf16x2, 0x3f803f80u);
      #pragma unroll
      for (int s=0;s<2;s++) { u32x4 u = __builtin_bit_cast(u32x4, xf[rr][s]);
        #pragma unroll
        for (int e=0;e<4;e++) { unsigned ue = u[e]; bf16x2 v = __builtin_bit_cast(bf16x2, ue); s1 = __builtin_amdgcn_fdot2_f32_bf16(v, one2, s1, false); s2 = __builtin_amdgcn_fdot2_f32_bf16(v, v, s2, false); } }
      float ny = wave_sum((lane & 1) ? -s1 : s1);
      s1 = wave_sum(s1); s2 = wave_sum(s2);
      float mean = s1 * (1.0f/D_), var = fmaxf(s2 * (1.0f/D_) - mean*mean, 0.f);
      float rstd = __builtin_amdgcn_rsqf(var + 1e-5f);
      if (lane == 0) ((f32x2*)(ws + WS_STAT))[b*8 + wid*2 + rr] = (f32x2){rstd * 1.4426950408889634f, mean * rstd * 1.4426950408889634f};
      float sub = (l31 == 0) ? 32.f*mean : 0.f;
      float tr_[16], ti_[16];
      #pragma unroll
      for (int r=0;r<16;r++) { float a = (Pr[r] - sub) * rstd, bb = Pi[r] * rstd; tr_[r] = a*twc[r] + bb*tws[r]; ti_[r] = bb*twc[r] - a*tws[r]; }
      f32x16 Y = zero16();
      #pragma unroll
      for (int s=0;s<2;s++) { Y = mfma32(M1[s], pack8(tr_ + 8*s), Y); Y = mfma32(M2[s], pack8(ti_ + 8*s), Y); }
      if (lane == 0) { Y[0] = ny * rstd; Y[8] = 0.f; }
      X[rr] = Y;
    }
    #pragma unroll
    for (int r=0;r<16;r++) { int k2 = crow(r & 7, h), ri = r >> 3; int cw = (l31 >> 1) + 16*k2; int ch = cw*4 + (l31 & 1)*2 + ri;
      *(LAS unsigned*)(size_t)(A_XR + ch*16 + ((wid ^ ((l31 >> 2) & 3))*4)) = cvtpk(X[0][r], X[1][r]); }
    LBAR();
    #pragma unroll
    for (int i=0;i<4;i++) { int ch = wid*64 + lane + 256*i; u32x4 v = *(LAS u32x4*)(size_t)(A_XR + ch*16);
      int sw = (ch >> 3) & 3;
      u32x4 o;
      if (sw & 1) v = (u32x4){v[1], v[0], v[3], v[2]};
      if (sw & 2) v = (u32x4){v[2], v[3], v[0], v[1]};
      o = v;
      *(u32x4*)(zw + (ch >> 2)*128 + (ch & 3)*16) = o; }
  }
}

constexpr int A_SLOT = 16384, B_SLOT = 14336;
constexpr int G1_A = 0, G1_B = 3*A_SLOT, G1_TOT = G1_B + 6*B_SLOT;
DI unsigned mish2(f32x2 u) {
  f32x2 t; t.x = __builtin_amdgcn_exp2f(u.x); t.y = __builtin_amdgcn_exp2f(u.y);
  f32x2 a = t + 1.0f; f32x2 d = a*a + 1.0f;
  f32x2 r; r.x = __builtin_amdgcn_rcpf(d.x); r.y = __builtin_amdgcn_rcpf(d.y);
  f32x2 f = r * (-1.3862943611198906f) + 0.6931471805599453f;
  f32x2 o = u * f;
  return cvtpk(o.x, o.y);
}
__global__ __launch_bounds__(512, 2) void k_b(const u16* __restrict__ x, const u16* __restrict__ w1, const u16* __restrict__ b1, char* __restrict__ ws) {
  extern __shared__ __attribute__((aligned(16))) char smem[];
  const int wid = __builtin_amdgcn_readfirstlane(threadIdx.x >> 6), lane = threadIdx.x & 63;
  const int b = blockIdx.x, xcd = b & 7, idx = b >> 3;
  const int tm = (xcd >> 1)*4 + (idx & 3), tn = (xcd & 1)*8 + (idx >> 2);
  constexpr int NK = D_/64;
  if (wid >= 4) {
    const int w = wid - 4;
    i32x4 ra = mkrsrc(x, S_*D_*2), rb = mkrsrc(w1, D_*F_*2);
    unsigned va[4], vb[7];
    #pragma unroll
    for (int i=0;i<4;i++) { int p = w*4+i; int r = p*8 + (lane>>3); va[i] = (unsigned)((tm*128 + r)*D_*2 + (((lane&7) ^ (r&7))*16)); }
    #pragma unroll
    for (int i=0;i<7;i++) { int p = w + 4*i; int ci = p*64 + lane; int r = (ci*9363)>>18, c = ci - r*28; c ^= ((r>>3)&1)<<1; vb[i] = (unsigned)(r*F_*2 + tn*448 + c*16); }
    const unsigned da = G1_A + w*4096, db = G1_B + w*1024;
    auto issue = [&](int k, int slot) {
      unsigned soa = k*128, sob = k*64*F_*2, a = da + slot*A_SLOT, bb = db + slot*2*B_SLOT;
      #pragma unroll
      for (int i=0;i<4;i++) dma16(ra, va[i], soa, a + i*1024);
      #pragma unroll
      for (int i=0;i<7;i++) dma16(rb, vb[i], sob, bb + i*4096);
    };
    issue(0, 0); issue(1, 1);
    WAITV(11); BAR();
    issue(2, 2);
    int slot = 0;
    #pragma unroll 1
    for (int k = 0; k < NK-3; k++) { WAITV(11); BAR(); issue(k+3, slot); slot = slot == 2 ? 0 : slot+1; }
    WAITV(11); BAR();
    const int t = w*64 + lane;
    float csn = 0.f, bn = 0.f;
    if (t < 224) { const float* cs = (const float*)(ws + WS_CS) + tn*224 + t;
      #pragma unroll
      for (int i=0;i<16;i++) csn += cs[i*F_];
      bn = bflo(b1[tn*224 + t]) * 1.4426950408889634f; }
    WAITV(0); BAR();
    if (t < 224) { *(LAS float*)(size_t)(G1_A + A_SLOT + t*4) = bn; *(LAS float*)(size_t)(G1_A + A_SLOT + 1024 + t*4) = csn; }
    LBAR();
  } else {
    const int wm = wid & 1, wn = wid >> 1;
    const int g = lane >> 4, c = lane & 15, q = (lane & 15) >> 2, p = lane & 3;
    f32x4 acc[7][4];
    #pragma unroll
    for (int i=0;i<7;i++)
      #pragma unroll
      for (int j=0;j<4;j++) { acc[i][j] = (f32x4){0,0,0,0}; asm volatile("" : "+v"(acc[i][j])); }
    f32x2 st[4];
    #pragma unroll
    for (int j=0;j<4;j++) st[j] = ((const f32x2*)(ws + WS_STAT))[tm*128 + wm*64 + j*16 + c];
    unsigned xak[2];
    xak[0] = G1_A + ((wm*64 + c)>>3)*1024 + (c&7)*128 + ((g ^ (c&7))*16);
    xak[1] = G1_A + ((wm*64 + c)>>3)*1024 + (c&7)*128 + (((4+g) ^ (c&7))*16);
    const unsigned wbase = G1_B + (8*g + q)*448 + 8*p + wn*7*32;
    const unsigned sgn = (g&1)*32;
    unsigned wb[2][2];
    wb[0][0] = wn ? wbase - sgn : wbase + sgn;
    wb[0][1] = wn ? wbase + sgn : wbase - sgn;
    wb[1][0] = wb[0][0] + 3*B_SLOT; wb[1][1] = wb[0][1] + 3*B_SLOT;
    asm volatile("" : "+v"(wb[1][0]), "+v"(wb[1][1]), "+v"(xak[0]), "+v"(xak[1]), "+v"(wb[0][0]), "+v"(wb[0][1]));
    asm volatile("" : "+v"(st[0]), "+v"(st[1]), "+v"(st[2]), "+v"(st[3]));
    bf16x8 xf[2][4], wf[2][7];
    s16x4 lo_[7];
    auto rd = [&](int set, int r, int n) {
      if (n >= 2 && n < 6) { xf[set][n-2] = *(LAS bf16x8*)(size_t)(xak[r&1] + (r>>1)*A_SLOT + (n-2)*2048); return; }
      int i = n < 2 ? 0 : (n-6)/2+1, t = n < 2 ? n : (n-6)&1;
      s16x4 v = trrd(wb[r/3][i&1] + (r%3)*B_SLOT + i*32 + t*1792);
      if (t==0) lo_[i] = v; else wf[set][i] = cat8(lo_[i], v);
    };
    auto mm = [&](int set, int n) { int i = n>>2, j = (i&1) ? 3-(n&3) : (n&3);
      asm volatile("v_mfma_f32_16x16x32_bf16 %0, %1, %2, %0" : "+v"(acc[i][j]) : "v"(wf[set][i]), "v"(xf[set][j])); };
    BAR();
    #pragma unroll
    for (int n=0;n<18;n++) rd(0, 0, n);
    WAITL0();
    auto step = [&](int set, int r1, bool bar) {
      SB();
      #pragma unroll
      for (int n=0;n<28;n++) { mm(set, n); SB(); if (n < 18) { rd(set^1, r1, n); SB(); }
        if (n == 25) { WAITL0(); SB(); }
        if (n == 26 && bar) { BAR(); SB(); } }
    };
    #pragma unroll 1
    for (int s = 0; s < 30; s += 6) { step(0, 1, true); step(1, 2, false); step(0, 3, true); step(1, 4, false); step(0, 5, true); step(1, 0, false); }
    step(0, 1, true);
    #pragma unroll
    for (int n=0;n<28;n++) mm(1, n);
    asm volatile("s_nop 7\n\ts_nop 7" ::: "memory");
    SB();
    f32x4 bl[7], cl[7];
    #pragma unroll
    for (int i=0;i<7;i++) { unsigned a = G1_A + A_SLOT + (wn*112 + i*16 + 4*g)*4; bl[i] = *(LAS f32x4*)(size_t)a; cl[i] = *(LAS f32x4*)(size_t)(a + 1024); }
    #pragma unroll
    for (int j=0;j<4;j++) {
      int mb = tm*8 + wm*4 + j;
      char* hb = ws + WS_H + ((size_t)mb*112 + tn*7)*1024 + lane*16;
      unsigned pk[7][2];
      #pragma unroll
      for (int i=0;i<7;i++) { f32x4 v = acc[i][j] * st[j].x + (bl[i] - cl[i] * st[j].y); pk[i][0] = mish2((f32x2){v[0], v[1]}); pk[i][1] = mish2((f32x2){v[2], v[3]}); }
      if (wn == 0) {
        #pragma unroll
        for (int i=0;i<3;i++) *(u32x4*)(hb + i*1024) = (u32x4){pk[2*i][0], pk[2*i][1], pk[2*i+1][0], pk[2*i+1][1]};
        *(u32x2*)(hb + 3*1024) = (u32x2){pk[6][0], pk[6][1]};
      } else {
        *(u32x2*)(hb + 3*1024 + 8) = (u32x2){pk[0][0], pk[0][1]};
        #pragma unroll
        for (int i=0;i<3;i++) *(u32x4*)(hb + (4+i)*1024) = (u32x4){pk[2*i+1][0], pk[2*i+1][1], pk[2*i+2][0], pk[2*i+2][1]};
      }
    }
  }
}

constexpr int C_IN = 0, C_EX = 32768, C_OUT = C_EX + 16384, C_TOT = C_OUT + 16384;
__global__ __launch_bounds__(256, 2) void k_c(char* __restrict__ ws) {
  extern __shared__ __attribute__((aligned(16))) char smem[];
  const int w = __builtin_amdgcn_readfirstlane(threadIdx.x >> 6), lane = threadIdx.x & 63;
  const int cw = blockIdx.x, col = w >> 1, hf = w & 1;
  const int gi = lane >> 4, q = (lane & 15) >> 2, p = lane & 3, h = lane >> 5, l31 = lane & 31;
  { i32x4 rz = mkrsrc(ws + WS_ZW, 8u<<20);
    #pragma unroll
    for (int i=0;i<8;i++) { int pc = w*8 + i; int rw = pc*8 + (lane >> 3); int ch = (lane & 7) ^ ((rw >> 1) & 7);
      dma16(rz, (unsigned)(rw*32768 + ch*16), cw*128, C_IN + pc*1024); } }
  const int u = l31 + 32*hf;
  bf16x8 C64[4], S64[4], C32[2], S32[2];
  #pragma unroll
  for (int s=0;s<4;s++) dft_frag<64>(u, 16*s + 4*h, 0.f, C64[s], S64[s]);
  #pragma unroll
  for (int s=0;s<2;s++) dft_frag<32>(l31, 16*s + 4*h, 0.f, C32[s], S32[s]);
  float fc[16], fs[16], ic[16], is[16];
  #pragma unroll
  for (int r=0;r<16;r++) { float a = (float)(crow(r,h)*u) * (1.0f/2048); fc[r] = cosr(a); fs[r] = sinr(a);
    float a2 = (float)((crow(r,h) + 32*hf)*l31) * (1.0f/2048); ic[r] = cosr(a2); is[r] = sinr(a2); }
  WAITV(0); BAR();
  f32x16 Pr = zero16(), Pi = zero16();
  #pragma unroll
  for (int s=0;s<4;s++) {
    bf16x8 zr, zi; s16x4 v[2][2];
    #pragma unroll
    for (int t=0;t<2;t++) { int a = 16*s + 8*t + 4*(gi>>1) + q; int P = 4*(gi&1) + p; int rw = a*4 + (P >> 1);
      #pragma unroll
      for (int ri=0;ri<2;ri++) v[ri][t] = trrd(C_IN + rw*128 + (((col*2 + ri) ^ ((rw >> 1) & 7))*16) + (P & 1)*8); }
    zr = cat8(v[0][0], v[0][1]); zi = cat8(v[1][0], v[1][1]);
    Pr = mfma32(zr, C64[s], Pr); Pr = mfma32(zi, S64[s], Pr);
    Pi = mfma32(zi, C64[s], Pi); Pi = mfma32(neg8(zr), S64[s], Pi);
  }
  unsigned wv[16];
  #pragma unroll
  for (int r=0;r<16;r++) { int j = u + 64*crow(r,h); int rw = j >> 3; int ch = 4 + col*2 + ((j >> 2) & 1);
    wv[r] = *(LAS unsigned*)(size_t)(C_IN + rw*128 + ((ch ^ ((rw >> 1) & 7))*16) + (j & 3)*4); }
  float a_[16], b_[16];
  #pragma unroll
  for (int r=0;r<16;r++) { a_[r] = Pr[r]*fc[r] + Pi[r]*fs[r]; b_[r] = Pi[r]*fc[r] - Pr[r]*fs[r]; }
  f32x16 Ur = zero16(), Ui = zero16();
  #pragma unroll
  for (int s=0;s<2;s++) { bf16x8 tr_ = pack8(a_ + 8*s), ti_ = pack8(b_ + 8*s);
    Ur = mfma32(C32[s], tr_, Ur); Ur = mfma32(S32[s], ti_, Ur);
    Ui = mfma32(C32[s], ti_, Ui); Ui = mfma32(S32[s], neg8(tr_), Ui); }
  const float sc = ((cw == 0 && col == 0) ? 1.0f : 2.0f) / (float)(S_*D_);
  #pragma unroll
  for (int r=0;r<16;r++) { float wre = bflo(wv[r])*sc, wim = bfhi(wv[r])*sc; a_[r] = Ur[r]*wre - Ui[r]*wim; b_[r] = Ur[r]*wim + Ui[r]*wre; }
  f32x16 Qr = zero16(), Qi = zero16();
  #pragma unroll
  for (int s=0;s<2;s++) { bf16x8 yr = pack8(a_ + 8*s), yi = pack8(b_ + 8*s);
    Qr = mfma32(yr, C32[s], Qr); Qr = mfma32(neg8(yi), S32[s], Qr);
    Qi = mfma32(yi, C32[s], Qi); Qi = mfma32(yr, S32[s], Qi); }
  #pragma unroll
  for (int r=0;r<16;r++) { a_[r] = Qr[r]*ic[r] - Qi[r]*is[r]; b_[r] = Qi[r]*ic[r] + Qr[r]*is[r]; }
  bf16x8 Rr[4], Ri[4];
  { bf16x8 r0 = pack8(a_), r1 = pack8(a_+8), i0 = pack8(b_), i1 = pack8(b_+8);
    unsigned ex = C_EX + w*4096 + lane*16;
    *(LAS bf16x8*)(size_t)(ex) = r0; *(LAS bf16x8*)(size_t)(ex+1024) = r1; *(LAS bf16x8*)(size_t)(ex+2048) = i0; *(LAS bf16x8*)(size_t)(ex+3072) = i1;
    LBAR();
    #pragma unroll
    for (int s=0;s<4;s++) { unsigned e2 = C_EX + (col*2 + (s>>1))*4096 + lane*16 + (s&1)*1024; Rr[s] = *(LAS bf16x8*)(size_t)e2; Ri[s] = *(LAS bf16x8*)(size_t)(e2 + 2048); } }
  f32x16 Zr = zero16(), Zi = zero16();
  #pragma unroll
  for (int s=0;s<4;s++) { Zr = mfma32(Rr[s], C64[s], Zr); Zr = mfma32(neg8(Ri[s]), S64[s], Zr);
    Zi = mfma32(Ri[s], C64[s], Zi); Zi = mfma32(Rr[s], S64[s], Zi); }
  #pragma unroll
  for (int r=0;r<16;r++) { int rw = 4*u + (r >> 2), rp = 2*h + ((r & 3) >> 1), row = r & 1;
    unsigned pk = cvtpk(Zr[r], Zi[r]);
    unsigned ad = C_OUT + (((rw*4 + rp) ^ (l31 & 15))*16) + row*4 + col*2;
    *(LAS u16*)(size_t)(ad) = (u16)pk; *(LAS u16*)(size_t)(ad + 8) = (u16)(pk >> 16); }
  LBAR();
  char* z2 = ws + WS_Z2 + (size_t)cw*16384;
  #pragma unroll
  for (int i=0;i<4;i++) { int ch = w*64 + lane + 256*i; u32x4 v = *(LAS u32x4*)(size_t)(C_OUT + ((ch ^ ((ch >> 4) & 15))*16)); *(u32x4*)(z2 + ch*16) = v; }
}

constexpr int G2_NS = 4, G2_A = 0, G2_B = G2_NS*16384, G2_TOT = 2*G2_NS*16384;
__global__ __launch_bounds__(512, 2) void k_d(const u16* __restrict__ w2, char* __restrict__ ws) {
  extern __shared__ __attribute__((aligned(16))) char smem[];
  const int wid = __builtin_amdgcn_readfirstlane(threadIdx.x >> 6), lane = threadIdx.x & 63;
  const int b = blockIdx.x, xcd = b & 7, idx = b >> 3;
  const int ks = xcd & 1, tm = (xcd >> 1)*4 + (idx & 3), tn = idx >> 2;
  constexpr int NK = 28;
  if (wid >= 4) {
    const int w = wid - 4;
    i32x4 ra = mkrsrc(ws + WS_H, S_*F_*2), rb = mkrsrc(w2, F_*D_*2);
    unsigned va[4], vb[4];
    #pragma unroll
    for (int i=0;i<4;i++) { int p = w*4+i; va[i] = (unsigned)(((tm*8 + (p>>1))*112 + ks*56 + (p&1))*1024 + lane*16); }
    #pragma unroll
    for (int i=0;i<4;i++) { int p = w*4+i; int r = p*4 + (lane>>4); int c = (lane&15) ^ ((r&7)<<1); vb[i] = (unsigned)((ks*1792 + r)*D_*2 + tn*256 + c*16); }
    auto issue = [&](int k, int slot) {
      unsigned a = G2_A + slot*16384 + w*4096, bb = G2_B + slot*16384 + w*4096;
      #pragma unroll
      for (int i=0;i<4;i++) dma16(rb, vb[i], k*64*D_*2, bb + i*1024);
      #pragma unroll
      for (int i=0;i<4;i++) dma16(ra, va[i], k*2048, a + i*1024);
    };
    issue(0, 0); issue(1, 1);
    WAITV(8); BAR();
    issue(2, 2); issue(3, 3);
    #pragma unroll 1
    for (int k = 0; k < NK-4; k++) { WAITV(16); BAR(); issue(k+4, k & 3); }
    WAITV(16); BAR();
    WAITV(8); BAR();
    WAITV(0); BAR();
  } else {
    const int wm = wid & 1, wn = wid >> 1;
    const int g = lane >> 4, c = lane & 15, q = (lane & 15) >> 2, p = lane & 3;
    f32x4 acc[4][4];
    #pragma unroll
    for (int i=0;i<4;i++)
      #pragma unroll
      for (int j=0;j<4;j++) { acc[i][j] = (f32x4){0,0,0,0}; asm volatile("" : "+v"(acc[i][j])); }
    unsigned xa = G2_A + wm*8192 + lane*16;
    unsigned wb[4];
    { int rho = 4*(g&1) + q;
      #pragma unroll
      for (int i=0;i<4;i++) wb[i] = G2_B + (4*g + q)*256 + (((wn*4 + i) ^ rho)*32) + 8*p; }
    asm volatile("" : "+v"(xa), "+v"(wb[0]), "+v"(wb[1]), "+v"(wb[2]), "+v"(wb[3]));
    bf16x8 xf[2][2][4], wf[2][2][4];
    s16x4 lo_;
    auto rd = [&](int set, int r, int n) {
      int sub = n / 12, m = n % 12;
      if (m >= 2 && m < 6) { xf[set][sub][m-2] = *(LAS bf16x8*)(size_t)(xa + r*16384 + (m-2)*2048 + sub*1024); return; }
      int i = m < 2 ? 0 : (m-6)/2+1, t = m < 2 ? m : (m-6)&1;
      s16x4 v = trrd(wb[i] + r*16384 + sub*8192 + t*4096);
      if (t==0) lo_ = v; else wf[set][sub][i] = cat8(lo_, v);
    };
    auto mm = [&](int set, int n) { int sub = n & 1, m = n >> 1; int i = m>>2, j = (i&1) ? 3-(m&3) : (m&3);
      asm volatile("v_mfma_f32_16x16x32_bf16 %0, %1, %2, %0" : "+v"(acc[i][j]) : "v"(wf[set][sub][i]), "v"(xf[set][sub][j])); };
    BAR();
    #pragma unroll
    for (int n=0;n<24;n++) rd(0, 0, n);
    WAITL0(); BAR();
    auto step = [&](int set, int r1, bool bar) {
      SB();
      #pragma unroll
      for (int n=0;n<32;n++) { mm(set, n); SB(); if (n < 24) { rd(set^1, r1, n); SB(); }
        if (n == 29) { WAITL0(); SB(); }
        if (n == 30 && bar) { BAR(); SB(); } }
    };
    #pragma unroll 1
    for (int s = 0; s < 24; s += 4) { step(0, 1, true); step(1, 2, true); step(0, 3, true); step(1, 0, true); }
    step(0, 1, true); step(1, 2, true); step(0, 3, false);
    #pragma unroll
    for (int n=0;n<32;n++) mm(1, n);
    asm volatile("s_nop 7\n\ts_nop 7" ::: "memory");
    SB();
    #pragma unroll
    for (int j=0;j<4;j++) {
      int m = tm*128 + wm*64 + j*16 + c;
      #pragma unroll
      for (int ip=0; ip<2; ip++) { int n1 = tn*4 + wn*2 + ip;
        u32x4 v = { cvtpk(acc[2*ip][j][0], acc[2*ip][j][1]), cvtpk(acc[2*ip][j][2], acc[2*ip][j][3]), cvtpk(acc[2*ip+1][j][0], acc[2*ip+1][j][1]), cvtpk(acc[2*ip+1][j][2], acc[2*ip+1][j][3]) };
        *(u32x4*)(ws + WS_PART + (size_t)ks*(4u<<20) + (size_t)(m >> 3)*16384 + n1*512 + (m & 7)*64 + g*16) = v; }
    }
  }
}

constexpr int E_Z = 0, E_X = 16384, E_P = 32768, E_TOT = E_P + 32768;
__global__ __launch_bounds__(256, 2) void k_e(const u16* __restrict__ x, char* __restrict__ ws, u16* __restrict__ out) {
  extern __shared__ __attribute__((aligned(16))) char smem[];
  const int w = __builtin_amdgcn_readfirstlane(threadIdx.x >> 6), lane = threadIdx.x & 63;
  const int rw = blockIdx.x;
  const int gi = lane >> 4, q = (lane & 15) >> 2, p = lane & 3, h = lane >> 5, l31 = lane & 31;
  { i32x4 rz = mkrsrc(ws + WS_Z2, 4u<<20), rx = mkrsrc(x, S_*D_*2), rp = mkrsrc(ws + WS_PART, 8u<<20);
    #pragma unroll
    for (int i=0;i<4;i++) { int pc = w*4 + i; int cw = pc*16 + (lane >> 2); int ch = (lane & 3) ^ (pc & 3);
      dma16(rz, (unsigned)(cw*16384 + ch*16), rw*64, E_Z + pc*1024); }
    #pragma unroll
    for (int i=0;i<4;i++) dma16(rx, lane*16, rw*16384 + (w*4+i)*1024, E_X + (w*4+i)*1024);
    #pragma unroll
    for (int i=0;i<8;i++) { int pc = w*8 + i; dma16(rp, lane*16, (pc >> 4)*(4u<<20) + rw*16384 + (pc & 15)*1024, E_P + pc*1024); } }
  bf16x8 C16, S16, C2[2], S2[2];
  dft_frag<32>(l31, 4*h, 0.f, C16, S16);
  {
    #pragma unroll
    for (int s=0;s<2;s++) { float cv[8], sv[8];
      #pragma unroll
      for (int j=0;j<8;j++) { int k1 = 16*s + 2*(2*(j>>1) + h) + (j&1); float r = (float)((k1*l31) & 31) * (1.0f/32); cv[j] = cosr(r); sv[j] = -sinr(r); }
      C2[s] = pack8(cv); S2[s] = pack8(sv); } }
  float tc[16], ts_[16];
  #pragma unroll
  for (int s=0;s<2;s++)
    #pragma unroll
    for (int j=0;j<8;j++) { int k1 = 16*s + 2*(2*(j>>1) + h) + (j&1); float a = (float)(k1*l31) * (1.0f/1024); tc[8*s+j] = cosr(a); ts_[8*s+j] = sinr(a); }
  WAITV(0); BAR();
  f32x16 Qr[2], Qi[2];
  #pragma unroll
  for (int s=0;s<2;s++) {
    s16x4 v[2][2];
    #pragma unroll
    for (int t=0;t<2;t++) { int k2 = 8*t + 4*(gi>>1) + q; int cw = 16*k2 + 8*s + 4*(gi&1) + p;
      #pragma unroll
      for (int ri=0;ri<2;ri++) v[ri][t] = trrd(E_Z + cw*64 + ((w ^ (k2 & 3))*16) + ri*8); }
    bf16x8 zr = cat8(v[0][0], v[0][1]), zi = cat8(v[1][0], v[1][1]);
    Qr[s] = mfma32(zr, C16, zero16()); Qr[s] = mfma32(neg8(zi), S16, Qr[s]);
    Qi[s] = mfma32(zi, C16, zero16()); Qi[s] = mfma32(zr, S16, Qi[s]);
  }
  {
    u32x2 z0 = *(LAS u32x2*)(size_t)(E_Z + w*16);
    float f = (h == 0 && (l31 & 1)) ? -2.f : 0.f;
    Qr[0][0] += f * bflo(z0[0]); Qr[0][2] += f * bflo(z0[1]); }
  #pragma unroll
  for (int row=0; row<2; row++) {
    f32x16 Y = zero16();
    #pragma unroll
    for (int s=0;s<2;s++) { float a_[8], b_[8];
      #pragma unroll
      for (int j=0;j<8;j++) { int r = 4*(j>>1) + 2*row + (j&1); float qr = Qr[s][r], qi = Qi[s][r]; a_[j] = qr*tc[8*s+j] - qi*ts_[8*s+j]; b_[j] = qi*tc[8*s+j] + qr*ts_[8*s+j]; }
      Y = mfma32(pack8(a_), C2[s], Y); Y = mfma32(pack8(b_), S2[s], Y); }
    const int lr = 2*w + row;
    unsigned o[8];
    #pragma unroll
    for (int gg=0; gg<2; gg++) {
      u32x4 p0 = *(LAS u32x4*)(size_t)(E_P + l31*512 + lr*64 + (2*gg + h)*16);
      u32x4 p1 = *(LAS u32x4*)(size_t)(E_P + 16384 + l31*512 + lr*64 + (2*gg + h)*16);
      #pragma unroll
      for (int z=0; z<2; z++) { int ga = gg + 2*z;
        u32x2 xv = *(LAS u32x2*)(size_t)(E_X + lr*2048 + l31*64 + ga*16 + h*8);
        float v0 = Y[4*ga+0] + bflo(xv[0]) + bflo(p0[2*z]) + bflo(p1[2*z]);
        float v1 = Y[4*ga+1] + bfhi(xv[0]) + bfhi(p0[2*z]) + bfhi(p1[2*z]);
        float v2 = Y[4*ga+2] + bflo(xv[1]) + bflo(p0[2*z+1]) + bflo(p1[2*z+1]);
        float v3 = Y[4*ga+3] + bfhi(xv[1]) + bfhi(p0[2*z+1]) + bfhi(p1[2*z+1]);
        o[2*ga] = cvtpk(v0, v1); o[2*ga+1] = cvtpk(v2, v3); } }
    char* ob = (char*)out + (size_t)(rw*8 + lr)*2048 + l31*64 + h*16;
    #pragma unroll
    for (int k=0;k<4;k+=2) {
      auto r0 = __builtin_amdgcn_permlane32_swap(o[2*k], o[2*k+2], false, false);
      auto r1 = __builtin_amdgcn_permlane32_swap(o[2*k+1], o[2*k+3], false, false);
      *(u32x4*)(ob + k*16) = (u32x4){r0[0], r1[0], r0[1], r1[1]}; }
  }
}

extern "C" void kernel_launch(void* const* d_in, const int* in_sizes, int n_in, void* d_out, int out_size, void* d_ws, size_t ws_size, hipStream_t stream) {
  const u16* x=(const u16*)d_in[0]; const u16* wr=(const u16*)d_in[1]; const u16* wi=(const u16*)d_in[2];
  const u16* w1=(const u16*)d_in[4]; const u16* b1=(const u16*)d_in[5]; const u16* w2=(const u16*)d_in[6];
  char* ws=(char*)d_ws;
  (void)hipFuncSetAttribute((const void*)k_b, hipFuncAttributeMaxDynamicSharedMemorySize, G1_TOT);
  (void)hipFuncSetAttribute((const void*)k_d, hipFuncAttributeMaxDynamicSharedMemorySize, G2_TOT);
  (void)hipFuncSetAttribute((const void*)k_a, hipFuncAttributeMaxDynamicSharedMemorySize, A_TOT);
  (void)hipFuncSetAttribute((const void*)k_c, hipFuncAttributeMaxDynamicSharedMemorySize, C_TOT);
  (void)hipFuncSetAttribute((const void*)k_e, hipFuncAttributeMaxDynamicSharedMemorySize, E_TOT);
  k_a<<<256,512,A_TOT,stream>>>(x, wr, wi, w1, ws);
  k_b<<<256,512,G1_TOT,stream>>>(x, w1, b1, ws);
  k_c<<<256,256,C_TOT,stream>>>(ws);
  k_d<<<256,512,G2_TOT,stream>>>(w2, ws);
  k_e<<<256,256,E_TOT,stream>>>(x, ws, (u16*)d_out);
}
```

```cpp
#include <hip/hip_runtime.h>
#include <hip/hip_bf16.h>
#include <stdint.h>
typedef unsigned short u16;
typedef unsigned long long u64;
#define LAS __attribute__((address_space(3)))
typedef short s16x4 __attribute__((ext_vector_type(4)));
typedef __bf16 bf16x8 __attribute__((ext_vector_type(8)));
typedef __bf16 bf16x2 __attribute__((ext_vector_type(2)));
typedef float f32x2 __attribute__((ext_vector_type(2)));
typedef float f32x4 __attribute__((ext_vector_type(4)));
typedef float f32x16 __attribute__((ext_vector_type(16)));
typedef unsigned u32x4 __attribute__((ext_vector_type(4)));
typedef unsigned u32x2 __attribute__((ext_vector_type(2)));
typedef int i32x4 __attribute__((ext_vector_type(4)));
#define DI __device__ __forceinline__

#define S_ 2048
#define D_ 1024
#define F_ 3584
#define DF_ 513

constexpr size_t WS_H    = 0;
constexpr size_t WS_ZW   = 16u<<20;
constexpr size_t WS_Z2   = 24u<<20;
constexpr size_t WS_PART = 28u<<20;
constexpr size_t WS_CS   = 36u<<20;
constexpr size_t WS_STAT = 37u<<20;

DI void dma16(i32x4 rsrc, unsigned voff, unsigned soff, unsigned ldsdst) {
  asm volatile("s_mov_b32 m0, %3\n\ts_nop 0\n\tbuffer_load_dwordx4 %0, %1, %2 offen lds" :: "v"(voff), "s"(rsrc), "s"(soff), "s"(ldsdst) : "memory");
}
DI i32x4 mkrsrc(const void* p, unsigned bytes) {
  u64 a = (u64)p; i32x4 r; r[0] = __builtin_amdgcn_readfirstlane((unsigned)a); r[1] = __builtin_amdgcn_readfirstlane((unsigned)(a>>32)) & 0xffff; r[2] = bytes; r[3] = 0x00020000; return r;
}
DI unsigned cvtpk(float lo, float hi) { f32x2 v = {lo, hi}; return __builtin_bit_cast(unsigned, __builtin_convertvector(v, bf16x2)); }
DI float bflo(unsigned u) { return __uint_as_float(u << 16); }
DI float bfhi(unsigned u) { return __uint_as_float(u & 0xffff0000u); }
#define WAITV(n) asm volatile("s_waitcnt vmcnt(%0)" :: "n"(n) : "memory")
#define BAR() do { asm volatile("" ::: "memory"); __builtin_amdgcn_s_barrier(); asm volatile("" ::: "memory"); } while(0)
#define SB() __builtin_amdgcn_sched_barrier(0)
#define WAITL0() __builtin_amdgcn_s_waitcnt(0xC07F)
#define LBAR() do { WAITL0(); BAR(); } while(0)
DI s16x4 trrd(unsigned a) { return __builtin_amdgcn_ds_read_tr16_b64_v4i16((LAS s16x4*)(size_t)a); }
DI bf16x8 cat8(s16x4 lo, s16x4 hi) { return __builtin_bit_cast(bf16x8, __builtin_shufflevector(lo, hi, 0,1,2,3,4,5,6,7)); }
DI f32x16 mfma32(bf16x8 a, bf16x8 b, f32x16 c) { return __builtin_amdgcn_mfma_f32_32x32x16_bf16(a, b, c, 0,0,0); }
DI float cosr(float r) { return __builtin_amdgcn_cosf(r); }
DI float sinr(float r) { return __builtin_amdgcn_sinf(r); }
DI int crow(int r, int h) { return (r & 3) + 8*(r >> 2) + 4*h; }
DI bf16x8 pack8(const float* v) { u32x4 p = { cvtpk(v[0],v[1]), cvtpk(v[2],v[3]), cvtpk(v[4],v[5]), cvtpk(v[6],v[7]) }; return __builtin_bit_cast(bf16x8, p); }
DI f32x16 zero16() { f32x16 z;
  #pragma unroll
  for (int i=0;i<16;i++) z[i]=0.f; return z; }
DI bf16x8 neg8(bf16x8 v) { u32x4 p = __builtin_bit_cast(u32x4, v); p ^= 0x80008000u; return __builtin_bit_cast(bf16x8, p); }

template<int CTRL> DI float dppadd(float v) { return v + __builtin_bit_cast(float, __builtin_amdgcn_update_dpp(0, __builtin_bit_cast(int, v), CTRL, 0xf, 0xf, true)); }
DI float wave_sum(float v) {
  v = dppadd<0xB1>(v);
  v = dppadd<0x4E>(v);
  v = dppadd<0x141>(v);
  v = dppadd<0x140>(v);
  { auto r = __builtin_amdgcn_permlane16_swap(__float_as_uint(v), __float_as_uint(v), false, false); v = __uint_as_float(r[0]) + __uint_as_float(r[1]); }
  { auto r = __builtin_amdgcn_permlane32_swap(__float_as_uint(v), __float_as_uint(v), false, false); v = __uint_as_float(r[0]) + __uint_as_float(r[1]); }
  return v;
}
template<int N> DI void dft_frag(int idx, int kbase, float phase, bf16x8& c, bf16x8& s) {
  float cv[8], sv[8];
  #pragma unroll
  for (int j=0;j<8;j++) { int k = kbase + 8*(j>>2) + (j&3); float r = (float)((idx*k) & (N-1)) * (1.0f/N) + phase; cv[j] = cosr(r); sv[j] = sinr(r); }
  c = pack8(cv); s = pack8(sv);
}

constexpr int A_XR = 0, A_WR = 16384, A_WI = A_WR + 9216, A_CS = A_WI + 9216, A_TOT = A_CS + 28672;
__global__ __launch_bounds__(512, 2) void k_a(const u16* __restrict__ x, const u16* __restrict__ wr, const u16* __restrict__ wi, const u16* __restrict__ w1, char* __restrict__ ws) {
  extern __shared__ __attribute__((aligned(16))) char smem[];
  const int wid = __builtin_amdgcn_readfirstlane(threadIdx.x >> 6), lane = threadIdx.x & 63;
  const int b = blockIdx.x, xcd = b & 7, idx = b >> 3;
  const int tm = (xcd >> 1)*4 + (idx & 3), tn = (xcd & 1)*8 + (idx >> 2);
  const int gi = lane >> 4, q = (lane & 15) >> 2, p = lane & 3, h = lane >> 5, l31 = lane & 31;
  char* zw = ws + WS_ZW + (size_t)b*32768;
  if (wid >= 4) {
    const int w = wid - 4;
    i32x4 rx = mkrsrc(x, S_*D_*2), rr = mkrsrc(wr, S_*DF_*2), ri = mkrsrc(wi, S_*DF_*2), rb = mkrsrc(w1, D_*F_*2);
    #pragma unroll
    for (int i=0;i<4;i++) dma16(rx, lane*16, b*16384 + (w*4+i)*1024, A_XR + (w*4+i)*1024);
    #pragma unroll
    for (int i=0;i<5;i++) { int pc = w + 4*i; if (pc < 18) { if (pc < 9) dma16(rr, lane*16, b*8208 + pc*1024, A_WR + pc*1024); else dma16(ri, lane*16, b*8208 + (pc-9)*1024, A_WI + (pc-9)*1024); } }
    #pragma unroll
    for (int i=0;i<7;i++) { int pc = w + 4*i; int ci = pc*64 + lane; int r = (ci*9363)>>18, c = ci - r*28; c ^= ((r>>3)&1)<<1;
      dma16(rb, (unsigned)(r*F_*2 + tn*448 + c*16), tm*64*F_*2, A_CS + pc*1024); }
    WAITV(0);
    BAR();
    const int t = w*64 + lane;
    #pragma unroll
    for (int i=0;i<4;i++) {
      int id = t + 256*i, cw = id >> 2, col = (id >> 1) & 1, hf = id & 1;
      int k = 2*cw + col; if (k == 0) k = 512;
      u32x4 v;
      #pragma unroll
      for (int e=0;e<4;e++) { unsigned off = ((hf*4+e)*DF_ + k)*2; unsigned a = *(LAS u16*)(size_t)(A_WR + off), c = *(LAS u16*)(size_t)(A_WI + off); v[e] = a | (c << 16); }
      *(u32x4*)(zw + cw*128 + 64 + (id&3)*16) = v;
    }
    const u32x4 onesu = {0x3f803f80u,0x3f803f80u,0x3f803f80u,0x3f803f80u};
    const bf16x8 ones = __builtin_bit_cast(bf16x8, onesu);
    float* cs = (float*)(ws + WS_CS) + tm*F_ + tn*224;
    #pragma unroll
    for (int i=0;i<4;i++) { int u = w + 4*i; if (u < 14) {
      f32x4 a = {0,0,0,0};
      #pragma unroll
      for (int sub=0; sub<2; sub++) { unsigned base = A_CS + (32*sub + 8*gi + q)*448 + ((u ^ (gi&1))*32) + 8*p;
        a = __builtin_amdgcn_mfma_f32_16x16x32_bf16(cat8(trrd(base), trrd(base + 1792)), ones, a, 0,0,0); }
      if ((lane & 15) == 0) *(f32x4*)(cs + u*16 + 4*gi) = a; } }
    BAR();
    BAR();
  } else {
    bf16x8 Cp[2], Sp[2], M1[2], M2[2];
    #pragma unroll
    for (int s=0;s<2;s++) { dft_frag<32>(l31, 16*s + 4*h, 0.f, Cp[s], Sp[s]); Sp[s] = neg8(Sp[s]); dft_frag<32>(l31 & 15, 16*s + 4*h, (l31 >= 16) ? 0.25f : 0.f, M1[s], M2[s]); }
    float twc[16], tws[16];
    #pragma unroll
    for (int r=0;r<16;r++) { float a = (float)(crow(r,h)*l31) * (1.0f/1024); twc[r] = cosr(a); tws[r] = sinr(a); }
    BAR();
    f32x16 X[2];
    bf16x8 xf[2][2];
    #pragma unroll
    for (int rr=0; rr<2; rr++)
      #pragma unroll
      for (int s=0;s<2;s++) { unsigned base = A_XR + (wid*2+rr)*2048 + (16*s + 4*(gi>>1) + q)*64 + (gi&1)*32 + 8*p; xf[rr][s] = cat8(trrd(base), trrd(base + 512)); }
    LBAR();
    #pragma unroll
    for (int rr=0; rr<2; rr++) {
      f32x16 Pr = zero16(), Pi = zero16();
      #pragma unroll
      for (int s=0;s<2;s++) { Pr = mfma32(xf[rr][s], Cp[s], Pr); Pi = mfma32(xf[rr][s], Sp[s], Pi); }
      float s1 = 0.f, s2 = 0.f;
      const bf16x2 one2 = __builtin_bit_cast(bf16x2, 0x3f803f80u);
      #pragma unroll
      for (int s=0;s<2;s++) { u32x4 u = __builtin_bit_cast(u32x4, xf[rr][s]);
        #pragma unroll
        for (int e=0;e<4;e++) { unsigned ue = u[e]; bf16x2 v = __builtin_bit_cast(bf16x2, ue); s1 = __builtin_amdgcn_fdot2_f32_bf16(v, one2, s1, false); s2 = __builtin_amdgcn_fdot2_f32_bf16(v, v, s2, false); } }
      float ny = wave_sum((lane & 1) ? -s1 : s1);
      s1 = wave_sum(s1); s2 = wave_sum(s2);
      float mean = s1 * (1.0f/D_), var = fmaxf(s2 * (1.0f/D_) - mean*mean, 0.f);
      float rstd = __builtin_amdgcn_rsqf(var + 1e-5f);
      if (lane == 0) ((f32x2*)(ws + WS_STAT))[b*8 + wid*2 + rr] = (f32x2){rstd * 1.4426950408889634f, mean * rstd * 1.4426950408889634f};
      float sub = (l31 == 0) ? 32.f*mean : 0.f;
      float tr_[16], ti_[16];
      #pragma unroll
      for (int r=0;r<16;r++) { float a = (Pr[r] - sub) * rstd, bb = Pi[r] * rstd; tr_[r] = a*twc[r] + bb*tws[r]; ti_[r] = bb*twc[r] - a*tws[r]; }
      f32x16 Y = zero16();
      #pragma unroll
      for (int s=0;s<2;s++) { Y = mfma32(M1[s], pack8(tr_ + 8*s), Y); Y = mfma32(M2[s], pack8(ti_ + 8*s), Y); }
      if (lane == 0) { Y[0] = ny * rstd; Y[8] = 0.f; }
      X[rr] = Y;
    }
    #pragma unroll
    for (int r=0;r<16;r++) { int k2 = crow(r & 7, h), ri = r >> 3; int cw = (l31 >> 1) + 16*k2; int ch = cw*4 + (l31 & 1)*2 + ri;
      *(LAS unsigned*)(size_t)(A_XR + ch*16 + ((wid ^ ((l31 >> 2) & 3))*4)) = cvtpk(X[0][r], X[1][r]); }
    LBAR();
    #pragma unroll
    for (int i=0;i<4;i++) { int ch = wid*64 + lane + 256*i; u32x4 v = *(LAS u32x4*)(size_t)(A_XR + ch*16);
      int sw = (ch >> 3) & 3;
      u32x4 o;
      if (sw & 1) v = (u32x4){v[1], v[0], v[3], v[2]};
      if (sw & 2) v = (u32x4){v[2], v[3], v[0], v[1]};
      o = v;
      *(u32x4*)(zw + (ch >> 2)*128 + (ch & 3)*16) = o; }
  }
}

constexpr int A_SLOT = 16384, B_SLOT = 14336;
constexpr int G1_A = 0, G1_B = 3*A_SLOT, G1_TOT = G1_B + 6*B_SLOT;
DI unsigned mish2(f32x2 u) {
  f32x2 t; t.x = __builtin_amdgcn_exp2f(u.x); t.y = __builtin_amdgcn_exp2f(u.y);
  f32x2 a = t + 1.0f; f32x2 d = a*a + 1.0f;
  f32x2 r; r.x = __builtin_amdgcn_rcpf(d.x); r.y = __builtin_amdgcn_rcpf(d.y);
  f32x2 f = r * (-1.3862943611198906f) + 0.6931471805599453f;
  f32x2 o = u * f;
  return cvtpk(o.x, o.y);
}
__global__ __launch_bounds__(512, 2) void k_b(const u16* __restrict__ x, const u16* __restrict__ w1, const u16* __restrict__ b1, char* __restrict__ ws, const u16* __restrict__ w2) {
  extern __shared__ __attribute__((aligned(16))) char smem[];
  const int wid = __builtin_amdgcn_readfirstlane(threadIdx.x >> 6), lane = threadIdx.x & 63;
  const int b = blockIdx.x, xcd = b & 7, idx = b >> 3;
  const int tm = (xcd >> 1)*4 + (idx & 3), tn = (xcd & 1)*8 + (idx >> 2);
  constexpr int NK = D_/64;
  if (wid >= 4) {
    const int w = wid - 4;
    i32x4 ra = mkrsrc(x, S_*D_*2), rb = mkrsrc(w1, D_*F_*2);
    unsigned va[4], vb[7];
    #pragma unroll
    for (int i=0;i<4;i++) { int p = w*4+i; int r = p*8 + (lane>>3); va[i] = (unsigned)((tm*128 + r)*D_*2 + (((lane&7) ^ (r&7))*16)); }
    #pragma unroll
    for (int i=0;i<7;i++) { int p = w + 4*i; int ci = p*64 + lane; int r = (ci*9363)>>18, c = ci - r*28; c ^= ((r>>3)&1)<<1; vb[i] = (unsigned)(r*F_*2 + tn*448 + c*16); }
    const unsigned da = G1_A + w*4096, db = G1_B + w*1024;
    auto issue = [&](int k, int slot) {
      unsigned soa = k*128, sob = k*64*F_*2, a = da + slot*A_SLOT, bb = db + slot*2*B_SLOT;
      #pragma unroll
      for (int i=0;i<4;i++) dma16(ra, va[i], soa, a + i*1024);
      #pragma unroll
      for (int i=0;i<7;i++) dma16(rb, vb[i], sob, bb + i*4096);
    };
    issue(0, 0); issue(1, 1);
    WAITV(11); BAR();
    issue(2, 2);
    { i32x4 r2 = mkrsrc(w2, F_*D_*2); unsigned dummy; unsigned vo = (lane < 56 ? lane : 55)*128 + w*7168 + b*28672;
      asm volatile("buffer_load_dword %0, %1, %2, 0 offen" : "=v"(dummy) : "v"(vo), "s"(r2) : "memory"); }
    int slot = 0;
    #pragma unroll 1
    for (int k = 0; k < NK-3; k++) { WAITV(11); BAR(); issue(k+3, slot); slot = slot == 2 ? 0 : slot+1; }
    WAITV(11); BAR();
    const int t = w*64 + lane;
    float csn = 0.f, bn = 0.f;
    if (t < 224) { const float* cs = (const float*)(ws + WS_CS) + tn*224 + t;
      #pragma unroll
      for (int i=0;i<16;i++) csn += cs[i*F_];
      bn = bflo(b1[tn*224 + t]) * 1.4426950408889634f; }
    WAITV(0); BAR();
    if (t < 224) { *(LAS float*)(size_t)(G1_A + A_SLOT + t*4) = bn; *(LAS float*)(size_t)(G1_A + A_SLOT + 1024 + t*4) = csn; }
    LBAR();
  } else {
    const int wm = wid & 1, wn = wid >> 1;
    const int g = lane >> 4, c = lane & 15, q = (lane & 15) >> 2, p = lane & 3;
    f32x4 acc[7][4];
    #pragma unroll
    for (int i=0;i<7;i++)
      #pragma unroll
      for (int j=0;j<4;j++) { acc[i][j] = (f32x4){0,0,0,0}; asm volatile("" : "+v"(acc[i][j])); }
    f32x2 st[4];
    #pragma unroll
    for (int j=0;j<4;j++) st[j] = ((const f32x2*)(ws + WS_STAT))[tm*128 + wm*64 + j*16 + c];
    unsigned xak[2];
    xak[0] = G1_A + ((wm*64 + c)>>3)*1024 + (c&7)*128 + ((g ^ (c&7))*16);
    xak[1] = G1_A + ((wm*64 + c)>>3)*1024 + (c&7)*128 + (((4+g) ^ (c&7))*16);
    const unsigned wbase = G1_B + (8*g + q)*448 + 8*p + wn*7*32;
    const unsigned sgn = (g&1)*32;
    unsigned wb[2][2];
    wb[0][0] = wn ? wbase - sgn : wbase + sgn;
    wb[0][1] = wn ? wbase + sgn : wbase - sgn;
    wb[1][0] = wb[0][0] + 3*B_SLOT; wb[1][1] = wb[0][1] + 3*B_SLOT;
    asm volatile("" : "+v"(wb[1][0]), "+v"(wb[1][1]), "+v"(xak[0]), "+v"(xak[1]), "+v"(wb[0][0]), "+v"(wb[0][1]));
    asm volatile("" : "+v"(st[0]), "+v"(st[1]), "+v"(st[2]), "+v"(st[3]));
    bf16x8 xf[2][4], wf[2][7];
    s16x4 lo_[7];
    auto rd = [&](int set, int r, int n) {
      if (n >= 2 && n < 6) { xf[set][n-2] = *(LAS bf16x8*)(size_t)(xak[r&1] + (r>>1)*A_SLOT + (n-2)*2048); return; }
      int i = n < 2 ? 0 : (n-6)/2+1, t = n < 2 ? n : (n-6)&1;
      s16x4 v = trrd(wb[r/3][i&1] + (r%3)*B_SLOT + i*32 + t*1792);
      if (t==0) lo_[i] = v; else wf[set][i] = cat8(lo_[i], v);
    };
    auto mm = [&](int set, int n) { int i = n>>2, j = (i&1) ? 3-(n&3) : (n&3);
      asm volatile("v_mfma_f32_16x16x32_bf16 %0, %1, %2, %0" : "+v"(acc[i][j]) : "v"(wf[set][i]), "v"(xf[set][j])); };
    BAR();
    #pragma unroll
    for (int n=0;n<18;n++) rd(0, 0, n);
    WAITL0();
    auto step = [&](int set, int r1, bool bar) {
      SB();
      #pragma unroll
      for (int n=0;n<28;n++) { mm(set, n); SB(); if (n < 18) { rd(set^1, r1, n); SB(); }
        if (n == 25) { WAITL0(); SB(); }
        if (n == 26 && bar) { BAR(); SB(); } }
    };
    #pragma unroll 1
    for (int s = 0; s < 30; s += 6) { step(0, 1, true); step(1, 2, false); step(0, 3, true); step(1, 4, false); step(0, 5, true); step(1, 0, false); }
    step(0, 1, true);
    #pragma unroll
    for (int n=0;n<28;n++) mm(1, n);
    asm volatile("s_nop 7\n\ts_nop 7" ::: "memory");
    SB();
    f32x4 bl[7], cl[7];
    #pragma unroll
    for (int i=0;i<7;i++) { unsigned a = G1_A + A_SLOT + (wn*112 + i*16 + 4*g)*4; bl[i] = *(LAS f32x4*)(size_t)a; cl[i] = *(LAS f32x4*)(size_t)(a + 1024); }
    #pragma unroll
    for (int j=0;j<4;j++) {
      int mb = tm*8 + wm*4 + j;
      char* hb = ws + WS_H + ((size_t)mb*112 + tn*7)*1024 + lane*16;
      unsigned pk[7][2];
      #pragma unroll
      for (int i=0;i<7;i++) { f32x4 v = acc[i][j] * st[j].x + (bl[i] - cl[i] * st[j].y); pk[i][0] = mish2((f32x2){v[0], v[1]}); pk[i][1] = mish2((f32x2){v[2], v[3]}); }
      if (wn == 0) {
        #pragma unroll
        for (int i=0;i<3;i++) *(u32x4*)(hb + i*1024) = (u32x4){pk[2*i][0], pk[2*i][1], pk[2*i+1][0], pk[2*i+1][1]};
        *(u32x2*)(hb + 3*1024) = (u32x2){pk[6][0], pk[6][1]};
      } else {
        *(u32x2*)(hb + 3*1024 + 8) = (u32x2){pk[0][0], pk[0][1]};
        #pragma unroll
        for (int i=0;i<3;i++) *(u32x4*)(hb + (4+i)*1024) = (u32x4){pk[2*i+1][0], pk[2*i+1][1], pk[2*i+2][0], pk[2*i+2][1]};
      }
    }
  }
}

constexpr int C_IN = 0, C_EX = 32768, C_OUT = C_EX + 16384, C_TOT = C_OUT + 16384;
__global__ __launch_bounds__(256, 2) void k_c(char* __restrict__ ws) {
  extern __shared__ __attribute__((aligned(16))) char smem[];
  const int w = __builtin_amdgcn_readfirstlane(threadIdx.x >> 6), lane = threadIdx.x & 63;
  const int cw = blockIdx.x, col = w >> 1, hf = w & 1;
  const int gi = lane >> 4, q = (lane & 15) >> 2, p = lane & 3, h = lane >> 5, l31 = lane & 31;
  { i32x4 rz = mkrsrc(ws + WS_ZW, 8u<<20);
    #pragma unroll
    for (int i=0;i<8;i++) { int pc = w*8 + i; int rw = pc*8 + (lane >> 3); int ch = (lane & 7) ^ ((rw >> 1) & 7);
      dma16(rz, (unsigned)(rw*32768 + ch*16), cw*128, C_IN + pc*1024); } }
  const int u = l31 + 32*hf;
  bf16x8 C64[4], S64[4], C32[2], S32[2];
  #pragma unroll
  for (int s=0;s<4;s++) dft_frag<64>(u, 16*s + 4*h, 0.f, C64[s], S64[s]);
  #pragma unroll
  for (int s=0;s<2;s++) dft_frag<32>(l31, 16*s + 4*h, 0.f, C32[s], S32[s]);
  float fc[16], fs[16], ic[16], is[16];
  #pragma unroll
  for (int r=0;r<16;r++) { float a = (float)(crow(r,h)*u) * (1.0f/2048); fc[r] = cosr(a); fs[r] = sinr(a);
    float a2 = (float)((crow(r,h) + 32*hf)*l31) * (1.0f/2048); ic[r] = cosr(a2); is[r] = sinr(a2); }
  WAITV(0); BAR();
  f32x16 Pr = zero16(), Pi = zero16();
  #pragma unroll
  for (int s=0;s<4;s++) {
    bf16x8 zr, zi; s16x4 v[2][2];
    #pragma unroll
    for (int t=0;t<2;t++) { int a = 16*s + 8*t + 4*(gi>>1) + q; int P = 4*(gi&1) + p; int rw = a*4 + (P >> 1);
      #pragma unroll
      for (int ri=0;ri<2;ri++) v[ri][t] = trrd(C_IN + rw*128 + (((col*2 + ri) ^ ((rw >> 1) & 7))*16) + (P & 1)*8); }
    zr = cat8(v[0][0], v[0][1]); zi = cat8(v[1][0], v[1][1]);
    Pr = mfma32(zr, C64[s], Pr); Pr = mfma32(zi, S64[s], Pr);
    Pi = mfma32(zi, C64[s], Pi); Pi = mfma32(neg8(zr), S64[s], Pi);
  }
  unsigned wv[16];
  #pragma unroll
  for (int r=0;r<16;r++) { int j = u + 64*crow(r,h); int rw = j >> 3; int ch = 4 + col*2 + ((j >> 2) & 1);
    wv[r] = *(LAS unsigned*)(size_t)(C_IN + rw*128 + ((ch ^ ((rw >> 1) & 7))*16) + (j & 3)*4); }
  float a_[16], b_[16];
  #pragma unroll
  for (int r=0;r<16;r++) { a_[r] = Pr[r]*fc[r] + Pi[r]*fs[r]; b_[r] = Pi[r]*fc[r] - Pr[r]*fs[r]; }
  f32x16 Ur = zero16(), Ui = zero16();
  #pragma unroll
  for (int s=0;s<2;s++) { bf16x8 tr_ = pack8(a_ + 8*s), ti_ = pack8(b_ + 8*s);
    Ur = mfma32(C32[s], tr_, Ur); Ur = mfma32(S32[s], ti_, Ur);
    Ui = mfma32(C32[s], ti_, Ui); Ui = mfma32(S32[s], neg8(tr_), Ui); }
  const float sc = ((cw == 0 && col == 0) ? 1.0f : 2.0f) / (float)(S_*D_);
  #pragma unroll
  for (int r=0;r<16;r++) { float wre = bflo(wv[r])*sc, wim = bfhi(wv[r])*sc; a_[r] = Ur[r]*wre - Ui[r]*wim; b_[r] = Ur[r]*wim + Ui[r]*wre; }
  f32x16 Qr = zero16(), Qi = zero16();
  #pragma unroll
  for (int s=0;s<2;s++) { bf16x8 yr = pack8(a_ + 8*s), yi = pack8(b_ + 8*s);
    Qr = mfma32(yr, C32[s], Qr); Qr = mfma32(neg8(yi), S32[s], Qr);
    Qi = mfma32(yi, C32[s], Qi); Qi = mfma32(yr, S32[s], Qi); }
  #pragma unroll
  for (int r=0;r<16;r++) { a_[r] = Qr[r]*ic[r] - Qi[r]*is[r]; b_[r] = Qi[r]*ic[r] + Qr[r]*is[r]; }
  bf16x8 Rr[4], Ri[4];
  { bf16x8 r0 = pack8(a_), r1 = pack8(a_+8), i0 = pack8(b_), i1 = pack8(b_+8);
    unsigned ex = C_EX + w*4096 + lane*16;
    *(LAS bf16x8*)(size_t)(ex) = r0; *(LAS bf16x8*)(size_t)(ex+1024) = r1; *(LAS bf16x8*)(size_t)(ex+2048) = i0; *(LAS bf16x8*)(size_t)(ex+3072) = i1;
    LBAR();
    #pragma unroll
    for (int s=0;s<4;s++) { unsigned e2 = C_EX + (col*2 + (s>>1))*4096 + lane*16 + (s&1)*1024; Rr[s] = *(LAS bf16x8*)(size_t)e2; Ri[s] = *(LAS bf16x8*)(size_t)(e2 + 2048); } }
  f32x16 Zr = zero16(), Zi = zero16();
  #pragma unroll
  for (int s=0;s<4;s++) { Zr = mfma32(Rr[s], C64[s], Zr); Zr = mfma32(neg8(Ri[s]), S64[s], Zr);
    Zi = mfma32(Ri[s], C64[s], Zi); Zi = mfma32(Rr[s], S64[s], Zi); }
  #pragma unroll
  for (int r=0;r<16;r++) { int rw = 4*u + (r >> 2), rp = 2*h + ((r & 3) >> 1), row = r & 1;
    unsigned pk = cvtpk(Zr[r], Zi[r]);
    unsigned ad = C_OUT + (((rw*4 + rp) ^ (l31 & 15))*16) + row*4 + col*2;
    *(LAS u16*)(size_t)(ad) = (u16)pk; *(LAS u16*)(size_t)(ad + 8) = (u16)(pk >> 16); }
  LBAR();
  char* z2 = ws + WS_Z2 + (size_t)cw*16384;
  #pragma unroll
  for (int i=0;i<4;i++) { int ch = w*64 + lane + 256*i; u32x4 v = *(LAS u32x4*)(size_t)(C_OUT + ((ch ^ ((ch >> 4) & 15))*16)); *(u32x4*)(z2 + ch*16) = v; }
}

constexpr int G2_NS = 4, G2_A = 0, G2_B = G2_NS*16384, G2_TOT = 2*G2_NS*16384;
__global__ __launch_bounds__(512, 2) void k_d(const u16* __restrict__ w2, char* __restrict__ ws) {
  extern __shared__ __attribute__((aligned(16))) char smem[];
  const int wid = __builtin_amdgcn_readfirstlane(threadIdx.x >> 6), lane = threadIdx.x & 63;
  const int b = blockIdx.x, xcd = b & 7, idx = b >> 3;
  const int ks = xcd & 1, tm = (xcd >> 1)*4 + (idx & 3), tn = idx >> 2;
  constexpr int NK = 28;
  if (wid >= 4) {
    const int w = wid - 4;
    i32x4 ra = mkrsrc(ws + WS_H, S_*F_*2), rb = mkrsrc(w2, F_*D_*2);
    unsigned va[4], vb[4];
    #pragma unroll
    for (int i=0;i<4;i++) { int p = w*4+i; va[i] = (unsigned)(((tm*8 + (p>>1))*112 + ks*56 + (p&1))*1024 + lane*16); }
    #pragma unroll
    for (int i=0;i<4;i++) { int p = w*4+i; int r = p*4 + (lane>>4); int c = (lane&15) ^ ((r&7)<<1); vb[i] = (unsigned)((ks*1792 + r)*D_*2 + tn*256 + c*16); }
    auto issue = [&](int k, int slot) {
      unsigned a = G2_A + slot*16384 + w*4096, bb = G2_B + slot*16384 + w*4096;
      #pragma unroll
      for (int i=0;i<4;i++) dma16(rb, vb[i], k*64*D_*2, bb + i*1024);
      #pragma unroll
      for (int i=0;i<4;i++) dma16(ra, va[i], k*2048, a + i*1024);
    };
    issue(0, 0); issue(1, 1);
    WAITV(8); BAR();
    issue(2, 2); issue(3, 3);
    #pragma unroll 1
    for (int k = 0; k < NK-4; k++) { WAITV(16); BAR(); issue(k+4, k & 3); }
    WAITV(16); BAR();
    WAITV(8); BAR();
    WAITV(0); BAR();
  } else {
    const int wm = wid & 1, wn = wid >> 1;
    const int g = lane >> 4, c = lane & 15, q = (lane & 15) >> 2, p = lane & 3;
    f32x4 acc[4][4];
    #pragma unroll
    for (int i=0;i<4;i++)
      #pragma unroll
      for (int j=0;j<4;j++) { acc[i][j] = (f32x4){0,0,0,0}; asm volatile("" : "+v"(acc[i][j])); }
    unsigned xa = G2_A + wm*8192 + lane*16;
    unsigned wb[4];
    { int rho = 4*(g&1) + q;
      #pragma unroll
      for (int i=0;i<4;i++) wb[i] = G2_B + (4*g + q)*256 + (((wn*4 + i) ^ rho)*32) + 8*p; }
    asm volatile("" : "+v"(xa), "+v"(wb[0]), "+v"(wb[1]), "+v"(wb[2]), "+v"(wb[3]));
    bf16x8 xf[2][2][4], wf[2][2][4];
    s16x4 lo_;
    auto rd = [&](int set, int r, int n) {
      int sub = n / 12, m = n % 12;
      if (m >= 2 && m < 6) { xf[set][sub][m-2] = *(LAS bf16x8*)(size_t)(xa + r*16384 + (m-2)*2048 + sub*1024); return; }
      int i = m < 2 ? 0 : (m-6)/2+1, t = m < 2 ? m : (m-6)&1;
      s16x4 v = trrd(wb[i] + r*16384 + sub*8192 + t*4096);
      if (t==0) lo_ = v; else wf[set][sub][i] = cat8(lo_, v);
    };
    auto mm = [&](int set, int n) { int sub = n & 1, m = n >> 1; int i = m>>2, j = (i&1) ? 3-(m&3) : (m&3);
      asm volatile("v_mfma_f32_16x16x32_bf16 %0, %1, %2, %0" : "+v"(acc[i][j]) : "v"(wf[set][sub][i]), "v"(xf[set][sub][j])); };
    BAR();
    #pragma unroll
    for (int n=0;n<24;n++) rd(0, 0, n);
    WAITL0(); BAR();
    auto step = [&](int set, int r1, bool bar) {
      SB();
      #pragma unroll
      for (int n=0;n<32;n++) { mm(set, n); SB(); if (n < 24) { rd(set^1, r1, n); SB(); }
        if (n == 29) { WAITL0(); SB(); }
        if (n == 30 && bar) { BAR(); SB(); } }
    };
    #pragma unroll 1
    for (int s = 0; s < 24; s += 4) { step(0, 1, true); step(1, 2, true); step(0, 3, true); step(1, 0, true); }
    step(0, 1, true); step(1, 2, true); step(0, 3, false);
    #pragma unroll
    for (int n=0;n<32;n++) mm(1, n);
    asm volatile("s_nop 7\n\ts_nop 7" ::: "memory");
    SB();
    #pragma unroll
    for (int j=0;j<4;j++) {
      int m = tm*128 + wm*64 + j*16 + c;
      #pragma unroll
      for (int ip=0; ip<2; ip++) { int n1 = tn*4 + wn*2 + ip;
        u32x4 v = { cvtpk(acc[2*ip][j][0], acc[2*ip][j][1]), cvtpk(acc[2*ip][j][2], acc[2*ip][j][3]), cvtpk(acc[2*ip+1][j][0], acc[2*ip+1][j][1]), cvtpk(acc[2*ip+1][j][2], acc[2*ip+1][j][3]) };
        *(u32x4*)(ws + WS_PART + (size_t)ks*(4u<<20) + (size_t)(m >> 3)*16384 + n1*512 + (m & 7)*64 + g*16) = v; }
    }
  }
}

constexpr int E_Z = 0, E_X = 16384, E_P = 32768, E_TOT = E_P + 32768;
__global__ __launch_bounds__(256, 2) void k_e(const u16* __restrict__ x, char* __restrict__ ws, u16* __restrict__ out) {
  extern __shared__ __attribute__((aligned(16))) char smem[];
  const int w = __builtin_amdgcn_readfirstlane(threadIdx.x >> 6), lane = threadIdx.x & 63;
  const int rw = blockIdx.x;
  const int gi = lane >> 4, q = (lane & 15) >> 2, p = lane & 3, h = lane >> 5, l31 = lane & 31;
  { i32x4 rz = mkrsrc(ws + WS_Z2, 4u<<20), rx = mkrsrc(x, S_*D_*2), rp = mkrsrc(ws + WS_PART, 8u<<20);
    #pragma unroll
    for (int i=0;i<4;i++) { int pc = w*4 + i; int cw = pc*16 + (lane >> 2); int ch = (lane & 3) ^ (pc & 3);
      dma16(rz, (unsigned)(cw*16384 + ch*16), rw*64, E_Z + pc*1024); }
    #pragma unroll
    for (int i=0;i<4;i++) dma16(rx, lane*16, rw*16384 + (w*4+i)*1024, E_X + (w*4+i)*1024);
    #pragma unroll
    for (int i=0;i<8;i++) { int pc = w*8 + i; dma16(rp, lane*16, (pc >> 4)*(4u<<20) + rw*16384 + (pc & 15)*1024, E_P + pc*1024); } }
  bf16x8 C16, S16, C2[2], S2[2];
  dft_frag<32>(l31, 4*h, 0.f, C16, S16);
  {
    #pragma unroll
    for (int s=0;s<2;s++) { float cv[8], sv[8];
      #pragma unroll
      for (int j=0;j<8;j++) { int k1 = 16*s + 2*(2*(j>>1) + h) + (j&1); float r = (float)((k1*l31) & 31) * (1.0f/32); cv[j] = cosr(r); sv[j] = -sinr(r); }
      C2[s] = pack8(cv); S2[s] = pack8(sv); } }
  float tc[16], ts_[16];
  #pragma unroll
  for (int s=0;s<2;s++)
    #pragma unroll
    for (int j=0;j<8;j++) { int k1 = 16*s + 2*(2*(j>>1) + h) + (j&1); float a = (float)(k1*l31) * (1.0f/1024); tc[8*s+j] = cosr(a); ts_[8*s+j] = sinr(a); }
  WAITV(0); BAR();
  f32x16 Qr[2], Qi[2];
  #pragma unroll
  for (int s=0;s<2;s++) {
    s16x4 v[2][2];
    #pragma unroll
    for (int t=0;t<2;t++) { int k2 = 8*t + 4*(gi>>1) + q; int cw = 16*k2 + 8*s + 4*(gi&1) + p;
      #pragma unroll
      for (int ri=0;ri<2;ri++) v[ri][t] = trrd(E_Z + cw*64 + ((w ^ (k2 & 3))*16) + ri*8); }
    bf16x8 zr = cat8(v[0][0], v[0][1]), zi = cat8(v[1][0], v[1][1]);
    Qr[s] = mfma32(zr, C16, zero16()); Qr[s] = mfma32(neg8(zi), S16, Qr[s]);
    Qi[s] = mfma32(zi, C16, zero16()); Qi[s] = mfma32(zr, S16, Qi[s]);
  }
  {
    u32x2 z0 = *(LAS u32x2*)(size_t)(E_Z + w*16);
    float f = (h == 0 && (l31 & 1)) ? -2.f : 0.f;
    Qr[0][0] += f * bflo(z0[0]); Qr[0][2] += f * bflo(z0[1]); }
  #pragma unroll
  for (int row=0; row<2; row++) {
    f32x16 Y = zero16();
    #pragma unroll
    for (int s=0;s<2;s++) { float a_[8], b_[8];
      #pragma unroll
      for (int j=0;j<8;j++) { int r = 4*(j>>1) + 2*row + (j&1); float qr = Qr[s][r], qi = Qi[s][r]; a_[j] = qr*tc[8*s+j] - qi*ts_[8*s+j]; b_[j] = qi*tc[8*s+j] + qr*ts_[8*s+j]; }
      Y = mfma32(pack8(a_), C2[s], Y); Y = mfma32(pack8(b_), S2[s], Y); }
    const int lr = 2*w + row;
    unsigned o[8];
    #pragma unroll
    for (int gg=0; gg<2; gg++) {
      u32x4 p0 = *(LAS u32x4*)(size_t)(E_P + l31*512 + lr*64 + (2*gg + h)*16);
      u32x4 p1 = *(LAS u32x4*)(size_t)(E_P + 16384 + l31*512 + lr*64 + (2*gg + h)*16);
      #pragma unroll
      for (int z=0; z<2; z++) { int ga = gg + 2*z;
        u32x2 xv = *(LAS u32x2*)(size_t)(E_X + lr*2048 + l31*64 + ga*16 + h*8);
        float v0 = Y[4*ga+0] + bflo(xv[0]) + bflo(p0[2*z]) + bflo(p1[2*z]);
        float v1 = Y[4*ga+1] + bfhi(xv[0]) + bfhi(p0[2*z]) + bfhi(p1[2*z]);
        float v2 = Y[4*ga+2] + bflo(xv[1]) + bflo(p0[2*z+1]) + bflo(p1[2*z+1]);
        float v3 = Y[4*ga+3] + bfhi(xv[1]) + bfhi(p0[2*z+1]) + bfhi(p1[2*z+1]);
        o[2*ga] = cvtpk(v0, v1); o[2*ga+1] = cvtpk(v2, v3); } }
    char* ob = (char*)out + (size_t)(rw*8 + lr)*2048 + l31*64 + h*16;
    #pragma unroll
    for (int k=0;k<4;k+=2) {
      auto r0 = __builtin_amdgcn_permlane32_swap(o[2*k], o[2*k+2], false, false);
      auto r1 = __builtin_amdgcn_permlane32_swap(o[2*k+1], o[2*k+3], false, false);
      *(u32x4*)(ob + k*16) = (u32x4){r0[0], r1[0], r0[1], r1[1]}; }
  }
}

extern "C" void kernel_launch(void* const* d_in, const int* in_sizes, int n_in, void* d_out, int out_size, void* d_ws, size_t ws_size, hipStream_t stream) {
  const u16* x=(const u16*)d_in[0]; const u16* wr=(const u16*)d_in[1]; const u16* wi=(const u16*)d_in[2];
  const u16* w1=(const u16*)d_in[4]; const u16* b1=(const u16*)d_in[5]; const u16* w2=(const u16*)d_in[6];
  char* ws=(char*)d_ws;
  (void)hipFuncSetAttribute((const void*)k_b, hipFuncAttributeMaxDynamicSharedMemorySize, G1_TOT);
  (void)hipFuncSetAttribute((const void*)k_d, hipFuncAttributeMaxDynamicSharedMemorySize, G2_TOT);
  (void)hipFuncSetAttribute((const void*)k_a, hipFuncAttributeMaxDynamicSharedMemorySize, A_TOT);
  (void)hipFuncSetAttribute((const void*)k_c, hipFuncAttributeMaxDynamicSharedMemorySize, C_TOT);
  (void)hipFuncSetAttribute((const void*)k_e, hipFuncAttributeMaxDynamicSharedMemorySize, E_TOT);
  k_a<<<256,512,A_TOT,stream>>>(x, wr, wi, w1, ws);
  k_b<<<256,512,G1_TOT,stream>>>(x, w1, b1, ws, w2);
  k_c<<<256,256,C_TOT,stream>>>(ws);
  k_d<<<256,512,G2_TOT,stream>>>(w2, ws);
  k_e<<<256,256,E_TOT,stream>>>(x, ws, (u16*)d_out);
}
```

```cpp
#include <hip/hip_runtime.h>
#include <hip/hip_bf16.h>
#include <stdint.h>
typedef unsigned short u16;
typedef unsigned long long u64;
#define LAS __attribute__((address_space(3)))
typedef short s16x4 __attribute__((ext_vector_type(4)));
typedef __bf16 bf16x8 __attribute__((ext_vector_type(8)));
typedef __bf16 bf16x2 __attribute__((ext_vector_type(2)));
typedef float f32x2 __attribute__((ext_vector_type(2)));
typedef float f32x4 __attribute__((ext_vector_type(4)));
typedef float f32x16 __attribute__((ext_vector_type(16)));
typedef unsigned u32x4 __attribute__((ext_vector_type(4)));
typedef unsigned u32x2 __attribute__((ext_vector_type(2)));
typedef int i32x4 __attribute__((ext_vector_type(4)));
#define DI __device__ __forceinline__

#define S_ 2048
#define D_ 1024
#define F_ 3584
#define DF_ 513

constexpr unsigned WS_H    = 0;
constexpr unsigned WS_ZW   = 16u<<20;
constexpr unsigned WS_Z2   = 24u<<20;
constexpr unsigned WS_PART = 28u<<20;
constexpr unsigned WS_CS   = 36u<<20;
constexpr unsigned WS_STAT = 37u<<20;
constexpr unsigned WS_FLAG = 38u<<20;
constexpr unsigned WS_E = WS_FLAG, WS_F1 = WS_FLAG + 4096, WS_F2 = WS_FLAG + 8192, WS_F3 = WS_FLAG + 12288, WS_F4 = WS_FLAG + 20480;
constexpr unsigned WS_USED = 40u<<20;

constexpr int A_SLOT = 16384, B_SLOT = 14336;
constexpr int G1_A = 0, G1_B = 3*A_SLOT, L_EX = G1_B + 6*B_SLOT;
constexpr int L_MISC = L_EX + 18432;
constexpr int L_BN = L_MISC, L_CN = L_MISC + 1024, L_ST = L_MISC + 2048, L_RS = L_MISC + 3072, L_FL = L_MISC + 3200, L_ODD = L_MISC + 4096;
constexpr int L_TOT = 163840;
constexpr int C_IN0 = L_EX, C_IN1 = 16384, C_EXC = 32768, C_OUT = 0;
constexpr int G2_A = 0, G2_B = 65536, E_Z = 131072, E_Y = 65536;

#define TS(slot)

DI void dma16(i32x4 rsrc, unsigned voff, unsigned soff, unsigned ldsdst) {
  asm volatile("s_mov_b32 m0, %3\n\ts_nop 0\n\tbuffer_load_dwordx4 %0, %1, %2 offen lds" :: "v"(voff), "s"(rsrc), "s"(soff), "s"(ldsdst) : "memory");
}
DI void dma16s(i32x4 rsrc, unsigned voff, unsigned soff, unsigned ldsdst) {
  asm volatile("s_mov_b32 m0, %3\n\ts_nop 0\n\tbuffer_load_dwordx4 %0, %1, %2 offen sc1 lds" :: "v"(voff), "s"(rsrc), "s"(soff), "s"(ldsdst) : "memory");
}
DI void touch(i32x4 rsrc, unsigned voff, unsigned soff, unsigned ldsdst) {
  asm volatile("s_mov_b32 m0, %3\n\ts_nop 0\n\tbuffer_load_dword %0, %1, %2 offen lds" :: "v"(voff), "s"(rsrc), "s"(soff), "s"(ldsdst) : "memory");
}
DI i32x4 mkrsrc(const void* p, unsigned bytes) {
  u64 a = (u64)p; i32x4 r; r[0] = __builtin_amdgcn_readfirstlane((unsigned)a); r[1] = __builtin_amdgcn_readfirstlane((unsigned)(a>>32)) & 0xffff; r[2] = bytes; r[3] = 0x00020000; return r;
}
typedef __amdgpu_buffer_rsrc_t rsrc_t;
DI void st16(rsrc_t r, unsigned off, u32x4 v) { __builtin_amdgcn_raw_buffer_store_b128(v, r, off, 0, 16); }
DI void st8(rsrc_t r, unsigned off, u32x2 v) { __builtin_amdgcn_raw_buffer_store_b64(v, r, off, 0, 16); }
DI u32x4 ld16(rsrc_t r, unsigned off) { return __builtin_amdgcn_raw_buffer_load_b128(r, off, 0, 16); }
DI u32x2 ld8(rsrc_t r, unsigned off) { return __builtin_amdgcn_raw_buffer_load_b64(r, off, 0, 16); }
DI unsigned cvtpk(float lo, float hi) { f32x2 v = {lo, hi}; return __builtin_bit_cast(unsigned, __builtin_convertvector(v, bf16x2)); }
DI float bflo(unsigned u) { return __uint_as_float(u << 16); }
DI float bfhi(unsigned u) { return __uint_as_float(u & 0xffff0000u); }
#define WAITV(n) asm volatile("s_waitcnt vmcnt(%0)" :: "n"(n) : "memory")
#define BAR() do { asm volatile("" ::: "memory"); __builtin_amdgcn_s_barrier(); asm volatile("" ::: "memory"); } while(0)
#define SB() __builtin_amdgcn_sched_barrier(0)
#define WAITL0() __builtin_amdgcn_s_waitcnt(0xC07F)
#define LBAR() do { WAITL0(); BAR(); } while(0)
#define PIN(x) asm volatile("" : "+v"(x))
DI s16x4 trrd(unsigned a) { return __builtin_amdgcn_ds_read_tr16_b64_v4i16((LAS s16x4*)(size_t)a); }
DI bf16x8 cat8(s16x4 lo, s16x4 hi) { return __builtin_bit_cast(bf16x8, __builtin_shufflevector(lo, hi, 0,1,2,3,4,5,6,7)); }
DI f32x16 mfma32(bf16x8 a, bf16x8 b, f32x16 c) { return __builtin_amdgcn_mfma_f32_32x32x16_bf16(a, b, c, 0,0,0); }
DI float cosr(float r) { return __builtin_amdgcn_cosf(r); }
DI float sinr(float r) { return __builtin_amdgcn_sinf(r); }
DI int crow(int r, int h) { return (r & 3) + 8*(r >> 2) + 4*h; }
DI bf16x8 pack8(const float* v) { u32x4 p = { cvtpk(v[0],v[1]), cvtpk(v[2],v[3]), cvtpk(v[4],v[5]), cvtpk(v[6],v[7]) }; return __builtin_bit_cast(bf16x8, p); }
DI f32x16 zero16() { f32x16 z;
  #pragma unroll
  for (int i=0;i<16;i++) z[i]=0.f; return z; }
DI bf16x8 xor8(bf16x8 v, unsigned m) { u32x4 p = __builtin_bit_cast(u32x4, v); p ^= m; return __builtin_bit_cast(bf16x8, p); }
DI bf16x8 neg8(bf16x8 v) { return xor8(v, 0x80008000u); }
template<typename T> DI T ldsr(unsigned a) { return *(LAS T*)(size_t)a; }
template<typename T> DI void ldsw(unsigned a, T v) { *(LAS T*)(size_t)a = v; }

template<int CTRL> DI float dppadd(float v) { return v + __builtin_bit_cast(float, __builtin_amdgcn_update_dpp(0, __builtin_bit_cast(int, v), CTRL, 0xf, 0xf, true)); }
DI float wave_sum(float v) {
  v = dppadd<0xB1>(v); v = dppadd<0x4E>(v); v = dppadd<0x141>(v); v = dppadd<0x140>(v);
  { auto r = __builtin_amdgcn_permlane16_swap(__float_as_uint(v), __float_as_uint(v), false, false); v = __uint_as_float(r[0]) + __uint_as_float(r[1]); }
  { auto r = __builtin_amdgcn_permlane32_swap(__float_as_uint(v), __float_as_uint(v), false, false); v = __uint_as_float(r[0]) + __uint_as_float(r[1]); }
  return v;
}
DI void trig_frag(float step, float base, bf16x8& c, bf16x8& s) {
  float cv[8], sv[8];
  #pragma unroll
  for (int j=0;j<8;j++) { float r = fmaf(step, (float)(8*(j>>2) + (j&3)), base); cv[j] = cosr(r); sv[j] = sinr(r); }
  c = pack8(cv); s = pack8(sv);
}
DI unsigned mish2(f32x2 u) {
  f32x2 t; t.x = __builtin_amdgcn_exp2f(u.x); t.y = __builtin_amdgcn_exp2f(u.y);
  f32x2 a = t + 1.0f; f32x2 d = a*a + 1.0f;
  f32x2 r; r.x = __builtin_amdgcn_rcpf(d.x); r.y = __builtin_amdgcn_rcpf(d.y);
  f32x2 f = r * (-1.3862943611198906f) + 0.6931471805599453f;
  f32x2 o = u * f;
  return cvtpk(o.x, o.y);
}
constexpr int SPIN_MAX = 1 << 17;
DI void poll1(rsrc_t r, unsigned off, bool act, u32x2 tag) {
  for (int it = 0; it < SPIN_MAX; it++) {
    u32x2 v = ld8(r, off);
    if (__builtin_amdgcn_ballot_w64(act && (v[0] != tag[0] || v[1] != tag[1])) == 0) break;
    __builtin_amdgcn_s_sleep(2);
  }
  asm volatile("" ::: "memory");
}
DI void poll256(rsrc_t r, unsigned base, int lane, u32x2 tag) {
  for (int it = 0; it < SPIN_MAX; it++) {
    u32x4 a = ld16(r, base + lane*16), b = ld16(r, base + 1024 + lane*16);
    bool bad = a[0] != tag[0] || a[1] != tag[1] || a[2] != tag[0] || a[3] != tag[1] || b[0] != tag[0] || b[1] != tag[1] || b[2] != tag[0] || b[3] != tag[1];
    if (__builtin_amdgcn_ballot_w64(bad) == 0) break;
    __builtin_amdgcn_s_sleep(4);
  }
  asm volatile("" ::: "memory");
}
DI void lsync(unsigned flbase, int me, int lane, unsigned phase, unsigned mask) {
  WAITL0(); asm volatile("" ::: "memory");
  if (lane == 0) *(volatile LAS unsigned*)(size_t)(flbase + me*4) = phase;
  for (int it = 0; it < (1 << 22); it++) {
    unsigned v = *(volatile LAS unsigned*)(size_t)(flbase + (lane & 3)*4);
    if (__builtin_amdgcn_ballot_w64(((mask >> (lane & 3)) & 1) && v < phase) == 0) break;
  }
  asm volatile("" ::: "memory");
}

__global__ __launch_bounds__(512, 2) void k_all(const u16* __restrict__ x, const u16* __restrict__ wr, const u16* __restrict__ wi,
    const u16* __restrict__ w1, const u16* __restrict__ b1, const u16* __restrict__ w2, char* __restrict__ ws, u16* __restrict__ out) {
  extern __shared__ __attribute__((aligned(16))) char smem[];
  const int wid = __builtin_amdgcn_readfirstlane(threadIdx.x >> 6), lane = threadIdx.x & 63;
  const int b = blockIdx.x, xcd = b & 7, idx = b >> 3;
  const int tm = (xcd >> 1)*4 + (idx & 3), ks = xcd & 1, tn2 = idx >> 2, tn = ks*8 + tn2;
  const int gid = tm*16 + tn;
  const int gi = lane >> 4, q = (lane & 15) >> 2, p = lane & 3, h = lane >> 5, l31 = lane & 31;
  TS(0);
  u64 ep;
  asm volatile("s_load_dwordx2 %0, %1, 0x0" : "=s"(ep) : "s"(ws + WS_E) : "memory");
  const rsrc_t rw_ = __builtin_amdgcn_make_buffer_rsrc(ws, 0, WS_USED, 0x00020000);
  const i32x4 rx = mkrsrc(x, S_*D_*2);
  u32x2 tag;
  #define GETTAG() do { asm volatile("s_waitcnt lgkmcnt(0)" : "+s"(ep) :: "memory"); u64 t_ = ep * 0x9E3779B97F4A7C15ull + 0x632BE59BD9B4E019ull; tag = (u32x2){(unsigned)t_, (unsigned)(t_ >> 32)}; } while (0)

  if (wid < 4) {
    #pragma unroll
    for (int i=0;i<4;i++) dma16(rx, lane*16, gid*16384 + (wid*4+i)*1024, L_EX + (wid*4+i)*1024);
    {
      bf16x8 Cp[2], Sp[2], M1[2], M2[2];
      { float f = (float)l31 * (1.0f/32); trig_frag(f, f*(float)(4*h), Cp[0], Sp[0]); Sp[0] = neg8(Sp[0]);
        unsigned m = (l31 & 1) ? 0x80008000u : 0u; Cp[1] = xor8(Cp[0], m); Sp[1] = xor8(Sp[0], m);
        float f2 = (float)(l31 & 15) * (1.0f/32); trig_frag(f2, f2*(float)(4*h) + ((l31 >= 16) ? 0.25f : 0.f), M1[0], M2[0]);
        M1[1] = xor8(M1[0], m); M2[1] = xor8(M2[0], m); }
      float twc[16], tws[16];
      { float f = (float)l31 * (1.0f/1024), bs = f*(float)(4*h);
        #pragma unroll
        for (int r=0;r<16;r++) { float a = fmaf(f, (float)crow(r,0), bs); twc[r] = cosr(a); tws[r] = sinr(a); PIN(twc[r]); PIN(tws[r]); } }
      GETTAG();
      WAITV(0);
      TS(1);
      bf16x8 xf[2][2];
      #pragma unroll
      for (int rr=0; rr<2; rr++)
        #pragma unroll
        for (int s=0;s<2;s++) { unsigned base = L_EX + (wid*2+rr)*2048 + (16*s + 4*(gi>>1) + q)*64 + (gi&1)*32 + 8*p; xf[rr][s] = cat8(trrd(base), trrd(base + 512)); }
      f32x16 X[2];
      f32x2 stat[2];
      #pragma unroll
      for (int rr=0; rr<2; rr++) {
        f32x16 Pr = zero16(), Pi = zero16();
        #pragma unroll
        for (int s=0;s<2;s++) { Pr = mfma32(xf[rr][s], Cp[s], Pr); Pi = mfma32(xf[rr][s], Sp[s], Pi); }
        float s1 = 0.f, s2 = 0.f;
        const bf16x2 one2 = __builtin_bit_cast(bf16x2, 0x3f803f80u);
        #pragma unroll
        for (int s=0;s<2;s++) { u32x4 u = __builtin_bit_cast(u32x4, xf[rr][s]);
          #pragma unroll
          for (int e=0;e<4;e++) { unsigned ue = u[e]; bf16x2 v = __builtin_bit_cast(bf16x2, ue); s1 = __builtin_amdgcn_fdot2_f32_bf16(v, one2, s1, false); s2 = __builtin_amdgcn_fdot2_f32_bf16(v, v, s2, false); } }
        float ny = wave_sum((lane & 1) ? -s1 : s1);
        s1 = wave_sum(s1); s2 = wave_sum(s2);
        float mean = s1 * (1.0f/D_), var = fmaxf(s2 * (1.0f/D_) - mean*mean, 0.f);
        float rstd = __builtin_amdgcn_rsqf(var + 1e-5f);
        stat[rr] = (f32x2){rstd * 1.4426950408889634f, mean * rstd * 1.4426950408889634f};
        float sub = (l31 == 0) ? 32.f*mean : 0.f;
        float tr_[16], ti_[16];
        #pragma unroll
        for (int r=0;r<16;r++) { float a = Pr[r] - sub, bb = Pi[r]; tr_[r] = a*twc[r] + bb*tws[r]; ti_[r] = bb*twc[r] - a*tws[r]; }
        f32x16 Y = zero16();
        #pragma unroll
        for (int s=0;s<2;s++) { Y = mfma32(M1[s], pack8(tr_ + 8*s), Y); Y = mfma32(M2[s], pack8(ti_ + 8*s), Y); }
        if (lane == 0) { Y[0] = ny; Y[8] = 0.f; }
        X[rr] = Y * rstd;
      }
      asm volatile("" ::: "memory");
      #pragma unroll
      for (int r=0;r<8;r++) ldsw<u32x2>(L_EX + wid*4096 + (l31 + 32*crow(r,h))*8, (u32x2){cvtpk(X[0][r], X[1][r]), cvtpk(X[0][r+8], X[1][r+8])});
      if (lane == 0) ldsw<f32x4>(L_RS + wid*16, (f32x4){stat[0].x, stat[0].y, stat[1].x, stat[1].y});
    }
    const int wm = wid & 1, wn = wid >> 1;
    const int g = gi, c = lane & 15;
    {
      f32x4 acc[7][4];
      #pragma unroll
      for (int i=0;i<7;i++)
        #pragma unroll
        for (int j=0;j<4;j++) { acc[i][j] = (f32x4){0,0,0,0}; PIN(acc[i][j]); }
      unsigned xak[2];
      xak[0] = G1_A + ((wm*64 + c)>>3)*1024 + (c&7)*128 + ((g ^ (c&7))*16);
      xak[1] = G1_A + ((wm*64 + c)>>3)*1024 + (c&7)*128 + (((4+g) ^ (c&7))*16);
      const unsigned wbase = G1_B + (8*g + q)*448 + 8*p;
      const unsigned sgn = (g&1)*32;
      unsigned wb[2][3];
      wb[0][0] = wbase + wn*256 + sgn;
      wb[0][1] = wbase + wn*256 - sgn;
      wb[0][2] = wn ? wbase + 32 - sgn : wbase + sgn;
      #pragma unroll
      for (int i=0;i<3;i++) { wb[1][i] = wb[0][i] + 3*B_SLOT; PIN(wb[0][i]); PIN(wb[1][i]); }
      PIN(xak[0]); PIN(xak[1]);
      bf16x8 xf[2][4], wf[2][7];
      s16x4 lo_[7];
      auto rd = [&](int set, int r, int n) {
        if (n >= 2 && n < 6) { xf[set][n-2] = ldsr<bf16x8>(xak[r&1] + (r>>1)*A_SLOT + (n-2)*2048); return; }
        int i = n < 2 ? 0 : (n-6)/2+1, t = n < 2 ? n : (n-6)&1;
        s16x4 v = trrd(wb[r/3][i == 6 ? 2 : (i&1)] + (r%3)*B_SLOT + i*32 + t*1792);
        if (t==0) lo_[i] = v; else wf[set][i] = cat8(lo_[i], v);
      };
      auto mm = [&](int set, int n) { int i = n>>2, j = (i&1) ? 3-(n&3) : (n&3);
        asm volatile("v_mfma_f32_16x16x32_bf16 %0, %1, %2, %0" : "+v"(acc[i][j]) : "v"(wf[set][i]), "v"(xf[set][j])); };
      LBAR();
      TS(2);
      #pragma unroll
      for (int n=0;n<18;n++) rd(0, 0, n);
      WAITL0();
      auto step = [&](int set, int r1, bool bar) {
        SB();
        #pragma unroll
        for (int n=0;n<28;n++) { mm(set, n); SB(); if (n < 18) { rd(set^1, r1, n); SB(); }
          if (n == 25) { WAITL0(); SB(); }
          if (n == 26 && bar) { BAR(); SB(); } }
      };
      #pragma unroll 1
      for (int s = 0; s < 30; s += 6) { step(0, 1, true); step(1, 2, false); step(0, 3, true); step(1, 4, false); step(0, 5, true); step(1, 0, false); }
      step(0, 1, true);
      f32x4 bl[7], cl[7]; f32x2 st[4];
      #pragma unroll
      for (int n=0;n<28;n++) { mm(1, n); SB();
        if (n < 7) bl[n] = ldsr<f32x4>(L_BN + ((n == 6 ? wn*16 : wn*128) + n*16 + 4*g)*4);
        else if (n < 14) cl[n-7] = ldsr<f32x4>(L_CN + ((n == 13 ? wn*16 : wn*128) + (n-7)*16 + 4*g)*4);
        else if (n < 18) st[n-14] = ldsr<f32x2>(L_ST + (wm*64 + (n-14)*16 + c)*8);
        SB(); }
      asm volatile("s_nop 7\n\ts_nop 7" ::: "memory");
      TS(3);
      SB();
      u32x2 od[4];
      {
        #pragma unroll
        for (int j=0;j<4;j++) { f32x4 v = acc[6][j] * st[j].x + (bl[6] - cl[6] * st[j].y); od[j] = (u32x2){mish2((f32x2){v[0], v[1]}), mish2((f32x2){v[2], v[3]})}; }
        u32x4 snd = wn ? (u32x4){od[0][0], od[0][1], od[1][0], od[1][1]} : (u32x4){od[2][0], od[2][1], od[3][0], od[3][1]};
        ldsw<u32x4>(L_ODD + wid*1024 + lane*16, snd);
        if (lane == 0) *(volatile LAS unsigned*)(size_t)(L_FL + 16 + wid*4) = 1u; }
      const unsigned hb0 = WS_H + (unsigned)(((tm*8 + wm*4)*112 + tn*7)*1024 + lane*16);
      #pragma unroll
      for (int j=0;j<4;j++) {
        #pragma unroll
        for (int ip=0; ip<3; ip++) {
          f32x4 v0 = acc[2*ip][j] * st[j].x + (bl[2*ip] - cl[2*ip] * st[j].y), v1 = acc[2*ip+1][j] * st[j].x + (bl[2*ip+1] - cl[2*ip+1] * st[j].y);
          u32x4 o = { mish2((f32x2){v0[0], v0[1]}), mish2((f32x2){v0[2], v0[3]}), mish2((f32x2){v1[0], v1[1]}), mish2((f32x2){v1[2], v1[3]}) };
          st16(rw_, hb0 + j*112*1024 + (wn*4 + ip)*1024, o);
        }
        if (j == 1) {
          for (int it = 0; it < (1 << 22); it++) { if (*(volatile LAS unsigned*)(size_t)(L_FL + 16 + (wid ^ 2)*4) != 0u) break; }
          asm volatile("" ::: "memory");
          u32x4 rcv = ldsr<u32x4>(L_ODD + (wid ^ 2)*1024 + lane*16);
          u32x4 o0 = wn ? (u32x4){rcv[0], rcv[1], od[2][0], od[2][1]} : (u32x4){od[0][0], od[0][1], rcv[0], rcv[1]};
          u32x4 o1 = wn ? (u32x4){rcv[2], rcv[3], od[3][0], od[3][1]} : (u32x4){od[1][0], od[1][1], rcv[2], rcv[3]};
          st16(rw_, hb0 + (2*wn)*112*1024 + 3*1024, o0);
          st16(rw_, hb0 + (2*wn+1)*112*1024 + 3*1024, o1);
        }
      }
      TS(4);
      WAITV(0);
      if (lane == 0) st8(rw_, WS_F3 + (gid*4 + wid)*8, tag);
    }
    {
      f32x4 acc[4][4];
      #pragma unroll
      for (int i=0;i<4;i++)
        #pragma unroll
        for (int j=0;j<4;j++) { acc[i][j] = (f32x4){0,0,0,0}; PIN(acc[i][j]); }
      unsigned xa = G2_A + wm*8192 + lane*16;
      unsigned wb[4];
      { int rho = 4*(g&1) + q;
        #pragma unroll
        for (int i=0;i<4;i++) wb[i] = G2_B + (4*g + q)*256 + (((wn*4 + i) ^ rho)*32) + 8*p; }
      asm volatile("" : "+v"(xa), "+v"(wb[0]), "+v"(wb[1]), "+v"(wb[2]), "+v"(wb[3]));
      bf16x8 xf[2][2][4], wf[2][2][4];
      s16x4 lo_;
      auto rd = [&](int set, int r, int n) {
        int sub = n / 12, m = n % 12;
        if (m >= 2 && m < 6) { xf[set][sub][m-2] = ldsr<bf16x8>(xa + r*16384 + (m-2)*2048 + sub*1024); return; }
        int i = m < 2 ? 0 : (m-6)/2+1, t = m < 2 ? m : (m-6)&1;
        s16x4 v = trrd(wb[i] + r*16384 + sub*8192 + t*4096);
        if (t==0) lo_ = v; else wf[set][sub][i] = cat8(lo_, v);
      };
      auto mm = [&](int set, int n) { int sub = n & 1, m = n >> 1; int i = m>>2, j = (i&1) ? 3-(m&3) : (m&3);
        asm volatile("v_mfma_f32_16x16x32_bf16 %0, %1, %2, %0" : "+v"(acc[i][j]) : "v"(wf[set][sub][i]), "v"(xf[set][sub][j])); };
      BAR();
      TS(5);
      #pragma unroll
      for (int n=0;n<24;n++) rd(0, 0, n);
      WAITL0(); BAR();
      auto step = [&](int set, int r1, bool bar) {
        SB();
        #pragma unroll
        for (int n=0;n<32;n++) { mm(set, n); SB(); if (n < 24) { rd(set^1, r1, n); SB(); }
          if (n == 29) { WAITL0(); SB(); }
          if (n == 30 && bar) { BAR(); SB(); } }
      };
      #pragma unroll 1
      for (int s = 0; s < 24; s += 4) { step(0, 1, true); step(1, 2, true); step(0, 3, true); step(1, 0, true); }
      step(0, 1, true); step(1, 2, true); step(0, 3, false);
      #pragma unroll
      for (int n=0;n<32;n++) { mm(1, n); SB(); if (n == 27) { BAR(); SB(); } }
      asm volatile("s_nop 7\n\ts_nop 7" ::: "memory");
      TS(6);
      SB();
      const unsigned pb = G2_A + wid*8192;
      #pragma unroll
      for (int j=0;j<4;j++)
        #pragma unroll
        for (int i=0;i<4;i++) ldsw<u32x2>(pb + (j*16 + c)*128 + (((i*2 + (g>>1)) ^ (c & 7))*16) + (g&1)*8, (u32x2){cvtpk(acc[i][j][0], acc[i][j][1]), cvtpk(acc[i][j][2], acc[i][j][3])});
      asm volatile("" ::: "memory");
      #pragma unroll
      for (int i=0;i<8;i++) { int r = i*8 + (lane >> 3); u32x4 v = ldsr<u32x4>(pb + r*128 + (((lane & 7) ^ (r & 7))*16));
        st16(rw_, WS_PART + ks*(4u<<20) + (unsigned)((tm*128 + wm*64 + r)*2048 + tn2*256 + wn*128 + (lane & 7)*16), v); }
      WAITV(0);
      if (lane == 0) st8(rw_, WS_F4 + (gid*4 + wid)*8, tag);
    }
  } else {
    const int w = wid - 4;
    const i32x4 rb = mkrsrc(w1, D_*F_*2), rz = mkrsrc(ws, WS_USED);
    { int r = w*16 + (lane >> 2); unsigned o = (unsigned)(tn*448 + (lane & 3)*128); if ((lane & 3) == 3) o = tn*448 + 384;
      touch(rb, (unsigned)(r*F_*2) + o, tm*64*F_*2, L_ODD + w*256); }
    const int col = w >> 1, hf = w & 1;
    bf16x8 C64[4], S64[4], CA[2], SA[2], CB[2], SB_[2];
    float twc[16], tws[16];
    auto col_consts = [&]() {
      const int u = l31 + 32*hf;
      { float f = (float)u * (1.0f/64); trig_frag(f, f*(float)(4*h), C64[0], S64[0]); trig_frag(f, f*(float)(16 + 4*h), C64[1], S64[1]);
        unsigned m = (u & 1) ? 0x80008000u : 0u; C64[2] = xor8(C64[0], m); S64[2] = xor8(S64[0], m); C64[3] = xor8(C64[1], m); S64[3] = xor8(S64[1], m); }
      { float f = (float)(2*l31 + hf) * (1.0f/64); trig_frag(f, f*(float)(4*h), CA[0], SA[0]); trig_frag(f, f*(float)(16 + 4*h), CA[1], SA[1]); }
      { float f = (float)l31 * (1.0f/32); trig_frag(f, f*((float)(4*h) + 0.5f*(float)hf), CB[0], SB_[0]);
        unsigned m = (l31 & 1) ? 0x80008000u : 0u; CB[1] = xor8(CB[0], m); SB_[1] = xor8(SB_[0], m); }
      { float f = (float)l31 * (1.0f/2048), bs = f*(float)(4*h);
        #pragma unroll
        for (int r=0;r<16;r++) { float a = fmaf(f, (float)crow(r,0), bs); twc[r] = cosr(a); tws[r] = sinr(a); PIN(twc[r]); PIN(tws[r]); } }
      #pragma unroll
      for (int s=0;s<4;s++) { PIN(C64[s]); PIN(S64[s]); }
      #pragma unroll
      for (int s=0;s<2;s++) { PIN(CA[s]); PIN(SA[s]); PIN(CB[s]); PIN(SB_[s]); }
    };
    if (w < 2) {
      __builtin_amdgcn_s_setprio(2);
      unsigned va[8], vb[14];
      #pragma unroll
      for (int i=0;i<8;i++) { int pc = w*8+i; int r = pc*8 + (lane>>3); va[i] = (unsigned)((tm*128 + r)*D_*2 + (((lane&7) ^ (r&7))*16)); }
      #pragma unroll
      for (int i=0;i<14;i++) { int pc = w + 2*i; int ci = pc*64 + lane; int r = (ci*9363)>>18, c = ci - r*28; c ^= ((r>>3)&1)<<1; vb[i] = (unsigned)(r*F_*2 + tn*448 + c*16); }
      auto issueA = [&](int k, int slot) {
        #pragma unroll
        for (int i=0;i<8;i++) dma16(rx, va[i], k*128, G1_A + w*8192 + slot*A_SLOT + i*1024); };
      auto issueB = [&](int k, int slot) {
        #pragma unroll
        for (int i=0;i<14;i++) dma16(rb, vb[i], k*64*F_*2, G1_B + w*1024 + slot*2*B_SLOT + i*2048); };
      issueA(0,0); issueB(0,0); issueA(1,1); issueB(1,1); issueA(2,2);
      __builtin_amdgcn_s_setprio(0);
      col_consts();
      GETTAG();
      __builtin_amdgcn_s_setprio(2);
      WAITV(30); BAR();
      issueB(2,2);
      int slot = 0;
      #pragma unroll 1
      for (int k = 0; k < 13; k++) { WAITV(22); BAR(); issueA(k+3, slot); issueB(k+3, slot); slot = slot == 2 ? 0 : slot+1; }
      __builtin_amdgcn_s_setprio(0);
      WAITV(22); BAR();
      WAITV(0); BAR();
      BAR();
      { const i32x4 r2 = mkrsrc(w2, F_*D_*2);
        unsigned vo = (unsigned)((lane < 56 ? lane : 55)*128);
        touch(r2, vo, b*28672 + w*14336, L_MISC + 8192 + w*512); touch(r2, vo, b*28672 + w*14336 + 7168, L_MISC + 8192 + w*512 + 256); }
    } else {
      const int a = w - 2, t = a*64 + lane;
      const i32x4 rwt = mkrsrc(a ? wi : wr, S_*DF_*2);
      if (a == 0) {
        #pragma unroll 4
        for (int pc=0; pc<28; pc++) { int ci = pc*64 + lane; int r = (ci*9363)>>18, c = ci - r*28; c ^= ((r>>3)&1)<<1;
          dma16(rb, (unsigned)(r*F_*2 + tn*448 + c*16), tm*64*F_*2, G1_B + 4*B_SLOT + pc*1024); }
      } else { if (lane < 16) *(volatile LAS unsigned*)(size_t)(L_FL + lane*4) = 0u; }
      col_consts();
      GETTAG();
      if (a == 0) {
        WAITV(0);
        const u32x4 onesu = {0x3f803f80u,0x3f803f80u,0x3f803f80u,0x3f803f80u};
        const bf16x8 ones = __builtin_bit_cast(bf16x8, onesu);
        #pragma unroll 2
        for (int u=0; u<14; u++) {
          f32x4 s4 = {0,0,0,0};
          #pragma unroll
          for (int sub=0; sub<2; sub++) { unsigned base = G1_B + 4*B_SLOT + (32*sub + 8*gi + q)*448 + ((u ^ (gi&1))*32) + 8*p;
            s4 = __builtin_amdgcn_mfma_f32_16x16x32_bf16(cat8(trrd(base), trrd(base + 1792)), ones, s4, 0,0,0); }
          if ((lane & 15) == 0) st16(rw_, WS_CS + (unsigned)((tm*F_ + tn*224 + u*16 + 4*gi)*4), __builtin_bit_cast(u32x4, s4));
        }
      }
      touch(rwt, lane*128, gid*8208, L_ODD + 1024 + a*512); touch(rwt, lane*128, gid*8208 + 128, L_ODD + 1024 + a*512 + 256);
      LBAR();
      const unsigned zw = WS_ZW + gid*32768;
      #pragma unroll
      for (int i=0;i<8;i++) { int ch = t + 128*i; u32x4 v;
        #pragma unroll
        for (int e=0;e<4;e++) v[e] = ldsr<unsigned>(L_EX + e*4096 + ch*4);
        st16(rw_, zw + (ch >> 2)*128 + (ch & 3)*16, v); }
      if (t < 4) st16(rw_, WS_STAT + gid*64 + t*16, ldsr<u32x4>(L_RS + t*16));
      LBAR();
      #pragma unroll
      for (int i=0;i<9;i++) dma16(rwt, lane*16, gid*8208 + i*1024, L_EX + a*9216 + i*1024);
      BAR();
      WAITV(0); BAR();
      #pragma unroll 1
      for (int half=0; half<2; half++) {
        #pragma unroll
        for (int i=0;i<4;i++) { int id = t + 128*(i + 4*half); int k = id >> 1, hh = id & 1; if (k == 0) k = 512;
          u32x4 v;
          #pragma unroll
          for (int e=0;e<4;e++) { unsigned off = L_EX + ((hh*4+e)*DF_ + k)*2; v[e] = (unsigned)ldsr<u16>(off) | ((unsigned)ldsr<u16>(off + 9216) << 16); }
          st16(rw_, zw + (id >> 2)*128 + 64 + (id & 3)*16, v); }
        LBAR();
      }
      WAITV(0); BAR();
      if (t == 0) st8(rw_, WS_F1 + gid*8, tag);
      BAR(); BAR(); BAR(); BAR();
      poll256(rw_, WS_F1, lane, tag);
      if (b == 0 && t == 0) { u64 e1 = ep + 1; st8(rw_, WS_E, (u32x2){(unsigned)e1, (unsigned)(e1 >> 32)}); }
      unsigned gv = (unsigned)((lane >> 3)*32768);
      #pragma unroll
      for (int i=0;i<8;i++) { int pc = a*8 + i; dma16s(rz, gv + (((lane & 7) ^ ((pc*4 + (lane >> 4)) & 7))*16), WS_ZW + pc*8*32768 + gid*128, C_IN0 + pc*1024); }
      BAR();
      f32x2 csn = {0.f, 0.f}, bn = {0.f, 0.f}; u32x2 stv;
      { const int tt = t < 112 ? t : 111;
        #pragma unroll
        for (int i=0;i<16;i++) csn += __builtin_bit_cast(f32x2, ld8(rw_, WS_CS + (unsigned)((i*F_ + tn*224 + tt*2)*4)));
        unsigned bv = *(const unsigned*)(b1 + tn*224 + tt*2); bn = (f32x2){bflo(bv), bfhi(bv)} * 1.4426950408889634f;
        stv = ld8(rw_, WS_STAT + (unsigned)((tm*128 + t)*8)); }
      BAR();
      PIN(csn); PIN(bn); PIN(stv);
      BAR();
      BAR();
      #pragma unroll
      for (int i=0;i<8;i++) { int pc = 16 + a*8 + i; dma16s(rz, gv + (((lane & 7) ^ ((pc*4 + (lane >> 4)) & 7))*16), WS_ZW + pc*8*32768 + gid*128, C_IN1 + (pc-16)*1024); }
      WAITV(0); BAR();
      if (t < 112) { ldsw<f32x2>(L_BN + t*8, bn); ldsw<f32x2>(L_CN + t*8, csn); }
      ldsw<u32x2>(L_ST + t*8, stv);
      LBAR();
    }
    TS(3);
    {
      const int u = l31 + 32*hf;
      f32x16 Pr = zero16(), Pi = zero16();
      #pragma unroll
      for (int s=0;s<4;s++) {
        s16x4 v[2][2];
        #pragma unroll
        for (int t=0;t<2;t++) { int a = 16*(s&1) + 8*t + 4*(gi>>1) + q; int P = 4*(gi&1) + p; int rw = a*4 + (P >> 1);
          #pragma unroll
          for (int ri=0;ri<2;ri++) v[ri][t] = trrd(((s>>1) ? C_IN1 : C_IN0) + rw*128 + (((col*2 + ri) ^ ((rw >> 1) & 7))*16) + (P & 1)*8); }
        bf16x8 zr = cat8(v[0][0], v[0][1]), zi = cat8(v[1][0], v[1][1]);
        Pr = mfma32(zr, C64[s], Pr); Pi = mfma32(zi, C64[s], Pi);
        Pr = mfma32(zi, S64[s], Pr); Pi = mfma32(neg8(zr), S64[s], Pi);
      }
      unsigned wv[16];
      #pragma unroll
      for (int r=0;r<16;r++) { int j = u + 64*crow(r & 7, h); int rw = j >> 3; int ch = 4 + col*2 + ((j >> 2) & 1);
        wv[r] = ldsr<unsigned>(((r >> 3) ? C_IN1 : C_IN0) + rw*128 + ((ch ^ ((rw >> 1) & 7))*16) + (j & 3)*4); }
      float a_[16], b_[16];
      #pragma unroll
      for (int r=0;r<16;r++) { a_[r] = Pr[r]*twc[r] + Pi[r]*tws[r]; b_[r] = Pi[r]*twc[r] - Pr[r]*tws[r]; }
      f32x16 Ur = zero16(), Ui = zero16();
      #pragma unroll
      for (int s=0;s<2;s++) { bf16x8 tr_ = pack8(a_ + 8*s), ti_ = pack8(b_ + 8*s);
        Ur = mfma32(CA[s], tr_, Ur); Ui = mfma32(CA[s], ti_, Ui);
        Ur = mfma32(SA[s], ti_, Ur); Ui = mfma32(SA[s], neg8(tr_), Ui); }
      #pragma unroll
      for (int r=0;r<16;r++) { float wre = bflo(wv[r]), wim = bfhi(wv[r]); a_[r] = Ur[r]*wre - Ui[r]*wim; b_[r] = Ur[r]*wim + Ui[r]*wre; }
      f32x16 Qr = zero16(), Qi = zero16();
      #pragma unroll
      for (int s=0;s<2;s++) { bf16x8 yr = pack8(a_ + 8*s), yi = pack8(b_ + 8*s);
        Qr = mfma32(yr, CB[s], Qr); Qi = mfma32(yi, CB[s], Qi);
        Qr = mfma32(neg8(yi), SB_[s], Qr); Qi = mfma32(yr, SB_[s], Qi); }
      #pragma unroll
      for (int r=0;r<16;r++) { a_[r] = Qr[r]*twc[r] - Qi[r]*tws[r]; b_[r] = Qi[r]*twc[r] + Qr[r]*tws[r]; }
      bf16x8 Rr[4], Ri[4];
      { unsigned ex = C_EXC + w*4096 + lane*16;
        ldsw<bf16x8>(ex, pack8(a_)); ldsw<bf16x8>(ex+1024, pack8(a_+8)); ldsw<bf16x8>(ex+2048, pack8(b_)); ldsw<bf16x8>(ex+3072, pack8(b_+8));
        lsync(L_FL, w, lane, 1u, 3u << (col*2));
        #pragma unroll
        for (int s=0;s<4;s++) { unsigned e2 = C_EXC + (col*2 + (s>>1))*4096 + lane*16 + (s&1)*1024; Rr[s] = ldsr<bf16x8>(e2); Ri[s] = ldsr<bf16x8>(e2 + 2048); } }
      f32x16 Zr = zero16(), Zi = zero16();
      #pragma unroll
      for (int s=0;s<4;s++) { Zr = mfma32(Rr[s], C64[s], Zr); Zi = mfma32(Ri[s], C64[s], Zi);
        Zr = mfma32(neg8(Ri[s]), S64[s], Zr); Zi = mfma32(Rr[s], S64[s], Zi); }
      const float sc = ((gid == 0 && col == 0) ? 1.0f : 2.0f) / (float)(S_*D_);
      #pragma unroll
      for (int r=0;r<16;r++) { int rw = 4*u + (r >> 2), rp = 2*h + ((r & 3) >> 1), row = r & 1;
        unsigned pk = cvtpk(Zr[r]*sc, Zi[r]*sc);
        unsigned ad = C_OUT + (((rw*4 + rp) ^ (l31 & 15))*16) + row*4 + col*2;
        ldsw<u16>(ad, (u16)pk); ldsw<u16>(ad + 8, (u16)(pk >> 16)); }
      lsync(L_FL, w, lane, 2u, 15u);
      #pragma unroll
      for (int i=0;i<4;i++) { int ch = w*64 + lane + 256*i; st16(rw_, WS_Z2 + gid*16384 + ch*16, ldsr<u32x4>(C_OUT + ((ch ^ ((ch >> 4) & 15))*16))); }
    }
    TS(4);
    bf16x8 C16, S16, C2[2], S2[2];
    float tc[16], ts_[16];
    auto row_consts = [&]() {
      float f = (float)l31 * (1.0f/32); trig_frag(f, f*(float)(4*h), C16, S16);
      float cv[8], sv[8];
      #pragma unroll
      for (int j=0;j<8;j++) { float r = fmaf(f, (float)(4*(j>>1) + (j&1)), f*(float)(2*h)); cv[j] = cosr(r); sv[j] = -sinr(r); }
      C2[0] = pack8(cv); S2[0] = pack8(sv);
      unsigned m = (l31 & 1) ? 0x80008000u : 0u; C2[1] = xor8(C2[0], m); S2[1] = xor8(S2[0], m);
      float f2 = (float)l31 * (1.0f/1024);
      #pragma unroll
      for (int e=0;e<16;e++) { int j = e & 7; float a = fmaf(f2, (float)(16*(e>>3) + 4*(j>>1) + (j&1)), f2*(float)(2*h)); tc[e] = cosr(a); ts_[e] = sinr(a); PIN(tc[e]); PIN(ts_[e]); }
      PIN(C16); PIN(S16); PIN(C2[0]); PIN(C2[1]); PIN(S2[0]); PIN(S2[1]);
    };
    if (w < 2) {
      const i32x4 r2 = mkrsrc(w2, F_*D_*2);
      unsigned va[8], vb[8];
      #pragma unroll
      for (int i=0;i<8;i++) { int pc = w*8+i; va[i] = WS_H + (unsigned)(((tm*8 + (pc>>1))*112 + ks*56 + (pc&1))*1024 + lane*16);
        int r = pc*4 + (lane>>4); int c = (lane&15) ^ ((r&7)<<1); vb[i] = (unsigned)((ks*1792 + r)*D_*2 + tn2*256 + c*16); }
      auto issueA = [&](int k, int slot) {
        #pragma unroll
        for (int i=0;i<8;i++) dma16s(rz, va[i], k*2048, G2_A + slot*16384 + w*8192 + i*1024); };
      auto issueB = [&](int k, int slot) {
        #pragma unroll
        for (int i=0;i<8;i++) dma16(r2, vb[i], k*64*D_*2, G2_B + slot*16384 + w*8192 + i*1024); };
      lsync(L_FL, w, lane, 3u, 15u);
      __builtin_amdgcn_s_setprio(2);
      issueB(0,0); issueB(1,1); issueB(2,2); issueB(3,3);
      poll1(rw_, WS_F3 + (unsigned)((tm*16 + ks*8)*32 + l31*8), true, tag);
      TS(5);
      issueA(0,0); issueA(1,1); issueA(2,2); issueA(3,3);
      WAITV(24); BAR();
      WAITV(16); BAR(); issueB(4,0); issueA(4,0);
      WAITV(24); BAR(); issueB(5,1); issueA(5,1);
      #pragma unroll 1
      for (int k = 2; k < 24; k++) { WAITV(32); BAR(); issueB(k+4, k & 3); issueA(k+4, k & 3); }
      __builtin_amdgcn_s_setprio(0);
      WAITV(32); BAR();
      row_consts();
      WAITV(16); BAR();
      WAITV(0); BAR();
    } else {
      const int a = w - 2;
      lsync(L_FL, w, lane, 3u, 0u);
      row_consts();
      WAITV(0); BAR();
      if (a == 0 && lane == 0) st8(rw_, WS_F2 + gid*8, tag);
      #pragma unroll 1
      for (int k = 0; k < 16; k++) BAR();
      poll256(rw_, WS_F2, lane, tag);
      #pragma unroll 1
      for (int k = 0; k < 9; k++) BAR();
      #pragma unroll
      for (int i=0;i<8;i++) { int pc = a*8 + i; dma16s(rz, (unsigned)((lane >> 2)*16384 + (((lane & 3) ^ (pc & 3))*16)), WS_Z2 + pc*16*16384 + gid*64, E_Z + pc*1024); }
      BAR();
      BAR();
      WAITV(0);
    }
    BAR();
    TS(6);
    {
      f32x16 Qr[2], Qi[2];
      #pragma unroll
      for (int s=0;s<2;s++) {
        s16x4 v[2][2];
        #pragma unroll
        for (int t=0;t<2;t++) { int k2 = 8*t + 4*(gi>>1) + q; int cw = 16*k2 + 8*s + 4*(gi&1) + p;
          #pragma unroll
          for (int ri=0;ri<2;ri++) v[ri][t] = trrd(E_Z + cw*64 + ((w ^ (k2 & 3))*16) + ri*8); }
        bf16x8 zr = cat8(v[0][0], v[0][1]), zi = cat8(v[1][0], v[1][1]);
        Qr[s] = mfma32(zr, C16, zero16()); Qi[s] = mfma32(zi, C16, zero16());
        Qr[s] = mfma32(neg8(zi), S16, Qr[s]); Qi[s] = mfma32(zr, S16, Qi[s]);
      }
      { u32x2 z0 = ldsr<u32x2>(E_Z + w*16);
        float f = (h == 0 && (l31 & 1)) ? -2.f : 0.f;
        Qr[0][0] += f * bflo(z0[0]); Qr[0][2] += f * bflo(z0[1]); }
      #pragma unroll
      for (int row=0; row<2; row++) {
        f32x16 Y = zero16();
        #pragma unroll
        for (int s=0;s<2;s++) { float a_[8], b_[8];
          #pragma unroll
          for (int j=0;j<8;j++) { int r = 4*(j>>1) + 2*row + (j&1); float qr = Qr[s][r], qi = Qi[s][r]; a_[j] = qr*tc[8*s+j] - qi*ts_[8*s+j]; b_[j] = qi*tc[8*s+j] + qr*ts_[8*s+j]; }
          Y = mfma32(pack8(a_), C2[s], Y); Y = mfma32(pack8(b_), S2[s], Y); }
        #pragma unroll
        for (int ga=0; ga<4; ga++) ldsw<u32x2>(E_Y + (2*w + row)*2048 + l31*64 + ((ga ^ ((l31 >> 1) & 3))*16) + h*8, (u32x2){cvtpk(Y[4*ga], Y[4*ga+1]), cvtpk(Y[4*ga+2], Y[4*ga+3])});
      }
    }
    if (w == 3) poll1(rw_, WS_F4 + (unsigned)(tm*512 + lane*8), (lane & 1) == (tn >> 3), tag);
  }
  {
    const int t = threadIdx.x;
    u32x4 xv[2];
    #pragma unroll
    for (int i=0;i<2;i++) xv[i] = *(const u32x4*)((const char*)x + gid*16384 + (t + 512*i)*16);
    LBAR();
    TS(7);
    u32x4 p0[2], p1[2], yv[2];
    #pragma unroll
    for (int i=0;i<2;i++) { unsigned o = WS_PART + gid*16384 + (t + 512*i)*16; p0[i] = ld16(rw_, o); p1[i] = ld16(rw_, o + (4u<<20)); }
    #pragma unroll
    for (int i=0;i<2;i++) { int ch = t + 512*i; yv[i] = ldsr<u32x4>(E_Y + ((ch & ~3) | ((ch & 3) ^ ((ch >> 3) & 3)))*16); }
    #pragma unroll
    for (int i=0;i<2;i++) { u32x4 o;
      #pragma unroll
      for (int e=0;e<4;e++) { unsigned xe = xv[i][e], ye = yv[i][e], pe = p0[i][e], qe = p1[i][e];
        o[e] = cvtpk((bflo(xe) + bflo(ye)) + (bflo(pe) + bflo(qe)), (bfhi(xe) + bfhi(ye)) + (bfhi(pe) + bfhi(qe))); }
      *(u32x4*)((char*)out + gid*16384 + (t + 512*i)*16) = o; }
  }
}

extern "C" void kernel_launch(void* const* d_in, const int* in_sizes, int n_in, void* d_out, int out_size, void* d_ws, size_t ws_size, hipStream_t stream) {
  (void)hipFuncSetAttribute((const void*)k_all, hipFuncAttributeMaxDynamicSharedMemorySize, L_TOT);
  k_all<<<256,512,L_TOT,stream>>>((const u16*)d_in[0], (const u16*)d_in[1], (const u16*)d_in[2], (const u16*)d_in[4], (const u16*)d_in[5], (const u16*)d_in[6], (char*)d_ws, (u16*)d_out);
}
```

```cpp
#include <hip/hip_runtime.h>
#include <hip/hip_bf16.h>
#include <stdint.h>
typedef unsigned short u16;
typedef unsigned long long u64;
#define LAS __attribute__((address_space(3)))
typedef short s16x4 __attribute__((ext_vector_type(4)));
typedef __bf16 bf16x8 __attribute__((ext_vector_type(8)));
typedef __bf16 bf16x2 __attribute__((ext_vector_type(2)));
typedef float f32x2 __attribute__((ext_vector_type(2)));
typedef float f32x4 __attribute__((ext_vector_type(4)));
typedef float f32x16 __attribute__((ext_vector_type(16)));
typedef unsigned u32x4 __attribute__((ext_vector_type(4)));
typedef unsigned u32x2 __attribute__((ext_vector_type(2)));
typedef int i32x4 __attribute__((ext_vector_type(4)));
#define DI __device__ __forceinline__

#define S_ 2048
#define D_ 1024
#define F_ 3584
#define DF_ 513

constexpr unsigned WS_H    = 0;
constexpr unsigned WS_ZW   = 16u<<20;
constexpr unsigned WS_Z2   = 24u<<20;
constexpr unsigned WS_PART = 28u<<20;
constexpr unsigned WS_CS   = 36u<<20;
constexpr unsigned WS_STAT = 37u<<20;
constexpr unsigned WS_FLAG = 38u<<20;
constexpr unsigned WS_E = WS_FLAG, WS_F1 = WS_FLAG + 4096, WS_F2 = WS_FLAG + 8192, WS_F3 = WS_FLAG + 12288, WS_F4 = WS_FLAG + 20480;
constexpr unsigned WS_USED = 40u<<20;

constexpr int A_SLOT = 16384, B_SLOT = 14336;
constexpr int G1_A = 0, G1_B = 3*A_SLOT, L_EX = G1_B + 6*B_SLOT;
constexpr int L_MISC = L_EX + 18432;
constexpr int L_BN = L_MISC, L_CN = L_MISC + 1024, L_ST = L_MISC + 2048, L_RS = L_MISC + 3072, L_FL = L_MISC + 3200, L_ODD = L_MISC + 4096;
constexpr int L_TOT = 163840;
constexpr int C_IN0 = L_EX, C_IN1 = 16384, C_EXC = 32768, C_OUT = 0;
constexpr int G2_A = 0, G2_B = 65536, E_Z = 131072, E_Y = 65536;

#define TS(slot)

DI void dma16(i32x4 rsrc, unsigned voff, unsigned soff, unsigned ldsdst) {
  asm volatile("s_mov_b32 m0, %3\n\ts_nop 0\n\tbuffer_load_dwordx4 %0, %1, %2 offen lds" :: "v"(voff), "s"(rsrc), "s"(soff), "s"(ldsdst) : "memory");
}
DI void dma16s(i32x4 rsrc, unsigned voff, unsigned soff, unsigned ldsdst) {
  asm volatile("s_mov_b32 m0, %3\n\ts_nop 0\n\tbuffer_load_dwordx4 %0, %1, %2 offen sc1 lds" :: "v"(voff), "s"(rsrc), "s"(soff), "s"(ldsdst) : "memory");
}
DI void touch(i32x4 rsrc, unsigned voff, unsigned soff, unsigned ldsdst) {
  asm volatile("s_mov_b32 m0, %3\n\ts_nop 0\n\tbuffer_load_dword %0, %1, %2 offen lds" :: "v"(voff), "s"(rsrc), "s"(soff), "s"(ldsdst) : "memory");
}
DI i32x4 mkrsrc(const void* p, unsigned bytes) {
  u64 a = (u64)p; i32x4 r; r[0] = __builtin_amdgcn_readfirstlane((unsigned)a); r[1] = __builtin_amdgcn_readfirstlane((unsigned)(a>>32)) & 0xffff; r[2] = bytes; r[3] = 0x00020000; return r;
}
typedef __amdgpu_buffer_rsrc_t rsrc_t;
DI void st16(rsrc_t r, unsigned off, u32x4 v) { __builtin_amdgcn_raw_buffer_store_b128(v, r, off, 0, 16); }
DI void st8(rsrc_t r, unsigned off, u32x2 v) { __builtin_amdgcn_raw_buffer_store_b64(v, r, off, 0, 16); }
DI u32x4 ld16(rsrc_t r, unsigned off) { return __builtin_amdgcn_raw_buffer_load_b128(r, off, 0, 16); }
DI u32x2 ld8(rsrc_t r, unsigned off) { return __builtin_amdgcn_raw_buffer_load_b64(r, off, 0, 16); }
DI unsigned cvtpk(float lo, float hi) { f32x2 v = {lo, hi}; return __builtin_bit_cast(unsigned, __builtin_convertvector(v, bf16x2)); }
DI float bflo(unsigned u) { return __uint_as_float(u << 16); }
DI float bfhi(unsigned u) { return __uint_as_float(u & 0xffff0000u); }
#define WAITV(n) asm volatile("s_waitcnt vmcnt(%0)" :: "n"(n) : "memory")
#define BAR() do { asm volatile("" ::: "memory"); __builtin_amdgcn_s_barrier(); asm volatile("" ::: "memory"); } while(0)
#define SB() __builtin_amdgcn_sched_barrier(0)
#define WAITL0() __builtin_amdgcn_s_waitcnt(0xC07F)
#define LBAR() do { WAITL0(); BAR(); } while(0)
#define PIN(x) asm volatile("" : "+v"(x))
DI s16x4 trrd(unsigned a) { return __builtin_amdgcn_ds_read_tr16_b64_v4i16((LAS s16x4*)(size_t)a); }
DI bf16x8 cat8(s16x4 lo, s16x4 hi) { return __builtin_bit_cast(bf16x8, __builtin_shufflevector(lo, hi, 0,1,2,3,4,5,6,7)); }
DI f32x16 mfma32(bf16x8 a, bf16x8 b, f32x16 c) { return __builtin_amdgcn_mfma_f32_32x32x16_bf16(a, b, c, 0,0,0); }
DI float cosr(float r) { return __builtin_amdgcn_cosf(r); }
DI float sinr(float r) { return __builtin_amdgcn_sinf(r); }
DI int crow(int r, int h) { return (r & 3) + 8*(r >> 2) + 4*h; }
DI bf16x8 pack8(const float* v) { u32x4 p = { cvtpk(v[0],v[1]), cvtpk(v[2],v[3]), cvtpk(v[4],v[5]), cvtpk(v[6],v[7]) }; return __builtin_bit_cast(bf16x8, p); }
DI f32x16 zero16() { f32x16 z;
  #pragma unroll
  for (int i=0;i<16;i++) z[i]=0.f; return z; }
DI bf16x8 xor8(bf16x8 v, unsigned m) { u32x4 p = __builtin_bit_cast(u32x4, v); p ^= m; return __builtin_bit_cast(bf16x8, p); }
DI bf16x8 neg8(bf16x8 v) { return xor8(v, 0x80008000u); }
template<typename T> DI T ldsr(unsigned a) { return *(LAS T*)(size_t)a; }
template<typename T> DI void ldsw(unsigned a, T v) { *(LAS T*)(size_t)a = v; }

template<int CTRL> DI float dppadd(float v) { return v + __builtin_bit_cast(float, __builtin_amdgcn_update_dpp(0, __builtin_bit_cast(int, v), CTRL, 0xf, 0xf, true)); }
DI float wave_sum(float v) {
  v = dppadd<0xB1>(v); v = dppadd<0x4E>(v); v = dppadd<0x141>(v); v = dppadd<0x140>(v);
  { auto r = __builtin_amdgcn_permlane16_swap(__float_as_uint(v), __float_as_uint(v), false, false); v = __uint_as_float(r[0]) + __uint_as_float(r[1]); }
  { auto r = __builtin_amdgcn_permlane32_swap(__float_as_uint(v), __float_as_uint(v), false, false); v = __uint_as_float(r[0]) + __uint_as_float(r[1]); }
  return v;
}
DI void trig_frag(float step, float base, bf16x8& c, bf16x8& s) {
  float cv[8], sv[8];
  #pragma unroll
  for (int j=0;j<8;j++) { float r = fmaf(step, (float)(8*(j>>2) + (j&3)), base); cv[j] = cosr(r); sv[j] = sinr(r); }
  c = pack8(cv); s = pack8(sv);
}
DI unsigned mish2(f32x2 u) {
  f32x2 t; t.x = __builtin_amdgcn_exp2f(u.x); t.y = __builtin_amdgcn_exp2f(u.y);
  f32x2 a = t + 1.0f; f32x2 d = a*a + 1.0f;
  f32x2 r; r.x = __builtin_amdgcn_rcpf(d.x); r.y = __builtin_amdgcn_rcpf(d.y);
  f32x2 f = r * (-1.3862943611198906f) + 0.6931471805599453f;
  f32x2 o = u * f;
  return cvtpk(o.x, o.y);
}
constexpr int SPIN_MAX = 1 << 17;
DI void poll1(rsrc_t r, unsigned off, bool act, u32x2 tag) {
  for (int it = 0; it < SPIN_MAX; it++) {
    u32x2 v = ld8(r, off);
    if (__builtin_amdgcn_ballot_w64(act && (v[0] != tag[0] || v[1] != tag[1])) == 0) break;
    __builtin_amdgcn_s_sleep(2);
  }
  asm volatile("" ::: "memory");
}
DI void poll256(rsrc_t r, unsigned base, int lane, u32x2 tag) {
  for (int it = 0; it < SPIN_MAX; it++) {
    u32x4 a = ld16(r, base + lane*16), b = ld16(r, base + 1024 + lane*16);
    bool bad = a[0] != tag[0] || a[1] != tag[1] || a[2] != tag[0] || a[3] != tag[1] || b[0] != tag[0] || b[1] != tag[1] || b[2] != tag[0] || b[3] != tag[1];
    if (__builtin_amdgcn_ballot_w64(bad) == 0) break;
    __builtin_amdgcn_s_sleep(4);
  }
  asm volatile("" ::: "memory");
}
DI void lsync(unsigned flbase, int me, int lane, unsigned phase, unsigned mask) {
  WAITL0(); asm volatile("" ::: "memory");
  if (lane == 0) *(volatile LAS unsigned*)(size_t)(flbase + me*4) = phase;
  for (int it = 0; it < (1 << 22); it++) {
    unsigned v = *(volatile LAS unsigned*)(size_t)(flbase + (lane & 3)*4);
    if (__builtin_amdgcn_ballot_w64(((mask >> (lane & 3)) & 1) && v < phase) == 0) break;
  }
  asm volatile("" ::: "memory");
}

__global__ __launch_bounds__(512, 2) void k_all(const u16* __restrict__ x, const u16* __restrict__ wr, const u16* __restrict__ wi,
    const u16* __restrict__ w1, const u16* __restrict__ b1, const u16* __restrict__ w2, char* __restrict__ ws, u16* __restrict__ out) {
  extern __shared__ __attribute__((aligned(16))) char smem[];
  const int wid = __builtin_amdgcn_readfirstlane(threadIdx.x >> 6), lane = threadIdx.x & 63;
  const int b = blockIdx.x, xcd = b & 7, idx = b >> 3;
  const int tm = (xcd >> 1)*4 + (idx & 3), ks = xcd & 1, tn2 = idx >> 2, tn = ks*8 + tn2;
  const int gid = tm*16 + tn;
  const int gi = lane >> 4, q = (lane & 15) >> 2, p = lane & 3, h = lane >> 5, l31 = lane & 31;
  TS(0);
  u64 ep;
  asm volatile("s_load_dwordx2 %0, %1, 0x0" : "=s"(ep) : "s"(ws + WS_E) : "memory");
  const rsrc_t rw_ = __builtin_amdgcn_make_buffer_rsrc(ws, 0, WS_USED, 0x00020000);
  const i32x4 rx = mkrsrc(x, S_*D_*2);
  u32x2 tag;
  #define GETTAG() do { asm volatile("s_waitcnt lgkmcnt(0)" : "+s"(ep) :: "memory"); u64 t_ = ep * 0x9E3779B97F4A7C15ull + 0x632BE59BD9B4E019ull; tag = (u32x2){(unsigned)t_, (unsigned)(t_ >> 32)}; } while (0)

  if (wid < 4) {
    #pragma unroll
    for (int i=0;i<4;i++) dma16(rx, lane*16, gid*16384 + (wid*4+i)*1024, L_EX + (wid*4+i)*1024);
    {
      bf16x8 Cp[2], Sp[2], M1[2], M2[2];
      { float f = (float)l31 * (1.0f/32); trig_frag(f, f*(float)(4*h), Cp[0], Sp[0]); Sp[0] = neg8(Sp[0]);
        unsigned m = (l31 & 1) ? 0x80008000u : 0u; Cp[1] = xor8(Cp[0], m); Sp[1] = xor8(Sp[0], m);
        float f2 = (float)(l31 & 15) * (1.0f/32); trig_frag(f2, f2*(float)(4*h) + ((l31 >= 16) ? 0.25f : 0.f), M1[0], M2[0]);
        M1[1] = xor8(M1[0], m); M2[1] = xor8(M2[0], m); }
      float twc[16], tws[16];
      { float f = (float)l31 * (1.0f/1024), bs = f*(float)(4*h);
        #pragma unroll
        for (int r=0;r<16;r++) { float a = fmaf(f, (float)crow(r,0), bs); twc[r] = cosr(a); tws[r] = sinr(a); PIN(twc[r]); PIN(tws[r]); } }
      GETTAG();
      WAITV(0);
      TS(1);
      bf16x8 xf[2][2];
      #pragma unroll
      for (int rr=0; rr<2; rr++)
        #pragma unroll
        for (int s=0;s<2;s++) { unsigned base = L_EX + (wid*2+rr)*2048 + (16*s + 4*(gi>>1) + q)*64 + (gi&1)*32 + 8*p; xf[rr][s] = cat8(trrd(base), trrd(base + 512)); }
      f32x16 X[2];
      f32x2 stat[2];
      #pragma unroll
      for (int rr=0; rr<2; rr++) {
        f32x16 Pr = zero16(), Pi = zero16();
        #pragma unroll
        for (int s=0;s<2;s++) { Pr = mfma32(xf[rr][s], Cp[s], Pr); Pi = mfma32(xf[rr][s], Sp[s], Pi); }
        float s1 = 0.f, s2 = 0.f;
        const bf16x2 one2 = __builtin_bit_cast(bf16x2, 0x3f803f80u);
        #pragma unroll
        for (int s=0;s<2;s++) { u32x4 u = __builtin_bit_cast(u32x4, xf[rr][s]);
          #pragma unroll
          for (int e=0;e<4;e++) { unsigned ue = u[e]; bf16x2 v = __builtin_bit_cast(bf16x2, ue); s1 = __builtin_amdgcn_fdot2_f32_bf16(v, one2, s1, false); s2 = __builtin_amdgcn_fdot2_f32_bf16(v, v, s2, false); } }
        float ny = wave_sum((lane & 1) ? -s1 : s1);
        s1 = wave_sum(s1); s2 = wave_sum(s2);
        float mean = s1 * (1.0f/D_), var = fmaxf(s2 * (1.0f/D_) - mean*mean, 0.f);
        float rstd = __builtin_amdgcn_rsqf(var + 1e-5f);
        stat[rr] = (f32x2){rstd * 1.4426950408889634f, mean * rstd * 1.4426950408889634f};
        float sub = (l31 == 0) ? 32.f*mean : 0.f;
        float tr_[16], ti_[16];
        #pragma unroll
        for (int r=0;r<16;r++) { float a = Pr[r] - sub, bb = Pi[r]; tr_[r] = a*twc[r] + bb*tws[r]; ti_[r] = bb*twc[r] - a*tws[r]; }
        f32x16 Y = zero16();
        #pragma unroll
        for (int s=0;s<2;s++) { Y = mfma32(M1[s], pack8(tr_ + 8*s), Y); Y = mfma32(M2[s], pack8(ti_ + 8*s), Y); }
        if (lane == 0) { Y[0] = ny; Y[8] = 0.f; }
        X[rr] = Y * rstd;
      }
      asm volatile("" ::: "memory");
      #pragma unroll
      for (int r=0;r<8;r++) ldsw<u32x2>(L_EX + wid*4096 + (l31 + 32*crow(r,h))*8, (u32x2){cvtpk(X[0][r], X[1][r]), cvtpk(X[0][r+8], X[1][r+8])});
      if (lane == 0) ldsw<f32x4>(L_RS + wid*16, (f32x4){stat[0].x, stat[0].y, stat[1].x, stat[1].y});
    }
    const int wm = wid & 1, wn = wid >> 1;
    const int g = gi, c = lane & 15;
    {
      f32x4 acc[7][4];
      #pragma unroll
      for (int i=0;i<7;i++)
        #pragma unroll
        for (int j=0;j<4;j++) { acc[i][j] = (f32x4){0,0,0,0}; PIN(acc[i][j]); }
      unsigned xak[2];
      xak[0] = G1_A + ((wm*64 + c)>>3)*1024 + (c&7)*128 + ((g ^ (c&7))*16);
      xak[1] = G1_A + ((wm*64 + c)>>3)*1024 + (c&7)*128 + (((4+g) ^ (c&7))*16);
      const unsigned wbase = G1_B + (8*g + q)*448 + 8*p;
      const unsigned sgn = (g&1)*32;
      unsigned wb[2][3];
      wb[0][0] = wbase + wn*256 + sgn;
      wb[0][1] = wbase + wn*256 - sgn;
      wb[0][2] = wn ? wbase + 32 - sgn : wbase + sgn;
      #pragma unroll
      for (int i=0;i<3;i++) { wb[1][i] = wb[0][i] + 3*B_SLOT; PIN(wb[0][i]); PIN(wb[1][i]); }
      PIN(xak[0]); PIN(xak[1]);
      bf16x8 xf[2][4], wf[2][7];
      s16x4 lo_[7];
      auto rd = [&](int set, int r, int n) {
        if (n >= 2 && n < 6) { xf[set][n-2] = ldsr<bf16x8>(xak[r&1] + (r>>1)*A_SLOT + (n-2)*2048); return; }
        int i = n < 2 ? 0 : (n-6)/2+1, t = n < 2 ? n : (n-6)&1;
        s16x4 v = trrd(wb[r/3][i == 6 ? 2 : (i&1)] + (r%3)*B_SLOT + i*32 + t*1792);
        if (t==0) lo_[i] = v; else wf[set][i] = cat8(lo_[i], v);
      };
      auto mm = [&](int set, int n) { int i = n>>2, j = (i&1) ? 3-(n&3) : (n&3);
        asm volatile("v_mfma_f32_16x16x32_bf16 %0, %1, %2, %0" : "+v"(acc[i][j]) : "v"(wf[set][i]), "v"(xf[set][j])); };
      LBAR();
      TS(2);
      #pragma unroll
      for (int n=0;n<18;n++) rd(0, 0, n);
      WAITL0();
      auto step = [&](int set, int r1, bool bar) {
        SB();
        #pragma unroll
        for (int n=0;n<28;n++) { mm(set, n); SB(); if (n < 18) { rd(set^1, r1, n); SB(); }
          if (n == 25) { WAITL0(); SB(); }
          if (n == 26 && bar) { BAR(); SB(); } }
      };
      #pragma unroll 1
      for (int s = 0; s < 30; s += 6) { step(0, 1, true); step(1, 2, false); step(0, 3, true); step(1, 4, false); step(0, 5, true); step(1, 0, false); }
      step(0, 1, true);
      f32x4 bl[7], cl[7]; f32x2 st[4];
      #pragma unroll
      for (int n=0;n<28;n++) { mm(1, n); SB();
        if (n < 7) bl[n] = ldsr<f32x4>(L_BN + ((n == 6 ? wn*16 : wn*128) + n*16 + 4*g)*4);
        else if (n < 14) cl[n-7] = ldsr<f32x4>(L_CN + ((n == 13 ? wn*16 : wn*128) + (n-7)*16 + 4*g)*4);
        else if (n < 18) st[n-14] = ldsr<f32x2>(L_ST + (wm*64 + (n-14)*16 + c)*8);
        SB(); }
      asm volatile("s_nop 7\n\ts_nop 7" ::: "memory");
      TS(3);
      SB();
      u32x2 od[4];
      {
        #pragma unroll
        for (int j=0;j<4;j++) { f32x4 v = acc[6][j] * st[j].x + (bl[6] - cl[6] * st[j].y); od[j] = (u32x2){mish2((f32x2){v[0], v[1]}), mish2((f32x2){v[2], v[3]})}; }
        u32x4 snd = wn ? (u32x4){od[0][0], od[0][1], od[1][0], od[1][1]} : (u32x4){od[2][0], od[2][1], od[3][0], od[3][1]};
        ldsw<u32x4>(L_ODD + wid*1024 + lane*16, snd);
        if (lane == 0) *(volatile LAS unsigned*)(size_t)(L_FL + 16 + wid*4) = 1u; }
      const unsigned hb0 = WS_H + (unsigned)(((tm*8 + wm*4)*112 + tn*7)*1024 + lane*16);
      #pragma unroll
      for (int j=0;j<4;j++) {
        #pragma unroll
        for (int ip=0; ip<3; ip++) {
          f32x4 v0 = acc[2*ip][j] * st[j].x + (bl[2*ip] - cl[2*ip] * st[j].y), v1 = acc[2*ip+1][j] * st[j].x + (bl[2*ip+1] - cl[2*ip+1] * st[j].y);
          u32x4 o = { mish2((f32x2){v0[0], v0[1]}), mish2((f32x2){v0[2], v0[3]}), mish2((f32x2){v1[0], v1[1]}), mish2((f32x2){v1[2], v1[3]}) };
          st16(rw_, hb0 + j*112*1024 + (wn*4 + ip)*1024, o);
        }
        if (j == 1) {
          for (int it = 0; it < (1 << 22); it++) { if (*(volatile LAS unsigned*)(size_t)(L_FL + 16 + (wid ^ 2)*4) != 0u) break; }
          asm volatile("" ::: "memory");
          u32x4 rcv = ldsr<u32x4>(L_ODD + (wid ^ 2)*1024 + lane*16);
          u32x4 o0 = wn ? (u32x4){rcv[0], rcv[1], od[2][0], od[2][1]} : (u32x4){od[0][0], od[0][1], rcv[0], rcv[1]};
          u32x4 o1 = wn ? (u32x4){rcv[2], rcv[3], od[3][0], od[3][1]} : (u32x4){od[1][0], od[1][1], rcv[2], rcv[3]};
          st16(rw_, hb0 + (2*wn)*112*1024 + 3*1024, o0);
          st16(rw_, hb0 + (2*wn+1)*112*1024 + 3*1024, o1);
        }
      }
      TS(4);
      WAITV(0);
      if (lane == 0) st8(rw_, WS_F3 + (gid*4 + wid)*8, tag);
    }
    {
      f32x4 acc[4][4];
      #pragma unroll
      for (int i=0;i<4;i++)
        #pragma unroll
        for (int j=0;j<4;j++) { acc[i][j] = (f32x4){0,0,0,0}; PIN(acc[i][j]); }
      unsigned xa = G2_A + wm*8192 + lane*16;
      unsigned wb[4];
      { int rho = 4*(g&1) + q;
        #pragma unroll
        for (int i=0;i<4;i++) wb[i] = G2_B + (4*g + q)*256 + (((wn*4 + i) ^ rho)*32) + 8*p; }
      asm volatile("" : "+v"(xa), "+v"(wb[0]), "+v"(wb[1]), "+v"(wb[2]), "+v"(wb[3]));
      bf16x8 xf[2][2][4], wf[2][2][4];
      s16x4 lo_;
      auto rd = [&](int set, int r, int n) {
        int sub = n / 12, m = n % 12;
        if (m >= 2 && m < 6) { xf[set][sub][m-2] = ldsr<bf16x8>(xa + r*16384 + (m-2)*2048 + sub*1024); return; }
        int i = m < 2 ? 0 : (m-6)/2+1, t = m < 2 ? m : (m-6)&1;
        s16x4 v = trrd(wb[i] + r*16384 + sub*8192 + t*4096);
        if (t==0) lo_ = v; else wf[set][sub][i] = cat8(lo_, v);
      };
      auto mm = [&](int set, int n) { int sub = n & 1, m = n >> 1; int i = m>>2, j = (i&1) ? 3-(m&3) : (m&3);
        asm volatile("v_mfma_f32_16x16x32_bf16 %0, %1, %2, %0" : "+v"(acc[i][j]) : "v"(wf[set][sub][i]), "v"(xf[set][sub][j])); };
      BAR();
      TS(5);
      #pragma unroll
      for (int n=0;n<24;n++) rd(0, 0, n);
      WAITL0(); BAR();
      auto step = [&](int set, int r1, bool bar) {
        SB();
        #pragma unroll
        for (int n=0;n<32;n++) { mm(set, n); SB(); if (n < 24) { rd(set^1, r1, n); SB(); }
          if (n == 29) { WAITL0(); SB(); }
          if (n == 30 && bar) { BAR(); SB(); } }
      };
      #pragma unroll 1
      for (int s = 0; s < 24; s += 4) { step(0, 1, true); step(1, 2, true); step(0, 3, true); step(1, 0, true); }
      step(0, 1, true); step(1, 2, true); step(0, 3, false);
      #pragma unroll
      for (int n=0;n<32;n++) { mm(1, n); SB(); if (n == 27) { BAR(); SB(); } }
      asm volatile("s_nop 7\n\ts_nop 7" ::: "memory");
      TS(6);
      SB();
      const unsigned pb = G2_A + wid*8192;
      #pragma unroll
      for (int j=0;j<4;j++)
        #pragma unroll
        for (int i=0;i<4;i++) ldsw<u32x2>(pb + (j*16 + c)*128 + (((i*2 + (g>>1)) ^ (c & 7))*16) + (g&1)*8, (u32x2){cvtpk(acc[i][j][0], acc[i][j][1]), cvtpk(acc[i][j][2], acc[i][j][3])});
      asm volatile("" ::: "memory");
      #pragma unroll
      for (int i=0;i<8;i++) { int r = i*8 + (lane >> 3); u32x4 v = ldsr<u32x4>(pb + r*128 + (((lane & 7) ^ (r & 7))*16));
        st16(rw_, WS_PART + ks*(4u<<20) + (unsigned)((tm*128 + wm*64 + r)*2048 + tn2*256 + wn*128 + (lane & 7)*16), v); }
      WAITV(0);
      if (lane == 0) st8(rw_, WS_F4 + (gid*4 + wid)*8, tag);
    }
  } else {
    const int w = wid - 4;
    const i32x4 rb = mkrsrc(w1, D_*F_*2), rz = mkrsrc(ws, WS_USED);
    { int r = w*16 + (lane >> 2); unsigned o = (unsigned)(tn*448 + (lane & 3)*128); if ((lane & 3) == 3) o = tn*448 + 384;
      touch(rb, (unsigned)(r*F_*2) + o, tm*64*F_*2, L_ODD + w*256); }
    const int col = w >> 1, hf = w & 1;
    bf16x8 C64[4], S64[4], CA[2], SA[2], CB[2], SB_[2];
    float twc[16], tws[16];
    auto col_consts = [&]() {
      const int u = l31 + 32*hf;
      { float f = (float)u * (1.0f/64); trig_frag(f, f*(float)(4*h), C64[0], S64[0]); trig_frag(f, f*(float)(16 + 4*h), C64[1], S64[1]);
        unsigned m = (u & 1) ? 0x80008000u : 0u; C64[2] = xor8(C64[0], m); S64[2] = xor8(S64[0], m); C64[3] = xor8(C64[1], m); S64[3] = xor8(S64[1], m); }
      { float f = (float)(2*l31 + hf) * (1.0f/64); trig_frag(f, f*(float)(4*h), CA[0], SA[0]); trig_frag(f, f*(float)(16 + 4*h), CA[1], SA[1]); }
      { float f = (float)l31 * (1.0f/32); trig_frag(f, f*((float)(4*h) + 0.5f*(float)hf), CB[0], SB_[0]);
        unsigned m = (l31 & 1) ? 0x80008000u : 0u; CB[1] = xor8(CB[0], m); SB_[1] = xor8(SB_[0], m); }
      { float f = (float)l31 * (1.0f/2048), bs = f*(float)(4*h);
        #pragma unroll
        for (int r=0;r<16;r++) { float a = fmaf(f, (float)crow(r,0), bs); twc[r] = cosr(a); tws[r] = sinr(a); PIN(twc[r]); PIN(tws[r]); } }
      #pragma unroll
      for (int s=0;s<4;s++) { PIN(C64[s]); PIN(S64[s]); }
      #pragma unroll
      for (int s=0;s<2;s++) { PIN(CA[s]); PIN(SA[s]); PIN(CB[s]); PIN(SB_[s]); }
    };
    if (w < 2) {
      __builtin_amdgcn_s_setprio(2);
      unsigned va[8], vb[14];
      #pragma unroll
      for (int i=0;i<8;i++) { int pc = w*8+i; int r = pc*8 + (lane>>3); va[i] = (unsigned)((tm*128 + r)*D_*2 + (((lane&7) ^ (r&7))*16)); }
      #pragma unroll
      for (int i=0;i<14;i++) { int pc = w + 2*i; int ci = pc*64 + lane; int r = (ci*9363)>>18, c = ci - r*28; c ^= ((r>>3)&1)<<1; vb[i] = (unsigned)(r*F_*2 + tn*448 + c*16); }
      auto issueA = [&](int k, int slot) {
        #pragma unroll
        for (int i=0;i<8;i++) dma16(rx, va[i], k*128, G1_A + w*8192 + slot*A_SLOT + i*1024); };
      auto issueB = [&](int k, int slot) {
        #pragma unroll
        for (int i=0;i<14;i++) dma16(rb, vb[i], k*64*F_*2, G1_B + w*1024 + slot*2*B_SLOT + i*2048); };
      issueA(0,0); issueB(0,0); issueA(1,1); issueB(1,1); issueA(2,2);
      __builtin_amdgcn_s_setprio(0);
      col_consts();
      GETTAG();
      __builtin_amdgcn_s_setprio(2);
      WAITV(30); BAR();
      issueB(2,2);
      int slot = 0;
      #pragma unroll 1
      for (int k = 0; k < 13; k++) { WAITV(22); BAR(); issueA(k+3, slot); issueB(k+3, slot); slot = slot == 2 ? 0 : slot+1; }
      __builtin_amdgcn_s_setprio(0);
      WAITV(22); BAR();
      WAITV(0); BAR();
      BAR();
      { const i32x4 r2 = mkrsrc(w2, F_*D_*2);
        unsigned vo = (unsigned)((lane < 56 ? lane : 55)*128);
        touch(r2, vo, b*28672 + w*14336, L_MISC + 8192 + w*512); touch(r2, vo, b*28672 + w*14336 + 7168, L_MISC + 8192 + w*512 + 256); }
    } else {
      const int a = w - 2, t = a*64 + lane;
      const i32x4 rwt = mkrsrc(a ? wi : wr, S_*DF_*2);
      if (a == 0) {
        #pragma unroll 4
        for (int pc=0; pc<28; pc++) { int ci = pc*64 + lane; int r = (ci*9363)>>18, c = ci - r*28; c ^= ((r>>3)&1)<<1;
          dma16(rb, (unsigned)(r*F_*2 + tn*448 + c*16), tm*64*F_*2, G1_B + 4*B_SLOT + pc*1024); }
      } else { if (lane < 16) *(volatile LAS unsigned*)(size_t)(L_FL + lane*4) = 0u; }
      col_consts();
      GETTAG();
      if (a == 0) {
        WAITV(0);
        const u32x4 onesu = {0x3f803f80u,0x3f803f80u,0x3f803f80u,0x3f803f80u};
        const bf16x8 ones = __builtin_bit_cast(bf16x8, onesu);
        #pragma unroll 2
        for (int u=0; u<14; u++) {
          f32x4 s4 = {0,0,0,0};
          #pragma unroll
          for (int sub=0; sub<2; sub++) { unsigned base = G1_B + 4*B_SLOT + (32*sub + 8*gi + q)*448 + ((u ^ (gi&1))*32) + 8*p;
            s4 = __builtin_amdgcn_mfma_f32_16x16x32_bf16(cat8(trrd(base), trrd(base + 1792)), ones, s4, 0,0,0); }
          if ((lane & 15) == 0) st16(rw_, WS_CS + (unsigned)((tm*F_ + tn*224 + u*16 + 4*gi)*4), __builtin_bit_cast(u32x4, s4));
        }
      }
      touch(rwt, lane*128, gid*8208, L_ODD + 1024 + a*512); touch(rwt, lane*128, gid*8208 + 128, L_ODD + 1024 + a*512 + 256);
      LBAR();
      const unsigned zw = WS_ZW + gid*32768;
      #pragma unroll
      for (int i=0;i<8;i++) { int ch = t + 128*i; u32x4 v;
        #pragma unroll
        for (int e=0;e<4;e++) v[e] = ldsr<unsigned>(L_EX + e*4096 + ch*4);
        st16(rw_, zw + (ch >> 2)*128 + (ch & 3)*16, v); }
      if (t < 4) st16(rw_, WS_STAT + gid*64 + t*16, ldsr<u32x4>(L_RS + t*16));
      LBAR();
      #pragma unroll
      for (int i=0;i<9;i++) dma16(rwt, lane*16, gid*8208 + i*1024, L_EX + a*9216 + i*1024);
      BAR();
      WAITV(0); BAR();
      #pragma unroll 1
      for (int half=0; half<2; half++) {
        #pragma unroll
        for (int i=0;i<4;i++) { int id = t + 128*(i + 4*half); int k = id >> 1, hh = id & 1; if (k == 0) k = 512;
          u32x4 v;
          #pragma unroll
          for (int e=0;e<4;e++) { unsigned off = L_EX + ((hh*4+e)*DF_ + k)*2; v[e] = (unsigned)ldsr<u16>(off) | ((unsigned)ldsr<u16>(off + 9216) << 16); }
          st16(rw_, zw + (id >> 2)*128 + 64 + (id & 3)*16, v); }
        LBAR();
      }
      WAITV(0); BAR();
      if (t == 0) st8(rw_, WS_F1 + gid*8, tag);
      BAR(); BAR(); BAR(); BAR();
      poll256(rw_, WS_F1, lane, tag);
      if (b == 0 && t == 0) { u64 e1 = ep + 1; st8(rw_, WS_E, (u32x2){(unsigned)e1, (unsigned)(e1 >> 32)}); }
      unsigned gv = (unsigned)((lane >> 3)*32768);
      #pragma unroll
      for (int i=0;i<8;i++) { int pc = a*8 + i; dma16s(rz, gv + (((lane & 7) ^ ((pc*4 + (lane >> 4)) & 7))*16), WS_ZW + pc*8*32768 + gid*128, C_IN0 + pc*1024); }
      BAR();
      f32x2 csn = {0.f, 0.f}, bn = {0.f, 0.f}; u32x2 stv;
      { const int tt = t < 112 ? t : 111;
        #pragma unroll
        for (int i=0;i<16;i++) csn += __builtin_bit_cast(f32x2, ld8(rw_, WS_CS + (unsigned)((i*F_ + tn*224 + tt*2)*4)));
        unsigned bv = *(const unsigned*)(b1 + tn*224 + tt*2); bn = (f32x2){bflo(bv), bfhi(bv)} * 1.4426950408889634f;
        stv = ld8(rw_, WS_STAT + (unsigned)((tm*128 + t)*8)); }
      BAR();
      PIN(csn); PIN(bn); PIN(stv);
      BAR();
      BAR();
      #pragma unroll
      for (int i=0;i<8;i++) { int pc = 16 + a*8 + i; dma16s(rz, gv + (((lane & 7) ^ ((pc*4 + (lane >> 4)) & 7))*16), WS_ZW + pc*8*32768 + gid*128, C_IN1 + (pc-16)*1024); }
      WAITV(0); BAR();
      if (t < 112) { ldsw<f32x2>(L_BN + t*8, bn); ldsw<f32x2>(L_CN + t*8, csn); }
      ldsw<u32x2>(L_ST + t*8, stv);
      LBAR();
    }
    TS(3);
    {
      const int u = l31 + 32*hf;
      f32x16 Pr = zero16(), Pi = zero16();
      #pragma unroll
      for (int s=0;s<4;s++) {
        s16x4 v[2][2];
        #pragma unroll
        for (int t=0;t<2;t++) { int a = 16*(s&1) + 8*t + 4*(gi>>1) + q; int P = 4*(gi&1) + p; int rw = a*4 + (P >> 1);
          #pragma unroll
          for (int ri=0;ri<2;ri++) v[ri][t] = trrd(((s>>1) ? C_IN1 : C_IN0) + rw*128 + (((col*2 + ri) ^ ((rw >> 1) & 7))*16) + (P & 1)*8); }
        bf16x8 zr = cat8(v[0][0], v[0][1]), zi = cat8(v[1][0], v[1][1]);
        Pr = mfma32(zr, C64[s], Pr); Pi = mfma32(zi, C64[s], Pi);
        Pr = mfma32(zi, S64[s], Pr); Pi = mfma32(neg8(zr), S64[s], Pi);
      }
      unsigned wv[16];
      #pragma unroll
      for (int r=0;r<16;r++) { int j = u + 64*crow(r & 7, h); int rw = j >> 3; int ch = 4 + col*2 + ((j >> 2) & 1);
        wv[r] = ldsr<unsigned>(((r >> 3) ? C_IN1 : C_IN0) + rw*128 + ((ch ^ ((rw >> 1) & 7))*16) + (j & 3)*4); }
      float a_[16], b_[16];
      #pragma unroll
      for (int r=0;r<16;r++) { a_[r] = Pr[r]*twc[r] + Pi[r]*tws[r]; b_[r] = Pi[r]*twc[r] - Pr[r]*tws[r]; }
      f32x16 Ur = zero16(), Ui = zero16();
      #pragma unroll
      for (int s=0;s<2;s++) { bf16x8 tr_ = pack8(a_ + 8*s), ti_ = pack8(b_ + 8*s);
        Ur = mfma32(CA[s], tr_, Ur); Ui = mfma32(CA[s], ti_, Ui);
        Ur = mfma32(SA[s], ti_, Ur); Ui = mfma32(SA[s], neg8(tr_), Ui); }
      #pragma unroll
      for (int r=0;r<16;r++) { float wre = bflo(wv[r]), wim = bfhi(wv[r]); a_[r] = Ur[r]*wre - Ui[r]*wim; b_[r] = Ur[r]*wim + Ui[r]*wre; }
      f32x16 Qr = zero16(), Qi = zero16();
      #pragma unroll
      for (int s=0;s<2;s++) { bf16x8 yr = pack8(a_ + 8*s), yi = pack8(b_ + 8*s);
        Qr = mfma32(yr, CB[s], Qr); Qi = mfma32(yi, CB[s], Qi);
        Qr = mfma32(neg8(yi), SB_[s], Qr); Qi = mfma32(yr, SB_[s], Qi); }
      #pragma unroll
      for (int r=0;r<16;r++) { a_[r] = Qr[r]*twc[r] - Qi[r]*tws[r]; b_[r] = Qi[r]*twc[r] + Qr[r]*tws[r]; }
      bf16x8 Rr[4], Ri[4];
      { unsigned ex = C_EXC + w*4096 + lane*16;
        ldsw<bf16x8>(ex, pack8(a_)); ldsw<bf16x8>(ex+1024, pack8(a_+8)); ldsw<bf16x8>(ex+2048, pack8(b_)); ldsw<bf16x8>(ex+3072, pack8(b_+8));
        lsync(L_FL, w, lane, 1u, 3u << (col*2));
        #pragma unroll
        for (int s=0;s<4;s++) { unsigned e2 = C_EXC + (col*2 + (s>>1))*4096 + lane*16 + (s&1)*1024; Rr[s] = ldsr<bf16x8>(e2); Ri[s] = ldsr<bf16x8>(e2 + 2048); } }
      f32x16 Zr = zero16(), Zi = zero16();
      #pragma unroll
      for (int s=0;s<4;s++) { Zr = mfma32(Rr[s], C64[s], Zr); Zi = mfma32(Ri[s], C64[s], Zi);
        Zr = mfma32(neg8(Ri[s]), S64[s], Zr); Zi = mfma32(Rr[s], S64[s], Zi); }
      const float sc = ((gid == 0 && col == 0) ? 1.0f : 2.0f) / (float)(S_*D_);
      #pragma unroll
      for (int r=0;r<16;r++) { int rw = 4*u + (r >> 2), rp = 2*h + ((r & 3) >> 1), row = r & 1;
        unsigned pk = cvtpk(Zr[r]*sc, Zi[r]*sc);
        unsigned ad = C_OUT + (((rw*4 + rp) ^ (l31 & 15))*16) + row*4 + col*2;
        ldsw<u16>(ad, (u16)pk); ldsw<u16>(ad + 8, (u16)(pk >> 16)); }
      lsync(L_FL, w, lane, 2u, 15u);
      #pragma unroll
      for (int i=0;i<4;i++) { int ch = w*64 + lane + 256*i; st16(rw_, WS_Z2 + gid*16384 + ch*16, ldsr<u32x4>(C_OUT + ((ch ^ ((ch >> 4) & 15))*16))); }
    }
    TS(4);
    bf16x8 C16, S16, C2[2], S2[2];
    float tc[16], ts_[16];
    auto row_consts = [&]() {
      float f = (float)l31 * (1.0f/32); trig_frag(f, f*(float)(4*h), C16, S16);
      float cv[8], sv[8];
      #pragma unroll
      for (int j=0;j<8;j++) { float r = fmaf(f, (float)(4*(j>>1) + (j&1)), f*(float)(2*h)); cv[j] = cosr(r); sv[j] = -sinr(r); }
      C2[0] = pack8(cv); S2[0] = pack8(sv);
      unsigned m = (l31 & 1) ? 0x80008000u : 0u; C2[1] = xor8(C2[0], m); S2[1] = xor8(S2[0], m);
      float f2 = (float)l31 * (1.0f/1024);
      #pragma unroll
      for (int e=0;e<16;e++) { int j = e & 7; float a = fmaf(f2, (float)(16*(e>>3) + 4*(j>>1) + (j&1)), f2*(float)(2*h)); tc[e] = cosr(a); ts_[e] = sinr(a); PIN(tc[e]); PIN(ts_[e]); }
      PIN(C16); PIN(S16); PIN(C2[0]); PIN(C2[1]); PIN(S2[0]); PIN(S2[1]);
    };
    if (w < 2) {
      const i32x4 r2 = mkrsrc(w2, F_*D_*2);
      unsigned va[8], vb[8];
      #pragma unroll
      for (int i=0;i<8;i++) { int pc = w*8+i; va[i] = WS_H + (unsigned)(((tm*8 + (pc>>1))*112 + ks*56 + (pc&1))*1024 + lane*16);
        int r = pc*4 + (lane>>4); int c = (lane&15) ^ ((r&7)<<1); vb[i] = (unsigned)((ks*1792 + r)*D_*2 + tn2*256 + c*16); }
      auto issueA = [&](int k, int slot) {
        #pragma unroll
        for (int i=0;i<8;i++) dma16s(rz, va[i], k*2048, G2_A + slot*16384 + w*8192 + i*1024); };
      auto issueB = [&](int k, int slot) {
        #pragma unroll
        for (int i=0;i<8;i++) dma16(r2, vb[i], k*64*D_*2, G2_B + slot*16384 + w*8192 + i*1024); };
      lsync(L_FL, w, lane, 3u, 15u);
      __builtin_amdgcn_s_setprio(2);
      issueB(0,0); issueB(1,1); issueB(2,2); issueB(3,3);
      poll1(rw_, WS_F3 + (unsigned)((tm*16 + ks*8)*32 + l31*8), true, tag);
      TS(5);
      issueA(0,0); issueA(1,1); issueA(2,2); issueA(3,3);
      WAITV(24); BAR();
      WAITV(16); BAR(); issueB(4,0); issueA(4,0);
      WAITV(24); BAR(); issueB(5,1); issueA(5,1);
      #pragma unroll 1
      for (int k = 2; k < 24; k++) { WAITV(32); BAR(); issueB(k+4, k & 3); issueA(k+4, k & 3); }
      __builtin_amdgcn_s_setprio(0);
      WAITV(32); BAR();
      row_consts();
      WAITV(16); BAR();
      WAITV(0); BAR();
    } else {
      const int a = w - 2;
      lsync(L_FL, w, lane, 3u, 0u);
      row_consts();
      WAITV(0); BAR();
      if (a == 0 && lane == 0) st8(rw_, WS_F2 + gid*8, tag);
      #pragma unroll 1
      for (int k = 0; k < 16; k++) BAR();
      poll256(rw_, WS_F2, lane, tag);
      #pragma unroll 1
      for (int k = 0; k < 9; k++) BAR();
      #pragma unroll
      for (int i=0;i<8;i++) { int pc = a*8 + i; dma16s(rz, (unsigned)((lane >> 2)*16384 + (((lane & 3) ^ (pc & 3))*16)), WS_Z2 + pc*16*16384 + gid*64, E_Z + pc*1024); }
      BAR();
      BAR();
      WAITV(0);
    }
    BAR();
    TS(6);
    {
      f32x16 Qr[2], Qi[2];
      #pragma unroll
      for (int s=0;s<2;s++) {
        s16x4 v[2][2];
        #pragma unroll
        for (int t=0;t<2;t++) { int k2 = 8*t + 4*(gi>>1) + q; int cw = 16*k2 + 8*s + 4*(gi&1) + p;
          #pragma unroll
          for (int ri=0;ri<2;ri++) v[ri][t] = trrd(E_Z + cw*64 + ((w ^ (k2 & 3))*16) + ri*8); }
        bf16x8 zr = cat8(v[0][0], v[0][1]), zi = cat8(v[1][0], v[1][1]);
        Qr[s] = mfma32(zr, C16, zero16()); Qi[s] = mfma32(zi, C16, zero16());
        Qr[s] = mfma32(neg8(zi), S16, Qr[s]); Qi[s] = mfma32(zr, S16, Qi[s]);
      }
      { u32x2 z0 = ldsr<u32x2>(E_Z + w*16);
        float f = (h == 0 && (l31 & 1)) ? -2.f : 0.f;
        Qr[0][0] += f * bflo(z0[0]); Qr[0][2] += f * bflo(z0[1]); }
      #pragma unroll
      for (int row=0; row<2; row++) {
        f32x16 Y = zero16();
        #pragma unroll
        for (int s=0;s<2;s++) { float a_[8], b_[8];
          #pragma unroll
          for (int j=0;j<8;j++) { int r = 4*(j>>1) + 2*row + (j&1); float qr = Qr[s][r], qi = Qi[s][r]; a_[j] = qr*tc[8*s+j] - qi*ts_[8*s+j]; b_[j] = qi*tc[8*s+j] + qr*ts_[8*s+j]; }
          Y = mfma32(pack8(a_), C2[s], Y); Y = mfma32(pack8(b_), S2[s], Y); }
        #pragma unroll
        for (int ga=0; ga<4; ga++) ldsw<u32x2>(E_Y + (2*w + row)*2048 + l31*64 + ((ga ^ ((l31 >> 1) & 3))*16) + h*8, (u32x2){cvtpk(Y[4*ga], Y[4*ga+1]), cvtpk(Y[4*ga+2], Y[4*ga+3])});
      }
    }
    if (w == 3) poll1(rw_, WS_F4 + (unsigned)(tm*512 + lane*8), (lane & 1) == (tn >> 3), tag);
  }
  {
    const int t = threadIdx.x;
    u32x4 xv[2];
    #pragma unroll
    for (int i=0;i<2;i++) xv[i] = *(const u32x4*)((const char*)x + gid*16384 + (t + 512*i)*16);
    LBAR();
    TS(7);
    u32x4 p0[2], p1[2], yv[2];
    #pragma unroll
    for (int i=0;i<2;i++) { unsigned o = WS_PART + gid*16384 + (t + 512*i)*16; p0[i] = ld16(rw_, o); p1[i] = ld16(rw_, o + (4u<<20)); }
    #pragma unroll
    for (int i=0;i<2;i++) { int ch = t + 512*i; yv[i] = ldsr<u32x4>(E_Y + ((ch & ~3) | ((ch & 3) ^ ((ch >> 3) & 3)))*16); }
    #pragma unroll
    for (int i=0;i<2;i++) { u32x4 o;
      #pragma unroll
      for (int e=0;e<4;e++) { unsigned xe = xv[i][e], ye = yv[i][e], pe = p0[i][e], qe = p1[i][e];
        o[e] = cvtpk((bflo(xe) + bflo(ye)) + (bflo(pe) + bflo(qe)), (bfhi(xe) + bfhi(ye)) + (bfhi(pe) + bfhi(qe))); }
      *(u32x4*)((char*)out + gid*16384 + (t + 512*i)*16) = o; }
  }
}

__global__ __launch_bounds__(512) void k_probe(char* ws, int mode) {
  const u32x4* p = (const u32x4*)(ws + (64u<<20));
  size_t i = blockIdx.x*512 + threadIdx.x;
  if (mode == 1) { u32x4 v[8];
    #pragma unroll
    for (int u=0;u<8;u++) v[u] = p[i + (size_t)u*512*512];
    unsigned acc = 0;
    #pragma unroll
    for (int u=0;u<8;u++) acc ^= v[u][0]^v[u][3];
    if (acc == 0x12345678u && v[0][1] == 0x9abcdef0u && v[1][2] == 0x13572468u) *(unsigned*)(ws + (60u<<20)) = acc; }
  if (mode == 2) { u32x4* q = (u32x4*)(ws + (64u<<20));
    #pragma unroll
    for (int u=0;u<8;u++) q[i + (size_t)u*512*512] = (u32x4){1,2,3,(unsigned)i}; }
}
extern "C" void kernel_launch(void* const* d_in, const int* in_sizes, int n_in, void* d_out, int out_size, void* d_ws, size_t ws_size, hipStream_t stream) {
  (void)hipFuncSetAttribute((const void*)k_all, hipFuncAttributeMaxDynamicSharedMemorySize, L_TOT);
  k_probe<<<512,512,0,stream>>>((char*)d_ws, 0);
  k_all<<<256,512,L_TOT,stream>>>((const u16*)d_in[0], (const u16*)d_in[1], (const u16*)d_in[2], (const u16*)d_in[4], (const u16*)d_in[5], (const u16*)d_in[6], (char*)d_ws, (u16*)d_out);
}
```

```cpp
#define TSTAMP
#define PADSLOT 0
#include <hip/hip_runtime.h>
#include <hip/hip_bf16.h>
#include <stdint.h>
typedef unsigned short u16;
typedef unsigned long long u64;
#define LAS __attribute__((address_space(3)))
typedef short s16x4 __attribute__((ext_vector_type(4)));
typedef __bf16 bf16x8 __attribute__((ext_vector_type(8)));
typedef __bf16 bf16x2 __attribute__((ext_vector_type(2)));
typedef float f32x2 __attribute__((ext_vector_type(2)));
typedef float f32x4 __attribute__((ext_vector_type(4)));
typedef float f32x16 __attribute__((ext_vector_type(16)));
typedef unsigned u32x4 __attribute__((ext_vector_type(4)));
typedef unsigned u32x2 __attribute__((ext_vector_type(2)));
typedef int i32x4 __attribute__((ext_vector_type(4)));
#define DI __device__ __forceinline__

#define S_ 2048
#define D_ 1024
#define F_ 3584
#define DF_ 513

constexpr unsigned WS_H    = 0;
constexpr unsigned WS_ZW   = 16u<<20;
constexpr unsigned WS_Z2   = 24u<<20;
constexpr unsigned WS_PART = 28u<<20;
constexpr unsigned WS_CS   = 36u<<20;
constexpr unsigned WS_STAT = 37u<<20;
constexpr unsigned WS_FLAG = 38u<<20;
constexpr unsigned WS_E = WS_FLAG, WS_F1 = WS_FLAG + 4096, WS_F2 = WS_FLAG + 8192, WS_F3 = WS_FLAG + 12288, WS_F4 = WS_FLAG + 20480;
constexpr unsigned WS_USED = 40u<<20;

constexpr int A_SLOT = 16384, B_SLOT = 14336;
constexpr int G1_A = 0, G1_B = 3*A_SLOT, L_EX = G1_B + 6*B_SLOT;
constexpr int L_MISC = L_EX + 18432;
constexpr int L_BN = L_MISC, L_CN = L_MISC + 1024, L_ST = L_MISC + 2048, L_RS = L_MISC + 3072, L_FL = L_MISC + 3200, L_ODD = L_MISC + 4096;
constexpr int L_TOT = 163840;
constexpr int C_IN0 = L_EX, C_IN1 = 16384, C_EXC = 32768, C_OUT = 0;
constexpr int G2_A = 0, G2_B = 65536, E_Z = 131072, E_Y = 65536;

#define TS(slot) do { SB(); u64 t_; asm volatile("s_memrealtime %0\n\ts_waitcnt lgkmcnt(0)" : "=s"(t_) :: "memory"); tsv[slot] = t_; SB(); } while (0)

DI void dma16(i32x4 rsrc, unsigned voff, unsigned soff, unsigned ldsdst) {
  asm volatile("s_mov_b32 m0, %3\n\ts_nop 0\n\tbuffer_load_dwordx4 %0, %1, %2 offen lds" :: "v"(voff), "s"(rsrc), "s"(soff), "s"(ldsdst) : "memory");
}
DI void dma16s(i32x4 rsrc, unsigned voff, unsigned soff, unsigned ldsdst) {
  asm volatile("s_mov_b32 m0, %3\n\ts_nop 0\n\tbuffer_load_dwordx4 %0, %1, %2 offen sc1 lds" :: "v"(voff), "s"(rsrc), "s"(soff), "s"(ldsdst) : "memory");
}
DI void touch(i32x4 rsrc, unsigned voff, unsigned soff, unsigned ldsdst) {
  asm volatile("s_mov_b32 m0, %3\n\ts_nop 0\n\tbuffer_load_dword %0, %1, %2 offen lds" :: "v"(voff), "s"(rsrc), "s"(soff), "s"(ldsdst) : "memory");
}
DI i32x4 mkrsrc(const void* p, unsigned bytes) {
  u64 a = (u64)p; i32x4 r; r[0] = __builtin_amdgcn_readfirstlane((unsigned)a); r[1] = __builtin_amdgcn_readfirstlane((unsigned)(a>>32)) & 0xffff; r[2] = bytes; r[3] = 0x00020000; return r;
}
typedef __amdgpu_buffer_rsrc_t rsrc_t;
DI void st16(rsrc_t r, unsigned off, u32x4 v) { __builtin_amdgcn_raw_buffer_store_b128(v, r, off, 0, 16); }
DI void st8(rsrc_t r, unsigned off, u32x2 v) { __builtin_amdgcn_raw_buffer_store_b64(v, r, off, 0, 16); }
DI u32x4 ld16(rsrc_t r, unsigned off) { return __builtin_amdgcn_raw_buffer_load_b128(r, off, 0, 16); }
DI u32x2 ld8(rsrc_t r, unsigned off) { return __builtin_amdgcn_raw_buffer_load_b64(r, off, 0, 16); }
DI unsigned cvtpk(float lo, float hi) { f32x2 v = {lo, hi}; return __builtin_bit_cast(unsigned, __builtin_convertvector(v, bf16x2)); }
DI float bflo(unsigned u) { return __uint_as_float(u << 16); }
DI float bfhi(unsigned u) { return __uint_as_float(u & 0xffff0000u); }
#define WAITV(n) asm volatile("s_waitcnt vmcnt(%0)" :: "n"(n) : "memory")
#define BAR() do { asm volatile("" ::: "memory"); __builtin_amdgcn_s_barrier(); asm volatile("" ::: "memory"); } while(0)
#define SB() __builtin_amdgcn_sched_barrier(0)
#define WAITL0() __builtin_amdgcn_s_waitcnt(0xC07F)
#define LBAR() do { WAITL0(); BAR(); } while(0)
#define PIN(x) asm volatile("" : "+v"(x))
DI s16x4 trrd(unsigned a) { return __builtin_amdgcn_ds_read_tr16_b64_v4i16((LAS s16x4*)(size_t)a); }
DI bf16x8 cat8(s16x4 lo, s16x4 hi) { return __builtin_bit_cast(bf16x8, __builtin_shufflevector(lo, hi, 0,1,2,3,4,5,6,7)); }
DI f32x16 mfma32(bf16x8 a, bf16x8 b, f32x16 c) { return __builtin_amdgcn_mfma_f32_32x32x16_bf16(a, b, c, 0,0,0); }
DI float cosr(float r) { return __builtin_amdgcn_cosf(r); }
DI float sinr(float r) { return __builtin_amdgcn_sinf(r); }
DI int crow(int r, int h) { return (r & 3) + 8*(r >> 2) + 4*h; }
DI bf16x8 pack8(const float* v) { u32x4 p = { cvtpk(v[0],v[1]), cvtpk(v[2],v[3]), cvtpk(v[4],v[5]), cvtpk(v[6],v[7]) }; return __builtin_bit_cast(bf16x8, p); }
DI f32x16 zero16() { f32x16 z;
  #pragma unroll
  for (int i=0;i<16;i++) z[i]=0.f; return z; }
DI bf16x8 xor8(bf16x8 v, unsigned m) { u32x4 p = __builtin_bit_cast(u32x4, v); p ^= m; return __builtin_bit_cast(bf16x8, p); }
DI bf16x8 neg8(bf16x8 v) { return xor8(v, 0x80008000u); }
template<typename T> DI T ldsr(unsigned a) { return *(LAS T*)(size_t)a; }
template<typename T> DI void ldsw(unsigned a, T v) { *(LAS T*)(size_t)a = v; }

template<int CTRL> DI float dppadd(float v) { return v + __builtin_bit_cast(float, __builtin_amdgcn_update_dpp(0, __builtin_bit_cast(int, v), CTRL, 0xf, 0xf, true)); }
DI float wave_sum(float v) {
  v = dppadd<0xB1>(v); v = dppadd<0x4E>(v); v = dppadd<0x141>(v); v = dppadd<0x140>(v);
  { auto r = __builtin_amdgcn_permlane16_swap(__float_as_uint(v), __float_as_uint(v), false, false); v = __uint_as_float(r[0]) + __uint_as_float(r[1]); }
  { auto r = __builtin_amdgcn_permlane32_swap(__float_as_uint(v), __float_as_uint(v), false, false); v = __uint_as_float(r[0]) + __uint_as_float(r[1]); }
  return v;
}
DI void trig_frag(float step, float base, bf16x8& c, bf16x8& s) {
  float cv[8], sv[8];
  #pragma unroll
  for (int j=0;j<8;j++) { float r = fmaf(step, (float)(8*(j>>2) + (j&3)), base); cv[j] = cosr(r); sv[j] = sinr(r); }
  c = pack8(cv); s = pack8(sv);
}
DI unsigned mish2(f32x2 u) {
  f32x2 t; t.x = __builtin_amdgcn_exp2f(u.x); t.y = __builtin_amdgcn_exp2f(u.y);
  f32x2 a = t + 1.0f; f32x2 d = a*a + 1.0f;
  f32x2 r; r.x = __builtin_amdgcn_rcpf(d.x); r.y = __builtin_amdgcn_rcpf(d.y);
  f32x2 f = r * (-1.3862943611198906f) + 0.6931471805599453f;
  f32x2 o = u * f;
  return cvtpk(o.x, o.y);
}
constexpr int SPIN_MAX = 1 << 17;
DI void poll1(rsrc_t r, unsigned off, bool act, u32x2 tag) {
  for (int it = 0; it < SPIN_MAX; it++) {
    u32x2 v = ld8(r, off);
    if (__builtin_amdgcn_ballot_w64(act && (v[0] != tag[0] || v[1] != tag[1])) == 0) break;
    __builtin_amdgcn_s_sleep(2);
  }
  asm volatile("" ::: "memory");
}
DI void poll256(rsrc_t r, unsigned base, int lane, u32x2 tag) {
  for (int it = 0; it < SPIN_MAX; it++) {
    u32x4 a = ld16(r, base + lane*16), b = ld16(r, base + 1024 + lane*16);
    bool bad = a[0] != tag[0] || a[1] != tag[1] || a[2] != tag[0] || a[3] != tag[1] || b[0] != tag[0] || b[1] != tag[1] || b[2] != tag[0] || b[3] != tag[1];
    if (__builtin_amdgcn_ballot_w64(bad) == 0) break;
    __builtin_amdgcn_s_sleep(4);
  }
  asm volatile("" ::: "memory");
}
DI void lsync(unsigned flbase, int me, int lane, unsigned phase, unsigned mask) {
  WAITL0(); asm volatile("" ::: "memory");
  if (lane == 0) *(volatile LAS unsigned*)(size_t)(flbase + me*4) = phase;
  for (int it = 0; it < (1 << 22); it++) {
    unsigned v = *(volatile LAS unsigned*)(size_t)(flbase + (lane & 3)*4);
    if (__builtin_amdgcn_ballot_w64(((mask >> (lane & 3)) & 1) && v < phase) == 0) break;
  }
  asm volatile("" ::: "memory");
}

__global__ __launch_bounds__(512, 2) void k_all(const u16* __restrict__ x, const u16* __restrict__ wr, const u16* __restrict__ wi,
    const u16* __restrict__ w1, const u16* __restrict__ b1, const u16* __restrict__ w2, char* __restrict__ ws, u16* __restrict__ out) {
  extern __shared__ __attribute__((aligned(16))) char smem[];
  const int wid = __builtin_amdgcn_readfirstlane(threadIdx.x >> 6), lane = threadIdx.x & 63;
  const int b = blockIdx.x, xcd = b & 7, idx = b >> 3;
  const int tm = (xcd >> 1)*4 + (idx & 3), ks = xcd & 1, tn2 = idx >> 2, tn = ks*8 + tn2;
  const int gid = tm*16 + tn;
  const int gi = lane >> 4, q = (lane & 15) >> 2, p = lane & 3, h = lane >> 5, l31 = lane & 31;
  u64 tsv[8] = {0,0,0,0,0,0,0,0};
  TS(0);
  u64 ep;
  asm volatile("s_load_dwordx2 %0, %1, 0x0" : "=s"(ep) : "s"(ws + WS_E) : "memory");
  const rsrc_t rw_ = __builtin_amdgcn_make_buffer_rsrc(ws, 0, WS_USED, 0x00020000);
  const i32x4 rx = mkrsrc(x, S_*D_*2);
  u32x2 tag;
  #define GETTAG() do { asm volatile("s_waitcnt lgkmcnt(0)" : "+s"(ep) :: "memory"); u64 t_ = ep * 0x9E3779B97F4A7C15ull + 0x632BE59BD9B4E019ull; tag = (u32x2){(unsigned)t_, (unsigned)(t_ >> 32)}; } while (0)

  if (wid < 4) {
    #pragma unroll
    for (int i=0;i<4;i++) dma16(rx, lane*16, gid*16384 + (wid*4+i)*1024, L_EX + (wid*4+i)*1024);
    {
      bf16x8 Cp[2], Sp[2], M1[2], M2[2];
      { float f = (float)l31 * (1.0f/32); trig_frag(f, f*(float)(4*h), Cp[0], Sp[0]); Sp[0] = neg8(Sp[0]);
        unsigned m = (l31 & 1) ? 0x80008000u : 0u; Cp[1] = xor8(Cp[0], m); Sp[1] = xor8(Sp[0], m);
        float f2 = (float)(l31 & 15) * (1.0f/32); trig_frag(f2, f2*(float)(4*h) + ((l31 >= 16) ? 0.25f : 0.f), M1[0], M2[0]);
        M1[1] = xor8(M1[0], m); M2[1] = xor8(M2[0], m); }
      float twc[16], tws[16];
      { float f = (float)l31 * (1.0f/1024), bs = f*(float)(4*h);
        #pragma unroll
        for (int r=0;r<16;r++) { float a = fmaf(f, (float)crow(r,0), bs); twc[r] = cosr(a); tws[r] = sinr(a); PIN(twc[r]); PIN(tws[r]); } }
      GETTAG();
      WAITV(0);
      TS(1);
      bf16x8 xf[2][2];
      #pragma unroll
      for (int rr=0; rr<2; rr++)
        #pragma unroll
        for (int s=0;s<2;s++) { unsigned base = L_EX + (wid*2+rr)*2048 + (16*s + 4*(gi>>1) + q)*64 + (gi&1)*32 + 8*p; xf[rr][s] = cat8(trrd(base), trrd(base + 512)); }
      f32x16 X[2];
      f32x2 stat[2];
      #pragma unroll
      for (int rr=0; rr<2; rr++) {
        f32x16 Pr = zero16(), Pi = zero16();
        #pragma unroll
        for (int s=0;s<2;s++) { Pr = mfma32(xf[rr][s], Cp[s], Pr); Pi = mfma32(xf[rr][s], Sp[s], Pi); }
        float s1 = 0.f, s2 = 0.f;
        const bf16x2 one2 = __builtin_bit_cast(bf16x2, 0x3f803f80u);
        #pragma unroll
        for (int s=0;s<2;s++) { u32x4 u = __builtin_bit_cast(u32x4, xf[rr][s]);
          #pragma unroll
          for (int e=0;e<4;e++) { unsigned ue = u[e]; bf16x2 v = __builtin_bit_cast(bf16x2, ue); s1 = __builtin_amdgcn_fdot2_f32_bf16(v, one2, s1, false); s2 = __builtin_amdgcn_fdot2_f32_bf16(v, v, s2, false); } }
        float ny = wave_sum((lane & 1) ? -s1 : s1);
        s1 = wave_sum(s1); s2 = wave_sum(s2);
        float mean = s1 * (1.0f/D_), var = fmaxf(s2 * (1.0f/D_) - mean*mean, 0.f);
        float rstd = __builtin_amdgcn_rsqf(var + 1e-5f);
        stat[rr] = (f32x2){rstd * 1.4426950408889634f, mean * rstd * 1.4426950408889634f};
        float sub = (l31 == 0) ? 32.f*mean : 0.f;
        float tr_[16], ti_[16];
        #pragma unroll
        for (int r=0;r<16;r++) { float a = Pr[r] - sub, bb = Pi[r]; tr_[r] = a*twc[r] + bb*tws[r]; ti_[r] = bb*twc[r] - a*tws[r]; }
        f32x16 Y = zero16();
        #pragma unroll
        for (int s=0;s<2;s++) { Y = mfma32(M1[s], pack8(tr_ + 8*s), Y); Y = mfma32(M2[s], pack8(ti_ + 8*s), Y); }
        if (lane == 0) { Y[0] = ny; Y[8] = 0.f; }
        X[rr] = Y * rstd;
      }
      asm volatile("" ::: "memory");
      #pragma unroll
      for (int r=0;r<8;r++) ldsw<u32x2>(L_EX + wid*4096 + (l31 + 32*crow(r,h))*8, (u32x2){cvtpk(X[0][r], X[1][r]), cvtpk(X[0][r+8], X[1][r+8])});
      if (lane == 0) ldsw<f32x4>(L_RS + wid*16, (f32x4){stat[0].x, stat[0].y, stat[1].x, stat[1].y});
    }
    const int wm = wid & 1, wn = wid >> 1;
    const int g = gi, c = lane & 15;
    {
      f32x4 acc[7][4];
      #pragma unroll
      for (int i=0;i<7;i++)
        #pragma unroll
        for (int j=0;j<4;j++) { acc[i][j] = (f32x4){0,0,0,0}; PIN(acc[i][j]); }
      unsigned xak[2];
      xak[0] = G1_A + ((wm*64 + c)>>3)*1024 + (c&7)*128 + ((g ^ (c&7))*16);
      xak[1] = G1_A + ((wm*64 + c)>>3)*1024 + (c&7)*128 + (((4+g) ^ (c&7))*16);
      const unsigned wbase = G1_B + (8*g + q)*448 + 8*p;
      const unsigned sgn = (g&1)*32;
      unsigned wb[2][3];
      wb[0][0] = wbase + wn*256 + sgn;
      wb[0][1] = wbase + wn*256 - sgn;
      wb[0][2] = wn ? wbase + 32 - sgn : wbase + sgn;
      #pragma unroll
      for (int i=0;i<3;i++) { wb[1][i] = wb[0][i] + 3*B_SLOT; PIN(wb[0][i]); PIN(wb[1][i]); }
      PIN(xak[0]); PIN(xak[1]);
      bf16x8 xf[2][4], wf[2][7];
      s16x4 lo_[7];
      auto rd = [&](int set, int r, int n) {
        if (n >= 2 && n < 6) { xf[set][n-2] = ldsr<bf16x8>(xak[r&1] + (r>>1)*A_SLOT + (n-2)*2048); return; }
        int i = n < 2 ? 0 : (n-6)/2+1, t = n < 2 ? n : (n-6)&1;
        s16x4 v = trrd(wb[r/3][i == 6 ? 2 : (i&1)] + (r%3)*B_SLOT + i*32 + t*1792);
        if (t==0) lo_[i] = v; else wf[set][i] = cat8(lo_[i], v);
      };
      auto mm = [&](int set, int n) { int i = n>>2, j = (i&1) ? 3-(n&3) : (n&3);
        asm volatile("v_mfma_f32_16x16x32_bf16 %0, %1, %2, %0" : "+v"(acc[i][j]) : "v"(wf[set][i]), "v"(xf[set][j])); };
      LBAR();
      TS(2);
      #pragma unroll
      for (int n=0;n<18;n++) rd(0, 0, n);
      WAITL0();
      auto step = [&](int set, int r1, bool bar) {
        SB();
        #pragma unroll
        for (int n=0;n<28;n++) { mm(set, n); SB(); if (n < 18) { rd(set^1, r1, n); SB(); }
          if (n == 25) { WAITL0(); SB(); }
          if (n == 26 && bar) { BAR(); SB(); } }
      };
      #pragma unroll 1
      for (int s = 0; s < 30; s += 6) { step(0, 1, true); step(1, 2, false); step(0, 3, true); step(1, 4, false); step(0, 5, true); step(1, 0, false); }
      step(0, 1, true);
      f32x4 bl[7], cl[7]; f32x2 st[4];
      #pragma unroll
      for (int n=0;n<28;n++) { mm(1, n); SB();
        if (n < 7) bl[n] = ldsr<f32x4>(L_BN + ((n == 6 ? wn*16 : wn*128) + n*16 + 4*g)*4);
        else if (n < 14) cl[n-7] = ldsr<f32x4>(L_CN + ((n == 13 ? wn*16 : wn*128) + (n-7)*16 + 4*g)*4);
        else if (n < 18) st[n-14] = ldsr<f32x2>(L_ST + (wm*64 + (n-14)*16 + c)*8);
        SB(); }
      asm volatile("s_nop 7\n\ts_nop 7" ::: "memory");
      TS(3);
      SB();
      u32x2 od[4];
      {
        #pragma unroll
        for (int j=0;j<4;j++) { f32x4 v = acc[6][j] * st[j].x + (bl[6] - cl[6] * st[j].y); od[j] = (u32x2){mish2((f32x2){v[0], v[1]}), mish2((f32x2){v[2], v[3]})}; }
        u32x4 snd = wn ? (u32x4){od[0][0], od[0][1], od[1][0], od[1][1]} : (u32x4){od[2][0], od[2][1], od[3][0], od[3][1]};
        ldsw<u32x4>(L_ODD + wid*1024 + lane*16, snd);
        if (lane == 0) *(volatile LAS unsigned*)(size_t)(L_FL + 16 + wid*4) = 1u; }
      const unsigned hb0 = WS_H + (unsigned)(((tm*8 + wm*4)*112 + tn*7)*1024 + lane*16);
      #pragma unroll
      for (int j=0;j<4;j++) {
        #pragma unroll
        for (int ip=0; ip<3; ip++) {
          f32x4 v0 = acc[2*ip][j] * st[j].x + (bl[2*ip] - cl[2*ip] * st[j].y), v1 = acc[2*ip+1][j] * st[j].x + (bl[2*ip+1] - cl[2*ip+1] * st[j].y);
          u32x4 o = { mish2((f32x2){v0[0], v0[1]}), mish2((f32x2){v0[2], v0[3]}), mish2((f32x2){v1[0], v1[1]}), mish2((f32x2){v1[2], v1[3]}) };
          st16(rw_, hb0 + j*112*1024 + (wn*4 + ip)*1024, o);
        }
        if (j == 1) {
          for (int it = 0; it < (1 << 22); it++) { if (*(volatile LAS unsigned*)(size_t)(L_FL + 16 + (wid ^ 2)*4) != 0u) break; }
          asm volatile("" ::: "memory");
          u32x4 rcv = ldsr<u32x4>(L_ODD + (wid ^ 2)*1024 + lane*16);
          u32x4 o0 = wn ? (u32x4){rcv[0], rcv[1], od[2][0], od[2][1]} : (u32x4){od[0][0], od[0][1], rcv[0], rcv[1]};
          u32x4 o1 = wn ? (u32x4){rcv[2], rcv[3], od[3][0], od[3][1]} : (u32x4){od[1][0], od[1][1], rcv[2], rcv[3]};
          st16(rw_, hb0 + (2*wn)*112*1024 + 3*1024, o0);
          st16(rw_, hb0 + (2*wn+1)*112*1024 + 3*1024, o1);
        }
      }
      TS(4);
      WAITV(0);
      if (lane == 0) st8(rw_, WS_F3 + (gid*4 + wid)*8, tag);
    }
    {
      f32x4 acc[4][4];
      #pragma unroll
      for (int i=0;i<4;i++)
        #pragma unroll
        for (int j=0;j<4;j++) { acc[i][j] = (f32x4){0,0,0,0}; PIN(acc[i][j]); }
      unsigned xa = G2_A + wm*8192 + lane*16;
      unsigned wb[4];
      { int rho = 4*(g&1) + q;
        #pragma unroll
        for (int i=0;i<4;i++) wb[i] = G2_B + (4*g + q)*256 + (((wn*4 + i) ^ rho)*32) + 8*p; }
      asm volatile("" : "+v"(xa), "+v"(wb[0]), "+v"(wb[1]), "+v"(wb[2]), "+v"(wb[3]));
      bf16x8 xf[2][2][4], wf[2][2][4];
      s16x4 lo_;
      auto rd = [&](int set, int r, int n) {
        int sub = n / 12, m = n % 12;
        if (m >= 2 && m < 6) { xf[set][sub][m-2] = ldsr<bf16x8>(xa + r*16384 + (m-2)*2048 + sub*1024); return; }
        int i = m < 2 ? 0 : (m-6)/2+1, t = m < 2 ? m : (m-6)&1;
        s16x4 v = trrd(wb[i] + r*16384 + sub*8192 + t*4096);
        if (t==0) lo_ = v; else wf[set][sub][i] = cat8(lo_, v);
      };
      auto mm = [&](int set, int n) { int sub = n & 1, m = n >> 1; int i = m>>2, j = (i&1) ? 3-(m&3) : (m&3);
        asm volatile("v_mfma_f32_16x16x32_bf16 %0, %1, %2, %0" : "+v"(acc[i][j]) : "v"(wf[set][sub][i]), "v"(xf[set][sub][j])); };
      BAR();
      TS(5);
      #pragma unroll
      for (int n=0;n<24;n++) rd(0, 0, n);
      WAITL0(); BAR();
      auto step = [&](int set, int r1, bool bar) {
        SB();
        #pragma unroll
        for (int n=0;n<32;n++) { mm(set, n); SB(); if (n < 24) { rd(set^1, r1, n); SB(); }
          if (n == 29) { WAITL0(); SB(); }
          if (n == 30 && bar) { BAR(); SB(); } }
      };
      #pragma unroll 1
      for (int s = 0; s < 24; s += 4) { step(0, 1, true); step(1, 2, true); step(0, 3, true); step(1, 0, true); }
      step(0, 1, true); step(1, 2, true); step(0, 3, false);
      #pragma unroll
      for (int n=0;n<32;n++) { mm(1, n); SB(); if (n == 27) { BAR(); SB(); } }
      asm volatile("s_nop 7\n\ts_nop 7" ::: "memory");
      TS(6);
      SB();
      const unsigned pb = G2_A + wid*8192;
      #pragma unroll
      for (int j=0;j<4;j++)
        #pragma unroll
        for (int i=0;i<4;i++) ldsw<u32x2>(pb + (j*16 + c)*128 + (((i*2 + (g>>1)) ^ (c & 7))*16) + (g&1)*8, (u32x2){cvtpk(acc[i][j][0], acc[i][j][1]), cvtpk(acc[i][j][2], acc[i][j][3])});
      asm volatile("" ::: "memory");
      #pragma unroll
      for (int i=0;i<8;i++) { int r = i*8 + (lane >> 3); u32x4 v = ldsr<u32x4>(pb + r*128 + (((lane & 7) ^ (r & 7))*16));
        st16(rw_, WS_PART + ks*(4u<<20) + (unsigned)((tm*128 + wm*64 + r)*2048 + tn2*256 + wn*128 + (lane & 7)*16), v); }
      WAITV(0);
      if (lane == 0) st8(rw_, WS_F4 + (gid*4 + wid)*8, tag);
    }
  } else {
    const int w = wid - 4;
    const i32x4 rb = mkrsrc(w1, D_*F_*2), rz = mkrsrc(ws, WS_USED);
    { int r = w*16 + (lane >> 2); unsigned o = (unsigned)(tn*448 + (lane & 3)*128); if ((lane & 3) == 3) o = tn*448 + 384;
      touch(rb, (unsigned)(r*F_*2) + o, tm*64*F_*2, L_ODD + w*256); }
    const int col = w >> 1, hf = w & 1;
    bf16x8 C64[4], S64[4], CA[2], SA[2], CB[2], SB_[2];
    float twc[16], tws[16];
    auto col_consts = [&]() {
      const int u = l31 + 32*hf;
      { float f = (float)u * (1.0f/64); trig_frag(f, f*(float)(4*h), C64[0], S64[0]); trig_frag(f, f*(float)(16 + 4*h), C64[1], S64[1]);
        unsigned m = (u & 1) ? 0x80008000u : 0u; C64[2] = xor8(C64[0], m); S64[2] = xor8(S64[0], m); C64[3] = xor8(C64[1], m); S64[3] = xor8(S64[1], m); }
      { float f = (float)(2*l31 + hf) * (1.0f/64); trig_frag(f, f*(float)(4*h), CA[0], SA[0]); trig_frag(f, f*(float)(16 + 4*h), CA[1], SA[1]); }
      { float f = (float)l31 * (1.0f/32); trig_frag(f, f*((float)(4*h) + 0.5f*(float)hf), CB[0], SB_[0]);
        unsigned m = (l31 & 1) ? 0x80008000u : 0u; CB[1] = xor8(CB[0], m); SB_[1] = xor8(SB_[0], m); }
      { float f = (float)l31 * (1.0f/2048), bs = f*(float)(4*h);
        #pragma unroll
        for (int r=0;r<16;r++) { float a = fmaf(f, (float)crow(r,0), bs); twc[r] = cosr(a); tws[r] = sinr(a); PIN(twc[r]); PIN(tws[r]); } }
      #pragma unroll
      for (int s=0;s<4;s++) { PIN(C64[s]); PIN(S64[s]); }
      #pragma unroll
      for (int s=0;s<2;s++) { PIN(CA[s]); PIN(SA[s]); PIN(CB[s]); PIN(SB_[s]); }
    };
    if (w < 2) {
      __builtin_amdgcn_s_setprio(2);
      unsigned va[8], vb[14];
      #pragma unroll
      for (int i=0;i<8;i++) { int pc = w*8+i; int r = pc*8 + (lane>>3); va[i] = (unsigned)((tm*128 + r)*D_*2 + (((lane&7) ^ (r&7))*16)); }
      #pragma unroll
      for (int i=0;i<14;i++) { int pc = w + 2*i; int ci = pc*64 + lane; int r = (ci*9363)>>18, c = ci - r*28; c ^= ((r>>3)&1)<<1; vb[i] = (unsigned)(r*F_*2 + tn*448 + c*16); }
      auto issueA = [&](int k, int slot) {
        #pragma unroll
        for (int i=0;i<8;i++) dma16(rx, va[i], k*128, G1_A + w*8192 + slot*A_SLOT + i*1024); };
      auto issueB = [&](int k, int slot) {
        #pragma unroll
        for (int i=0;i<14;i++) dma16(rb, vb[i], k*64*F_*2, G1_B + w*1024 + slot*2*B_SLOT + i*2048); };
      issueA(0,0); issueB(0,0); issueA(1,1); issueB(1,1); issueA(2,2);
      __builtin_amdgcn_s_setprio(0);
      col_consts();
      GETTAG();
      __builtin_amdgcn_s_setprio(2);
      WAITV(30); BAR();
      issueB(2,2);
      int slot = 0;
      #pragma unroll 1
      for (int k = 0; k < 13; k++) { WAITV(22); BAR(); issueA(k+3, slot); issueB(k+3, slot); slot = slot == 2 ? 0 : slot+1; }
      __builtin_amdgcn_s_setprio(0);
      WAITV(22); BAR();
      WAITV(0); BAR();
      BAR();
      { const i32x4 r2 = mkrsrc(w2, F_*D_*2);
        unsigned vo = (unsigned)((lane < 56 ? lane : 55)*128);
        touch(r2, vo, b*28672 + w*14336, L_MISC + 8192 + w*512); touch(r2, vo, b*28672 + w*14336 + 7168, L_MISC + 8192 + w*512 + 256); }
    } else {
      const int a = w - 2, t = a*64 + lane;
      const i32x4 rwt = mkrsrc(a ? wi : wr, S_*DF_*2);
      if (a == 0) {
        #pragma unroll 4
        for (int pc=0; pc<28; pc++) { int ci = pc*64 + lane; int r = (ci*9363)>>18, c = ci - r*28; c ^= ((r>>3)&1)<<1;
          dma16(rb, (unsigned)(r*F_*2 + tn*448 + c*16), tm*64*F_*2, G1_B + 4*B_SLOT + pc*1024); }
      } else { if (lane < 16) *(volatile LAS unsigned*)(size_t)(L_FL + lane*4) = 0u; }
      col_consts();
      GETTAG();
      if (a == 0) {
        WAITV(0);
        const u32x4 onesu = {0x3f803f80u,0x3f803f80u,0x3f803f80u,0x3f803f80u};
        const bf16x8 ones = __builtin_bit_cast(bf16x8, onesu);
        #pragma unroll 2
        for (int u=0; u<14; u++) {
          f32x4 s4 = {0,0,0,0};
          #pragma unroll
          for (int sub=0; sub<2; sub++) { unsigned base = G1_B + 4*B_SLOT + (32*sub + 8*gi + q)*448 + ((u ^ (gi&1))*32) + 8*p;
            s4 = __builtin_amdgcn_mfma_f32_16x16x32_bf16(cat8(trrd(base), trrd(base + 1792)), ones, s4, 0,0,0); }
          if ((lane & 15) == 0) st16(rw_, WS_CS + (unsigned)((tm*F_ + tn*224 + u*16 + 4*gi)*4), __builtin_bit_cast(u32x4, s4));
        }
      }
      touch(rwt, lane*128, gid*8208, L_ODD + 1024 + a*512); touch(rwt, lane*128, gid*8208 + 128, L_ODD + 1024 + a*512 + 256);
      LBAR();
      const unsigned zw = WS_ZW + gid*32768;
      #pragma unroll
      for (int i=0;i<8;i++) { int ch = t + 128*i; u32x4 v;
        #pragma unroll
        for (int e=0;e<4;e++) v[e] = ldsr<unsigned>(L_EX + e*4096 + ch*4);
        st16(rw_, zw + (ch >> 2)*128 + (ch & 3)*16, v); }
      if (t < 4) st16(rw_, WS_STAT + gid*64 + t*16, ldsr<u32x4>(L_RS + t*16));
      LBAR();
      #pragma unroll
      for (int i=0;i<9;i++) dma16(rwt, lane*16, gid*8208 + i*1024, L_EX + a*9216 + i*1024);
      BAR();
      WAITV(0); BAR();
      #pragma unroll 1
      for (int half=0; half<2; half++) {
        #pragma unroll
        for (int i=0;i<4;i++) { int id = t + 128*(i + 4*half); int k = id >> 1, hh = id & 1; if (k == 0) k = 512;
          u32x4 v;
          #pragma unroll
          for (int e=0;e<4;e++) { unsigned off = L_EX + ((hh*4+e)*DF_ + k)*2; v[e] = (unsigned)ldsr<u16>(off) | ((unsigned)ldsr<u16>(off + 9216) << 16); }
          st16(rw_, zw + (id >> 2)*128 + 64 + (id & 3)*16, v); }
        LBAR();
      }
      WAITV(0); BAR();
      if (t == 0) st8(rw_, WS_F1 + gid*8, tag);
      BAR(); BAR(); BAR(); BAR();
      poll256(rw_, WS_F1, lane, tag);
      if (b == 0 && t == 0) { u64 e1 = ep + 1; st8(rw_, WS_E, (u32x2){(unsigned)e1, (unsigned)(e1 >> 32)}); }
      unsigned gv = (unsigned)((lane >> 3)*32768);
      #pragma unroll
      for (int i=0;i<8;i++) { int pc = a*8 + i; dma16s(rz, gv + (((lane & 7) ^ ((pc*4 + (lane >> 4)) & 7))*16), WS_ZW + pc*8*32768 + gid*128, C_IN0 + pc*1024); }
      BAR();
      f32x2 csn = {0.f, 0.f}, bn = {0.f, 0.f}; u32x2 stv;
      { const int tt = t < 112 ? t : 111;
        #pragma unroll
        for (int i=0;i<16;i++) csn += __builtin_bit_cast(f32x2, ld8(rw_, WS_CS + (unsigned)((i*F_ + tn*224 + tt*2)*4)));
        unsigned bv = *(const unsigned*)(b1 + tn*224 + tt*2); bn = (f32x2){bflo(bv), bfhi(bv)} * 1.4426950408889634f;
        stv = ld8(rw_, WS_STAT + (unsigned)((tm*128 + t)*8)); }
      BAR();
      PIN(csn); PIN(bn); PIN(stv);
      BAR();
      BAR();
      #pragma unroll
      for (int i=0;i<8;i++) { int pc = 16 + a*8 + i; dma16s(rz, gv + (((lane & 7) ^ ((pc*4 + (lane >> 4)) & 7))*16), WS_ZW + pc*8*32768 + gid*128, C_IN1 + (pc-16)*1024); }
      WAITV(0); BAR();
      if (t < 112) { ldsw<f32x2>(L_BN + t*8, bn); ldsw<f32x2>(L_CN + t*8, csn); }
      ldsw<u32x2>(L_ST + t*8, stv);
      LBAR();
    }
    TS(3);
    {
      const int u = l31 + 32*hf;
      f32x16 Pr = zero16(), Pi = zero16();
      #pragma unroll
      for (int s=0;s<4;s++) {
        s16x4 v[2][2];
        #pragma unroll
        for (int t=0;t<2;t++) { int a = 16*(s&1) + 8*t + 4*(gi>>1) + q; int P = 4*(gi&1) + p; int rw = a*4 + (P >> 1);
          #pragma unroll
          for (int ri=0;ri<2;ri++) v[ri][t] = trrd(((s>>1) ? C_IN1 : C_IN0) + rw*128 + (((col*2 + ri) ^ ((rw >> 1) & 7))*16) + (P & 1)*8); }
        bf16x8 zr = cat8(v[0][0], v[0][1]), zi = cat8(v[1][0], v[1][1]);
        Pr = mfma32(zr, C64[s], Pr); Pi = mfma32(zi, C64[s], Pi);
        Pr = mfma32(zi, S64[s], Pr); Pi = mfma32(neg8(zr), S64[s], Pi);
      }
      unsigned wv[16];
      #pragma unroll
      for (int r=0;r<16;r++) { int j = u + 64*crow(r & 7, h); int rw = j >> 3; int ch = 4 + col*2 + ((j >> 2) & 1);
        wv[r] = ldsr<unsigned>(((r >> 3) ? C_IN1 : C_IN0) + rw*128 + ((ch ^ ((rw >> 1) & 7))*16) + (j & 3)*4); }
      float a_[16], b_[16];
      #pragma unroll
      for (int r=0;r<16;r++) { a_[r] = Pr[r]*twc[r] + Pi[r]*tws[r]; b_[r] = Pi[r]*twc[r] - Pr[r]*tws[r]; }
      f32x16 Ur = zero16(), Ui = zero16();
      #pragma unroll
      for (int s=0;s<2;s++) { bf16x8 tr_ = pack8(a_ + 8*s), ti_ = pack8(b_ + 8*s);
        Ur = mfma32(CA[s], tr_, Ur); Ui = mfma32(CA[s], ti_, Ui);
        Ur = mfma32(SA[s], ti_, Ur); Ui = mfma32(SA[s], neg8(tr_), Ui); }
      #pragma unroll
      for (int r=0;r<16;r++) { float wre = bflo(wv[r]), wim = bfhi(wv[r]); a_[r] = Ur[r]*wre - Ui[r]*wim; b_[r] = Ur[r]*wim + Ui[r]*wre; }
      f32x16 Qr = zero16(), Qi = zero16();
      #pragma unroll
      for (int s=0;s<2;s++) { bf16x8 yr = pack8(a_ + 8*s), yi = pack8(b_ + 8*s);
        Qr = mfma32(yr, CB[s], Qr); Qi = mfma32(yi, CB[s], Qi);
        Qr = mfma32(neg8(yi), SB_[s], Qr); Qi = mfma32(yr, SB_[s], Qi); }
      #pragma unroll
      for (int r=0;r<16;r++) { a_[r] = Qr[r]*twc[r] - Qi[r]*tws[r]; b_[r] = Qi[r]*twc[r] + Qr[r]*tws[r]; }
      bf16x8 Rr[4], Ri[4];
      { unsigned ex = C_EXC + w*4096 + lane*16;
        ldsw<bf16x8>(ex, pack8(a_)); ldsw<bf16x8>(ex+1024, pack8(a_+8)); ldsw<bf16x8>(ex+2048, pack8(b_)); ldsw<bf16x8>(ex+3072, pack8(b_+8));
        lsync(L_FL, w, lane, 1u, 3u << (col*2));
        #pragma unroll
        for (int s=0;s<4;s++) { unsigned e2 = C_EXC + (col*2 + (s>>1))*4096 + lane*16 + (s&1)*1024; Rr[s] = ldsr<bf16x8>(e2); Ri[s] = ldsr<bf16x8>(e2 + 2048); } }
      f32x16 Zr = zero16(), Zi = zero16();
      #pragma unroll
      for (int s=0;s<4;s++) { Zr = mfma32(Rr[s], C64[s], Zr); Zi = mfma32(Ri[s], C64[s], Zi);
        Zr = mfma32(neg8(Ri[s]), S64[s], Zr); Zi = mfma32(Rr[s], S64[s], Zi); }
      const float sc = ((gid == 0 && col == 0) ? 1.0f : 2.0f) / (float)(S_*D_);
      #pragma unroll
      for (int r=0;r<16;r++) { int rw = 4*u + (r >> 2), rp = 2*h + ((r & 3) >> 1), row = r & 1;
        unsigned pk = cvtpk(Zr[r]*sc, Zi[r]*sc);
        unsigned ad = C_OUT + (((rw*4 + rp) ^ (l31 & 15))*16) + row*4 + col*2;
        ldsw<u16>(ad, (u16)pk); ldsw<u16>(ad + 8, (u16)(pk >> 16)); }
      lsync(L_FL, w, lane, 2u, 15u);
      #pragma unroll
      for (int i=0;i<4;i++) { int ch = w*64 + lane + 256*i; st16(rw_, WS_Z2 + gid*16384 + ch*16, ldsr<u32x4>(C_OUT + ((ch ^ ((ch >> 4) & 15))*16))); }
    }
    TS(4);
    bf16x8 C16, S16, C2[2], S2[2];
    float tc[16], ts_[16];
    auto row_consts = [&]() {
      float f = (float)l31 * (1.0f/32); trig_frag(f, f*(float)(4*h), C16, S16);
      float cv[8], sv[8];
      #pragma unroll
      for (int j=0;j<8;j++) { float r = fmaf(f, (float)(4*(j>>1) + (j&1)), f*(float)(2*h)); cv[j] = cosr(r); sv[j] = -sinr(r); }
      C2[0] = pack8(cv); S2[0] = pack8(sv);
      unsigned m = (l31 & 1) ? 0x80008000u : 0u; C2[1] = xor8(C2[0], m); S2[1] = xor8(S2[0], m);
      float f2 = (float)l31 * (1.0f/1024);
      #pragma unroll
      for (int e=0;e<16;e++) { int j = e & 7; float a = fmaf(f2, (float)(16*(e>>3) + 4*(j>>1) + (j&1)), f2*(float)(2*h)); tc[e] = cosr(a); ts_[e] = sinr(a); PIN(tc[e]); PIN(ts_[e]); }
      PIN(C16); PIN(S16); PIN(C2[0]); PIN(C2[1]); PIN(S2[0]); PIN(S2[1]);
    };
    if (w < 2) {
      const i32x4 r2 = mkrsrc(w2, F_*D_*2);
      unsigned va[8], vb[8];
      #pragma unroll
      for (int i=0;i<8;i++) { int pc = w*8+i; va[i] = WS_H + (unsigned)(((tm*8 + (pc>>1))*112 + ks*56 + (pc&1))*1024 + lane*16);
        int r = pc*4 + (lane>>4); int c = (lane&15) ^ ((r&7)<<1); vb[i] = (unsigned)((ks*1792 + r)*D_*2 + tn2*256 + c*16); }
      auto issueA = [&](int k, int slot) {
        #pragma unroll
        for (int i=0;i<8;i++) dma16s(rz, va[i], k*2048, G2_A + slot*16384 + w*8192 + i*1024); };
      auto issueB = [&](int k, int slot) {
        #pragma unroll
        for (int i=0;i<8;i++) dma16(r2, vb[i], k*64*D_*2, G2_B + slot*16384 + w*8192 + i*1024); };
      lsync(L_FL, w, lane, 3u, 15u);
      __builtin_amdgcn_s_setprio(2);
      issueB(0,0); issueB(1,1); issueB(2,2); issueB(3,3);
      poll1(rw_, WS_F3 + (unsigned)((tm*16 + ks*8)*32 + l31*8), true, tag);
      TS(5);
      issueA(0,0); issueA(1,1); issueA(2,2); issueA(3,3);
      WAITV(24); BAR();
      WAITV(16); BAR(); issueB(4,0); issueA(4,0);
      WAITV(24); BAR(); issueB(5,1); issueA(5,1);
      #pragma unroll 1
      for (int k = 2; k < 24; k++) { WAITV(32); BAR(); issueB(k+4, k & 3); issueA(k+4, k & 3); }
      __builtin_amdgcn_s_setprio(0);
      WAITV(32); BAR();
      row_consts();
      WAITV(16); BAR();
      WAITV(0); BAR();
    } else {
      const int a = w - 2;
      lsync(L_FL, w, lane, 3u, 0u);
      row_consts();
      WAITV(0); BAR();
      if (a == 0 && lane == 0) st8(rw_, WS_F2 + gid*8, tag);
      #pragma unroll 1
      for (int k = 0; k < 16; k++) BAR();
      poll256(rw_, WS_F2, lane, tag);
      #pragma unroll 1
      for (int k = 0; k < 9; k++) BAR();
      #pragma unroll
      for (int i=0;i<8;i++) { int pc = a*8 + i; dma16s(rz, (unsigned)((lane >> 2)*16384 + (((lane & 3) ^ (pc & 3))*16)), WS_Z2 + pc*16*16384 + gid*64, E_Z + pc*1024); }
      BAR();
      BAR();
      WAITV(0);
    }
    BAR();
    TS(6);
    {
      f32x16 Qr[2], Qi[2];
      #pragma unroll
      for (int s=0;s<2;s++) {
        s16x4 v[2][2];
        #pragma unroll
        for (int t=0;t<2;t++) { int k2 = 8*t + 4*(gi>>1) + q; int cw = 16*k2 + 8*s + 4*(gi&1) + p;
          #pragma unroll
          for (int ri=0;ri<2;ri++) v[ri][t] = trrd(E_Z + cw*64 + ((w ^ (k2 & 3))*16) + ri*8); }
        bf16x8 zr = cat8(v[0][0], v[0][1]), zi = cat8(v[1][0], v[1][1]);
        Qr[s] = mfma32(zr, C16, zero16()); Qi[s] = mfma32(zi, C16, zero16());
        Qr[s] = mfma32(neg8(zi), S16, Qr[s]); Qi[s] = mfma32(zr, S16, Qi[s]);
      }
      { u32x2 z0 = ldsr<u32x2>(E_Z + w*16);
        float f = (h == 0 && (l31 & 1)) ? -2.f : 0.f;
        Qr[0][0] += f * bflo(z0[0]); Qr[0][2] += f * bflo(z0[1]); }
      #pragma unroll
      for (int row=0; row<2; row++) {
        f32x16 Y = zero16();
        #pragma unroll
        for (int s=0;s<2;s++) { float a_[8], b_[8];
          #pragma unroll
          for (int j=0;j<8;j++) { int r = 4*(j>>1) + 2*row + (j&1); float qr = Qr[s][r], qi = Qi[s][r]; a_[j] = qr*tc[8*s+j] - qi*ts_[8*s+j]; b_[j] = qi*tc[8*s+j] + qr*ts_[8*s+j]; }
          Y = mfma32(pack8(a_), C2[s], Y); Y = mfma32(pack8(b_), S2[s], Y); }
        #pragma unroll
        for (int ga=0; ga<4; ga++) ldsw<u32x2>(E_Y + (2*w + row)*2048 + l31*64 + ((ga ^ ((l31 >> 1) & 3))*16) + h*8, (u32x2){cvtpk(Y[4*ga], Y[4*ga+1]), cvtpk(Y[4*ga+2], Y[4*ga+3])});
      }
    }
    if (w == 3) poll1(rw_, WS_F4 + (unsigned)(tm*512 + lane*8), (lane & 1) == (tn >> 3), tag);
  }
  {
    const int t = threadIdx.x;
    u32x4 xv[2];
    #pragma unroll
    for (int i=0;i<2;i++) xv[i] = *(const u32x4*)((const char*)x + gid*16384 + (t + 512*i)*16);
    LBAR();
    TS(7);
    u32x4 p0[2], p1[2], yv[2];
    #pragma unroll
    for (int i=0;i<2;i++) { unsigned o = WS_PART + gid*16384 + (t + 512*i)*16; p0[i] = ld16(rw_, o); p1[i] = ld16(rw_, o + (4u<<20)); }
    #pragma unroll
    for (int i=0;i<2;i++) { int ch = t + 512*i; yv[i] = ldsr<u32x4>(E_Y + ((ch & ~3) | ((ch & 3) ^ ((ch >> 3) & 3)))*16); }
    #pragma unroll
    for (int i=0;i<2;i++) { u32x4 o;
      #pragma unroll
      for (int e=0;e<4;e++) { unsigned xe = xv[i][e], ye = yv[i][e], pe = p0[i][e], qe = p1[i][e];
        o[e] = cvtpk((bflo(xe) + bflo(ye)) + (bflo(pe) + bflo(qe)), (bfhi(xe) + bfhi(ye)) + (bfhi(pe) + bfhi(qe))); }
      *(u32x4*)((char*)out + gid*16384 + (t + 512*i)*16) = o; }
  }
  if (wid == 0) { while (__builtin_amdgcn_s_memrealtime() - tsv[PADSLOT] < 6000) __builtin_amdgcn_s_sleep(1); }
}

extern "C" void kernel_launch(void* const* d_in, const int* in_sizes, int n_in, void* d_out, int out_size, void* d_ws, size_t ws_size, hipStream_t stream) {
  (void)hipFuncSetAttribute((const void*)k_all, hipFuncAttributeMaxDynamicSharedMemorySize, L_TOT);
  k_all<<<256,512,L_TOT,stream>>>((const u16*)d_in[0], (const u16*)d_in[1], (const u16*)d_in[2], (const u16*)d_in[4], (const u16*)d_in[5], (const u16*)d_in[6], (char*)d_ws, (u16*)d_out);
}
```

```cpp
#include <hip/hip_runtime.h>
#include <hip/hip_bf16.h>
#include <stdint.h>
typedef unsigned short u16;
typedef unsigned long long u64;
#define LAS __attribute__((address_space(3)))
typedef short s16x4 __attribute__((ext_vector_type(4)));
typedef __bf16 bf16x8 __attribute__((ext_vector_type(8)));
typedef __bf16 bf16x2 __attribute__((ext_vector_type(2)));
typedef float f32x2 __attribute__((ext_vector_type(2)));
typedef float f32x4 __attribute__((ext_vector_type(4)));
typedef float f32x16 __attribute__((ext_vector_type(16)));
typedef unsigned u32x4 __attribute__((ext_vector_type(4)));
typedef unsigned u32x2 __attribute__((ext_vector_type(2)));
typedef int i32x4 __attribute__((ext_vector_type(4)));
#define DI __device__ __forceinline__

#define CS2_DELAY 1
#define CODE_BYTES 40960
#define FPRIO 0
#define ROT 0
#define LOOK 2
#define GDELAY 0
#define HPRIO 1
#define S_ 2048
#define D_ 1024
#define F_ 3584
#define DF_ 513

static __device__ const unsigned __attribute__((aligned(1024))) TAB[10240] = {
0x3f803f80,0x3f803f80,0x3f803f80,0x3f803f80,0x3f7f3f80,0x3f753f7b,0x3f223f35,0x3ef13f0e,0x3f7b3f80,0x3f553f6d,0xbe480000,0xbf0ebec4,0x3f753f80,0x3f223f55,0xbf62bf35,0xbf7fbf7b,
0x3f6d3f80,0x3ec43f35,0xbf6dbf80,0xbec4bf35,0x3f623f80,0x3dc93f0e,0xbe95bf35,0x3f223e48,0x3f553f80,0xbe483ec4,0x3f0e0000,0x3f7b3f6d,0x3f463f80,0xbef13e48,0x3f7f3f35,0x3e953f55,
0x3f353f80,0xbf350000,0x3f353f80,0xbf350000,0x3f223f80,0xbf62be48,0xbdc93f35,0xbf75bf55,0x3f0e3f80,0xbf7bbec4,0xbf550000,0xbe48bf6d,0x3ef13f80,0xbf7fbf0e,0xbf75bf35,0x3f46be48,
0x3ec43f80,0xbf6dbf35,0xbec4bf80,0x3f6d3f35,0x3e953f80,0xbf46bf55,0x3ef1bf35,0x3dc93f7b,0x3e483f80,0xbf0ebf6d,0x3f7b0000,0xbf553ec4,0x3dc93f80,0xbe95bf7b,0x3f463f35,0xbf62bf0e,
0x3f80,0xbf80,0x3f80,0xbf80,0xbdc93f80,0x3e95bf7b,0xbf463f35,0x3f62bf0e,0xbe483f80,0x3f0ebf6d,0xbf7b0000,0x3f553ec4,0xbe953f80,0x3f46bf55,0xbef1bf35,0xbdc93f7b,
0xbec43f80,0x3f6dbf35,0x3ec4bf80,0xbf6d3f35,0xbef13f80,0x3f7fbf0e,0x3f75bf35,0xbf46be48,0xbf0e3f80,0x3f7bbec4,0x3f550000,0x3e48bf6d,0xbf223f80,0x3f62be48,0x3dc93f35,0x3f75bf55,
0xbf353f80,0x3f350000,0xbf353f80,0x3f350000,0xbf463f80,0x3ef13e48,0xbf7f3f35,0xbe953f55,0xbf553f80,0x3e483ec4,0xbf0e0000,0xbf7b3f6d,0xbf623f80,0xbdc93f0e,0x3e95bf35,0xbf223e48,
0xbf6d3f80,0xbec43f35,0x3f6dbf80,0x3ec4bf35,0xbf753f80,0xbf223f55,0x3f62bf35,0x3f7fbf7b,0xbf7b3f80,0xbf553f6d,0x3e480000,0x3f0ebec4,0xbf7f3f80,0xbf753f7b,0xbf223f35,0xbef13f0e,
0x3f803f80,0x3f803f80,0x3f803f80,0x3f803f80,0x3f623f6d,0x3f463f55,0x3e953ec4,0x3dc93e48,0x3f0e3f35,0x3e483ec4,0xbf55bf35,0xbf7bbf6d,0x3dc93ec4,0xbef1be48,0xbf46bf6d,0xbe95bf0e,
0xbec40000,0xbf6dbf35,0x3ec40000,0x3f6d3f35,0xbf46bec4,0xbf75bf7b,0x3f7f3f6d,0x3ef13f55,0xbf7bbf35,0xbf0ebf6d,0x3e483f35,0xbf55bec4,0xbf75bf6d,0x3dc9bf0e,0xbf62bec4,0xbf22bf7b,
0xbf35bf80,0x3f350000,0xbf35bf80,0x3f350000,0xbe95bf6d,0x3f7f3f0e,0x3ef1bec4,0x3f463f7b,0x3e48bf35,0x3f553f6d,0x3f7b3f35,0xbf0e3ec4,0x3f22bec4,0x3e953f7b,0x3dc93f6d,0xbf62bf55,
0x3f6d0000,0xbec43f35,0xbf6d0000,0x3ec4bf35,0x3f7f3ec4,0xbf623e48,0xbf22bf6d,0x3f753f0e,0x3f553f35,0xbf7bbec4,0x3f0ebf35,0xbe483f6d,0x3ef13f6d,0xbf22bf55,0x3f753ec4,0xbf7fbe48,
0x3f80,0xbf80,0x3f80,0xbf80,0xbef13f6d,0x3f22bf55,0xbf753ec4,0x3f7fbe48,0xbf553f35,0x3f7bbec4,0xbf0ebf35,0x3e483f6d,0xbf7f3ec4,0x3f623e48,0x3f22bf6d,0xbf753f0e,
0xbf6d0000,0x3ec43f35,0x3f6d0000,0xbec4bf35,0xbf22bec4,0xbe953f7b,0xbdc93f6d,0x3f62bf55,0xbe48bf35,0xbf553f6d,0xbf7b3f35,0x3f0e3ec4,0x3e95bf6d,0xbf7f3f0e,0xbef1bec4,0xbf463f7b,
0x3f35bf80,0xbf350000,0x3f35bf80,0xbf350000,0x3f75bf6d,0xbdc9bf0e,0x3f62bec4,0x3f22bf7b,0x3f7bbf35,0x3f0ebf6d,0xbe483f35,0x3f55bec4,0x3f46bec4,0x3f75bf7b,0xbf7f3f6d,0xbef13f55,
0x3ec40000,0x3f6dbf35,0xbec40000,0xbf6d3f35,0xbdc93ec4,0x3ef1be48,0x3f46bf6d,0x3e95bf0e,0xbf0e3f35,0xbe483ec4,0x3f55bf35,0x3f7bbf6d,0xbf623f6d,0xbf463f55,0xbe953ec4,0xbdc93e48,
0x0,0x0,0x0,0x0,0x3dc90000,0x3e953e48,0x3f463f35,0x3f623f55,0x3e480000,0x3f0e3ec4,0x3f7b3f80,0x3f553f6d,0x3e950000,0x3f463f0e,0x3ef13f35,0xbdc93e48,
0x3ec40000,0x3f6d3f35,0xbec40000,0xbf6dbf35,0x3ef10000,0x3f7f3f55,0xbf75bf35,0xbf46bf7b,0x3f0e0000,0x3f7b3f6d,0xbf55bf80,0x3e48bec4,0x3f220000,0x3f623f7b,0xbdc9bf35,0x3f753f0e,
0x3f350000,0x3f353f80,0x3f350000,0x3f353f80,0x3f460000,0x3ef13f7b,0x3f7f3f35,0xbe953f0e,0x3f550000,0x3e483f6d,0x3f0e3f80,0xbf7bbec4,0x3f620000,0xbdc93f55,0xbe953f35,0xbf22bf7b,
0x3f6d0000,0xbec43f35,0xbf6d0000,0x3ec4bf35,0x3f750000,0xbf223f0e,0xbf62bf35,0x3f7f3e48,0x3f7b0000,0xbf553ec4,0xbe48bf80,0x3f0e3f6d,0x3f7f0000,0xbf753e48,0x3f22bf35,0xbef13f55,
0x3f800000,0xbf800000,0x3f800000,0xbf800000,0x3f7f0000,0xbf75be48,0x3f223f35,0xbef1bf55,0x3f7b0000,0xbf55bec4,0xbe483f80,0x3f0ebf6d,0x3f750000,0xbf22bf0e,0xbf623f35,0x3f7fbe48,
0x3f6d0000,0xbec4bf35,0xbf6d0000,0x3ec43f35,0x3f620000,0xbdc9bf55,0xbe95bf35,0xbf223f7b,0x3f550000,0x3e48bf6d,0x3f0ebf80,0xbf7b3ec4,0x3f460000,0x3ef1bf7b,0x3f7fbf35,0xbe95bf0e,
0x3f350000,0x3f35bf80,0x3f350000,0x3f35bf80,0x3f220000,0x3f62bf7b,0xbdc93f35,0x3f75bf0e,0x3f0e0000,0x3f7bbf6d,0xbf553f80,0x3e483ec4,0x3ef10000,0x3f7fbf55,0xbf753f35,0xbf463f7b,
0x3ec40000,0x3f6dbf35,0xbec40000,0xbf6d3f35,0x3e950000,0x3f46bf0e,0x3ef1bf35,0xbdc9be48,0x3e480000,0x3f0ebec4,0x3f7bbf80,0x3f55bf6d,0x3dc90000,0x3e95be48,0x3f46bf35,0x3f62bf55,
0x0,0x0,0x0,0x0,0x3ef13ec4,0x3f223f0e,0x3f753f6d,0x3f7f3f7b,0x3f553f35,0x3f7b3f6d,0x3f0e3f35,0x3e483ec4,0x3f7f3f6d,0x3f623f7b,0xbf22bec4,0xbf75bf55,
0x3f6d3f80,0x3ec43f35,0xbf6dbf80,0xbec4bf35,0x3f223f6d,0xbe953e48,0x3dc9bec4,0x3f623f0e,0x3e483f35,0xbf55bec4,0x3f7b3f35,0x3f0e3f6d,0xbe953ec4,0xbf7fbf55,0x3ef13f6d,0xbf46be48,
0xbf350000,0xbf35bf80,0xbf350000,0xbf35bf80,0xbf75bec4,0xbdc9bf55,0xbf62bf6d,0x3f22be48,0xbf7bbf35,0x3f0ebec4,0x3e48bf35,0x3f553f6d,0xbf46bf6d,0x3f753e48,0x3f7f3ec4,0xbef13f0e,
0xbec4bf80,0x3f6d3f35,0x3ec43f80,0xbf6dbf35,0x3dc9bf6d,0x3ef13f7b,0xbf463ec4,0x3e95bf55,0x3f0ebf35,0xbe483f6d,0xbf55bf35,0x3f7b3ec4,0x3f62bec4,0xbf463f0e,0x3e95bf6d,0xbdc93f7b,
0x3f800000,0xbf800000,0x3f800000,0xbf800000,0x3f623ec4,0xbf46bf0e,0x3e953f6d,0xbdc9bf7b,0x3f0e3f35,0xbe48bf6d,0xbf553f35,0x3f7bbec4,0x3dc93f6d,0x3ef1bf7b,0xbf46bec4,0x3e953f55,
0xbec43f80,0x3f6dbf35,0x3ec4bf80,0xbf6d3f35,0xbf463f6d,0x3f75be48,0x3f7fbec4,0xbef1bf0e,0xbf7b3f35,0x3f0e3ec4,0x3e483f35,0x3f55bf6d,0xbf753ec4,0xbdc93f55,0xbf623f6d,0x3f223e48,
0xbf350000,0xbf353f80,0xbf350000,0xbf353f80,0xbe95bec4,0xbf7f3f55,0x3ef1bf6d,0xbf463e48,0x3e48bf35,0xbf553ec4,0x3f7bbf35,0x3f0ebf6d,0x3f22bf6d,0xbe95be48,0x3dc93ec4,0x3f62bf0e,
0x3f6dbf80,0x3ec4bf35,0xbf6d3f80,0xbec43f35,0x3f7fbf6d,0x3f62bf7b,0xbf223ec4,0xbf753f55,0x3f55bf35,0x3f7bbf6d,0x3f0ebf35,0x3e48bec4,0x3ef1bec4,0x3f22bf0e,0x3f75bf6d,0x3f7fbf7b,
0x3f803f80,0x3f803f80,0x3f803f80,0x3f803f80,0xbdc90000,0xbe95be48,0xbf46bf35,0xbf62bf55,0xbf7bbf80,0xbf55bf6d,0x3e480000,0x3f0e3ec4,0x3e950000,0x3f463f0e,0x3ef13f35,0xbdc93e48,
0x3f6d3f80,0x3ec43f35,0xbf6dbf80,0xbec4bf35,0xbef10000,0xbf7fbf55,0x3f753f35,0x3f463f7b,0xbf55bf80,0x3e48bec4,0xbf0e0000,0xbf7bbf6d,0x3f220000,0x3f623f7b,0xbdc9bf35,0x3f753f0e,
0x3f353f80,0xbf350000,0x3f353f80,0xbf350000,0xbf460000,0xbef1bf7b,0xbf7fbf35,0x3e95bf0e,0xbf0ebf80,0x3f7b3ec4,0x3f550000,0x3e483f6d,0x3f620000,0xbdc93f55,0xbe953f35,0xbf22bf7b,
0x3ec43f80,0xbf6dbf35,0xbec4bf80,0x3f6d3f35,0xbf750000,0x3f22bf0e,0x3f623f35,0xbf7fbe48,0xbe48bf80,0x3f0e3f6d,0xbf7b0000,0x3f55bec4,0x3f7f0000,0xbf753e48,0x3f22bf35,0xbef13f55,
0x3f80,0xbf80,0x3f80,0xbf80,0xbf7f0000,0x3f753e48,0xbf22bf35,0x3ef13f55,0x3e48bf80,0xbf0e3f6d,0x3f7b0000,0xbf55bec4,0x3f750000,0xbf22bf0e,0xbf623f35,0x3f7fbe48,
0xbec43f80,0x3f6dbf35,0x3ec4bf80,0xbf6d3f35,0xbf620000,0x3dc93f55,0x3e953f35,0x3f22bf7b,0x3f0ebf80,0xbf7b3ec4,0xbf550000,0xbe483f6d,0x3f460000,0x3ef1bf7b,0x3f7fbf35,0xbe95bf0e,
0xbf353f80,0x3f350000,0xbf353f80,0x3f350000,0xbf220000,0xbf623f7b,0x3dc9bf35,0xbf753f0e,0x3f55bf80,0xbe48bec4,0x3f0e0000,0x3f7bbf6d,0x3ef10000,0x3f7fbf55,0xbf753f35,0xbf463f7b,
0xbf6d3f80,0xbec43f35,0x3f6dbf80,0x3ec4bf35,0xbe950000,0xbf463f0e,0xbef13f35,0x3dc93e48,0x3f7bbf80,0x3f55bf6d,0xbe480000,0xbf0e3ec4,0x3dc90000,0x3e95be48,0x3f46bf35,0x3f62bf55,
0x3f803f80,0x3f803f80,0x3f803f80,0x3f803f80,0xbef1bec4,0xbf22bf0e,0xbf75bf6d,0xbf7fbf7b,0xbf0ebf35,0xbe48bec4,0x3f553f35,0x3f7b3f6d,0x3f7f3f6d,0x3f623f7b,0xbf22bec4,0xbf75bf55,
0xbec40000,0xbf6dbf35,0x3ec40000,0x3f6d3f35,0xbf22bf6d,0x3e95be48,0xbdc93ec4,0xbf62bf0e,0x3f7b3f35,0x3f0e3f6d,0xbe48bf35,0x3f553ec4,0xbe953ec4,0xbf7fbf55,0x3ef13f6d,0xbf46be48,
0xbf35bf80,0x3f350000,0xbf35bf80,0x3f350000,0x3f753ec4,0x3dc93f55,0x3f623f6d,0xbf223e48,0xbe483f35,0xbf55bf6d,0xbf7bbf35,0x3f0ebec4,0xbf46bf6d,0x3f753e48,0x3f7f3ec4,0xbef13f0e,
0x3f6d0000,0xbec43f35,0xbf6d0000,0x3ec4bf35,0xbdc93f6d,0xbef1bf7b,0x3f46bec4,0xbe953f55,0xbf55bf35,0x3f7b3ec4,0xbf0e3f35,0x3e48bf6d,0x3f62bec4,0xbf463f0e,0x3e95bf6d,0xbdc93f7b,
0x3f80,0xbf80,0x3f80,0xbf80,0xbf62bec4,0x3f463f0e,0xbe95bf6d,0x3dc93f7b,0x3f55bf35,0xbf7b3ec4,0x3f0e3f35,0xbe48bf6d,0x3dc93f6d,0x3ef1bf7b,0xbf46bec4,0x3e953f55,
0xbf6d0000,0x3ec43f35,0x3f6d0000,0xbec4bf35,0x3f46bf6d,0xbf753e48,0xbf7f3ec4,0x3ef13f0e,0x3e483f35,0x3f55bf6d,0x3f7bbf35,0xbf0ebec4,0xbf753ec4,0xbdc93f55,0xbf623f6d,0x3f223e48,
0x3f35bf80,0xbf350000,0x3f35bf80,0xbf350000,0x3e953ec4,0x3f7fbf55,0xbef13f6d,0x3f46be48,0xbf7b3f35,0xbf0e3f6d,0x3e48bf35,0xbf553ec4,0x3f22bf6d,0xbe95be48,0x3dc93ec4,0x3f62bf0e,
0x3ec40000,0x3f6dbf35,0xbec40000,0xbf6d3f35,0xbf7f3f6d,0xbf623f7b,0x3f22bec4,0x3f75bf55,0x3f0ebf35,0x3e48bec4,0xbf553f35,0xbf7b3f6d,0x3ef1bec4,0x3f22bf0e,0x3f75bf6d,0x3f7fbf7b,
0x0,0x0,0x0,0x0,0x3f7f3f80,0x3f753f7b,0x3f223f35,0x3ef13f0e,0xbe480000,0xbf0ebec4,0xbf7bbf80,0xbf55bf6d,0xbf75bf80,0xbf22bf55,0x3f623f35,0x3f7f3f7b,
0x3ec40000,0x3f6d3f35,0xbec40000,0xbf6dbf35,0x3f623f80,0x3dc93f0e,0xbe95bf35,0x3f223e48,0xbf0e0000,0xbf7bbf6d,0x3f553f80,0xbe483ec4,0xbf46bf80,0x3ef1be48,0xbf7fbf35,0xbe95bf55,
0x3f350000,0x3f353f80,0x3f350000,0x3f353f80,0x3f223f80,0xbf62be48,0xbdc93f35,0xbf75bf55,0xbf550000,0xbe48bf6d,0xbf0ebf80,0x3f7b3ec4,0xbef1bf80,0x3f7f3f0e,0x3f753f35,0xbf463e48,
0x3f6d0000,0xbec43f35,0xbf6d0000,0x3ec4bf35,0x3e953f80,0xbf46bf55,0x3ef1bf35,0x3dc93f7b,0xbf7b0000,0x3f55bec4,0x3e483f80,0xbf0ebf6d,0xbdc9bf80,0x3e953f7b,0xbf46bf35,0x3f623f0e,
0x3f800000,0xbf800000,0x3f800000,0xbf800000,0xbdc93f80,0x3e95bf7b,0xbf463f35,0x3f62bf0e,0xbf7b0000,0x3f553ec4,0x3e48bf80,0xbf0e3f6d,0x3e95bf80,0xbf463f55,0x3ef13f35,0x3dc9bf7b,
0x3f6d0000,0xbec4bf35,0xbf6d0000,0x3ec43f35,0xbef13f80,0x3f7fbf0e,0x3f75bf35,0xbf46be48,0xbf550000,0xbe483f6d,0xbf0e3f80,0x3f7bbec4,0x3f22bf80,0xbf623e48,0xbdc9bf35,0xbf753f55,
0x3f350000,0x3f35bf80,0x3f350000,0x3f35bf80,0xbf463f80,0x3ef13e48,0xbf7f3f35,0xbe953f55,0xbf0e0000,0xbf7b3f6d,0x3f55bf80,0xbe48bec4,0x3f62bf80,0x3dc9bf0e,0xbe953f35,0x3f22be48,
0x3ec40000,0x3f6dbf35,0xbec40000,0xbf6d3f35,0xbf753f80,0xbf223f55,0x3f62bf35,0x3f7fbf7b,0xbe480000,0xbf0e3ec4,0xbf7b3f80,0xbf553f6d,0x3f7fbf80,0x3f75bf7b,0x3f22bf35,0x3ef1bf0e,
0x0,0x0,0x0,0x0,0x3f623f6d,0x3f463f55,0x3e953ec4,0x3dc93e48,0xbf55bf35,0xbf7bbf6d,0xbf0ebf35,0xbe48bec4,0xbdc9bec4,0x3ef13e48,0x3f463f6d,0x3e953f0e,
0x3f6d3f80,0x3ec43f35,0xbf6dbf80,0xbec4bf35,0xbf46bec4,0xbf75bf7b,0x3f7f3f6d,0x3ef13f55,0xbe48bf35,0x3f553ec4,0xbf7bbf35,0xbf0ebf6d,0x3f753f6d,0xbdc93f0e,0x3f623ec4,0x3f223f7b,
0xbf350000,0xbf35bf80,0xbf350000,0xbf35bf80,0xbe95bf6d,0x3f7f3f0e,0x3ef1bec4,0x3f463f7b,0x3f7b3f35,0xbf0e3ec4,0xbe483f35,0xbf55bf6d,0xbf223ec4,0xbe95bf7b,0xbdc9bf6d,0x3f623f55,
0xbec4bf80,0x3f6d3f35,0x3ec43f80,0xbf6dbf35,0x3f7f3ec4,0xbf623e48,0xbf22bf6d,0x3f753f0e,0xbf0e3f35,0x3e48bf6d,0x3f553f35,0xbf7bbec4,0xbef1bf6d,0x3f223f55,0xbf75bec4,0x3f7f3e48,
0x3f800000,0xbf800000,0x3f800000,0xbf800000,0xbef13f6d,0x3f22bf55,0xbf753ec4,0x3f7fbe48,0xbf0ebf35,0x3e483f6d,0x3f55bf35,0xbf7b3ec4,0x3f7fbec4,0xbf62be48,0xbf223f6d,0x3f75bf0e,
0xbec43f80,0x3f6dbf35,0x3ec4bf80,0xbf6d3f35,0xbf22bec4,0xbe953f7b,0xbdc93f6d,0x3f62bf55,0x3f7bbf35,0xbf0ebec4,0xbe48bf35,0xbf553f6d,0xbe953f6d,0x3f7fbf0e,0x3ef13ec4,0x3f46bf7b,
0xbf350000,0xbf353f80,0xbf350000,0xbf353f80,0x3f75bf6d,0xbdc9bf0e,0x3f62bec4,0x3f22bf7b,0xbe483f35,0x3f55bec4,0xbf7b3f35,0xbf0e3f6d,0xbf463ec4,0xbf753f7b,0x3f7fbf6d,0x3ef1bf55,
0x3f6dbf80,0x3ec4bf35,0xbf6d3f80,0xbec43f35,0xbdc93ec4,0x3ef1be48,0x3f46bf6d,0x3e95bf0e,0xbf553f35,0xbf7b3f6d,0xbf0e3f35,0xbe483ec4,0x3f62bf6d,0x3f46bf55,0x3e95bec4,0x3dc9be48,
0x3f803f80,0x3f803f80,0x3f803f80,0x3f803f80,0x3f7b3f80,0x3f553f6d,0xbe480000,0xbf0ebec4,0x3f6d3f80,0x3ec43f35,0xbf6dbf80,0xbec4bf35,0x3f553f80,0xbe483ec4,0x3f0e0000,0x3f7b3f6d,
0x3f353f80,0xbf350000,0x3f353f80,0xbf350000,0x3f0e3f80,0xbf7bbec4,0xbf550000,0xbe48bf6d,0x3ec43f80,0xbf6dbf35,0xbec4bf80,0x3f6d3f35,0x3e483f80,0xbf0ebf6d,0x3f7b0000,0xbf553ec4,
0x3f80,0xbf80,0x3f80,0xbf80,0xbe483f80,0x3f0ebf6d,0xbf7b0000,0x3f553ec4,0xbec43f80,0x3f6dbf35,0x3ec4bf80,0xbf6d3f35,0xbf0e3f80,0x3f7bbec4,0x3f550000,0x3e48bf6d,
0xbf353f80,0x3f350000,0xbf353f80,0x3f350000,0xbf553f80,0x3e483ec4,0xbf0e0000,0xbf7b3f6d,0xbf6d3f80,0xbec43f35,0x3f6dbf80,0x3ec4bf35,0xbf7b3f80,0xbf553f6d,0x3e480000,0x3f0ebec4,
0xbf803f80,0xbf803f80,0xbf803f80,0xbf803f80,0xbf7b3f80,0xbf553f6d,0x3e480000,0x3f0ebec4,0xbf6d3f80,0xbec43f35,0x3f6dbf80,0x3ec4bf35,0xbf553f80,0x3e483ec4,0xbf0e0000,0xbf7b3f6d,
0xbf353f80,0x3f350000,0xbf353f80,0x3f350000,0xbf0e3f80,0x3f7bbec4,0x3f550000,0x3e48bf6d,0xbec43f80,0x3f6dbf35,0x3ec4bf80,0xbf6d3f35,0xbe483f80,0x3f0ebf6d,0xbf7b0000,0x3f553ec4,
0x3f80,0xbf80,0x3f80,0xbf80,0x3e483f80,0xbf0ebf6d,0x3f7b0000,0xbf553ec4,0x3ec43f80,0xbf6dbf35,0xbec4bf80,0x3f6d3f35,0x3f0e3f80,0xbf7bbec4,0xbf550000,0xbe48bf6d,
0x3f353f80,0xbf350000,0x3f353f80,0xbf350000,0x3f553f80,0xbe483ec4,0x3f0e0000,0x3f7b3f6d,0x3f6d3f80,0x3ec43f35,0xbf6dbf80,0xbec4bf35,0x3f7b3f80,0x3f553f6d,0xbe480000,0xbf0ebec4,
0x3f803f80,0x3f803f80,0x3f803f80,0x3f803f80,0x3f0e3f35,0x3e483ec4,0xbf55bf35,0xbf7bbf6d,0xbec40000,0xbf6dbf35,0x3ec40000,0x3f6d3f35,0xbf7bbf35,0xbf0ebf6d,0x3e483f35,0xbf55bec4,
0xbf35bf80,0x3f350000,0xbf35bf80,0x3f350000,0x3e48bf35,0x3f553f6d,0x3f7b3f35,0xbf0e3ec4,0x3f6d0000,0xbec43f35,0xbf6d0000,0x3ec4bf35,0x3f553f35,0xbf7bbec4,0x3f0ebf35,0xbe483f6d,
0x3f80,0xbf80,0x3f80,0xbf80,0xbf553f35,0x3f7bbec4,0xbf0ebf35,0x3e483f6d,0xbf6d0000,0x3ec43f35,0x3f6d0000,0xbec4bf35,0xbe48bf35,0xbf553f6d,0xbf7b3f35,0x3f0e3ec4,
0x3f35bf80,0xbf350000,0x3f35bf80,0xbf350000,0x3f7bbf35,0x3f0ebf6d,0xbe483f35,0x3f55bec4,0x3ec40000,0x3f6dbf35,0xbec40000,0xbf6d3f35,0xbf0e3f35,0xbe483ec4,0x3f55bf35,0x3f7bbf6d,
0xbf803f80,0xbf803f80,0xbf803f80,0xbf803f80,0xbf0e3f35,0xbe483ec4,0x3f55bf35,0x3f7bbf6d,0x3ec40000,0x3f6dbf35,0xbec40000,0xbf6d3f35,0x3f7bbf35,0x3f0ebf6d,0xbe483f35,0x3f55bec4,
0x3f35bf80,0xbf350000,0x3f35bf80,0xbf350000,0xbe48bf35,0xbf553f6d,0xbf7b3f35,0x3f0e3ec4,0xbf6d0000,0x3ec43f35,0x3f6d0000,0xbec4bf35,0xbf553f35,0x3f7bbec4,0xbf0ebf35,0x3e483f6d,
0x3f80,0xbf80,0x3f80,0xbf80,0x3f553f35,0xbf7bbec4,0x3f0ebf35,0xbe483f6d,0x3f6d0000,0xbec43f35,0xbf6d0000,0x3ec4bf35,0x3e48bf35,0x3f553f6d,0x3f7b3f35,0xbf0e3ec4,
0xbf35bf80,0x3f350000,0xbf35bf80,0x3f350000,0xbf7bbf35,0xbf0ebf6d,0x3e483f35,0xbf55bec4,0xbec40000,0xbf6dbf35,0x3ec40000,0x3f6d3f35,0x3f0e3f35,0x3e483ec4,0xbf55bf35,0xbf7bbf6d,
0x0,0x0,0x0,0x0,0x3e480000,0x3f0e3ec4,0x3f7b3f80,0x3f553f6d,0x3ec40000,0x3f6d3f35,0xbec40000,0xbf6dbf35,0x3f0e0000,0x3f7b3f6d,0xbf55bf80,0x3e48bec4,
0x3f350000,0x3f353f80,0x3f350000,0x3f353f80,0x3f550000,0x3e483f6d,0x3f0e3f80,0xbf7bbec4,0x3f6d0000,0xbec43f35,0xbf6d0000,0x3ec4bf35,0x3f7b0000,0xbf553ec4,0xbe48bf80,0x3f0e3f6d,
0x3f800000,0xbf800000,0x3f800000,0xbf800000,0x3f7b0000,0xbf55bec4,0xbe483f80,0x3f0ebf6d,0x3f6d0000,0xbec4bf35,0xbf6d0000,0x3ec43f35,0x3f550000,0x3e48bf6d,0x3f0ebf80,0xbf7b3ec4,
0x3f350000,0x3f35bf80,0x3f350000,0x3f35bf80,0x3f0e0000,0x3f7bbf6d,0xbf553f80,0x3e483ec4,0x3ec40000,0x3f6dbf35,0xbec40000,0xbf6d3f35,0x3e480000,0x3f0ebec4,0x3f7bbf80,0x3f55bf6d,
0x0,0x0,0x0,0x0,0xbe480000,0xbf0e3ec4,0xbf7b3f80,0xbf553f6d,0xbec40000,0xbf6d3f35,0x3ec40000,0x3f6dbf35,0xbf0e0000,0xbf7b3f6d,0x3f55bf80,0xbe48bec4,
0xbf350000,0xbf353f80,0xbf350000,0xbf353f80,0xbf550000,0xbe483f6d,0xbf0e3f80,0x3f7bbec4,0xbf6d0000,0x3ec43f35,0x3f6d0000,0xbec4bf35,0xbf7b0000,0x3f553ec4,0x3e48bf80,0xbf0e3f6d,
0xbf800000,0x3f800000,0xbf800000,0x3f800000,0xbf7b0000,0x3f55bec4,0x3e483f80,0xbf0ebf6d,0xbf6d0000,0x3ec4bf35,0x3f6d0000,0xbec43f35,0xbf550000,0xbe48bf6d,0xbf0ebf80,0x3f7b3ec4,
0xbf350000,0xbf35bf80,0xbf350000,0xbf35bf80,0xbf0e0000,0xbf7bbf6d,0x3f553f80,0xbe483ec4,0xbec40000,0xbf6dbf35,0x3ec40000,0x3f6d3f35,0xbe480000,0xbf0ebec4,0xbf7bbf80,0xbf55bf6d,
0x0,0x0,0x0,0x0,0x3f553f35,0x3f7b3f6d,0x3f0e3f35,0x3e483ec4,0x3f6d3f80,0x3ec43f35,0xbf6dbf80,0xbec4bf35,0x3e483f35,0xbf55bec4,0x3f7b3f35,0x3f0e3f6d,
0xbf350000,0xbf35bf80,0xbf350000,0xbf35bf80,0xbf7bbf35,0x3f0ebec4,0x3e48bf35,0x3f553f6d,0xbec4bf80,0x3f6d3f35,0x3ec43f80,0xbf6dbf35,0x3f0ebf35,0xbe483f6d,0xbf55bf35,0x3f7b3ec4,
0x3f800000,0xbf800000,0x3f800000,0xbf800000,0x3f0e3f35,0xbe48bf6d,0xbf553f35,0x3f7bbec4,0xbec43f80,0x3f6dbf35,0x3ec4bf80,0xbf6d3f35,0xbf7b3f35,0x3f0e3ec4,0x3e483f35,0x3f55bf6d,
0xbf350000,0xbf353f80,0xbf350000,0xbf353f80,0x3e48bf35,0xbf553ec4,0x3f7bbf35,0x3f0ebf6d,0x3f6dbf80,0x3ec4bf35,0xbf6d3f80,0xbec43f35,0x3f55bf35,0x3f7bbf6d,0x3f0ebf35,0x3e48bec4,
0x0,0x0,0x0,0x0,0xbf553f35,0xbf7b3f6d,0xbf0e3f35,0xbe483ec4,0xbf6d3f80,0xbec43f35,0x3f6dbf80,0x3ec4bf35,0xbe483f35,0x3f55bec4,0xbf7b3f35,0xbf0e3f6d,
0x3f350000,0x3f35bf80,0x3f350000,0x3f35bf80,0x3f7bbf35,0xbf0ebec4,0xbe48bf35,0xbf553f6d,0x3ec4bf80,0xbf6d3f35,0xbec43f80,0x3f6dbf35,0xbf0ebf35,0x3e483f6d,0x3f55bf35,0xbf7b3ec4,
0xbf800000,0x3f800000,0xbf800000,0x3f800000,0xbf0e3f35,0x3e48bf6d,0x3f553f35,0xbf7bbec4,0x3ec43f80,0xbf6dbf35,0xbec4bf80,0x3f6d3f35,0x3f7b3f35,0xbf0e3ec4,0xbe483f35,0xbf55bf6d,
0x3f350000,0x3f353f80,0x3f350000,0x3f353f80,0xbe48bf35,0x3f553ec4,0xbf7bbf35,0xbf0ebf6d,0xbf6dbf80,0xbec4bf35,0x3f6d3f80,0x3ec43f35,0xbf55bf35,0xbf7bbf6d,0xbf0ebf35,0xbe48bec4,
0x3f803f80,0x3f803f80,0x3f803f80,0x3f803f80,0xbf7bbf80,0xbf55bf6d,0x3e480000,0x3f0e3ec4,0x3f6d3f80,0x3ec43f35,0xbf6dbf80,0xbec4bf35,0xbf55bf80,0x3e48bec4,0xbf0e0000,0xbf7bbf6d,
0x3f353f80,0xbf350000,0x3f353f80,0xbf350000,0xbf0ebf80,0x3f7b3ec4,0x3f550000,0x3e483f6d,0x3ec43f80,0xbf6dbf35,0xbec4bf80,0x3f6d3f35,0xbe48bf80,0x3f0e3f6d,0xbf7b0000,0x3f55bec4,
0x3f80,0xbf80,0x3f80,0xbf80,0x3e48bf80,0xbf0e3f6d,0x3f7b0000,0xbf55bec4,0xbec43f80,0x3f6dbf35,0x3ec4bf80,0xbf6d3f35,0x3f0ebf80,0xbf7b3ec4,0xbf550000,0xbe483f6d,
0xbf353f80,0x3f350000,0xbf353f80,0x3f350000,0x3f55bf80,0xbe48bec4,0x3f0e0000,0x3f7bbf6d,0xbf6d3f80,0xbec43f35,0x3f6dbf80,0x3ec4bf35,0x3f7bbf80,0x3f55bf6d,0xbe480000,0xbf0e3ec4,
0xbf803f80,0xbf803f80,0xbf803f80,0xbf803f80,0x3f7bbf80,0x3f55bf6d,0xbe480000,0xbf0e3ec4,0xbf6d3f80,0xbec43f35,0x3f6dbf80,0x3ec4bf35,0x3f55bf80,0xbe48bec4,0x3f0e0000,0x3f7bbf6d,
0xbf353f80,0x3f350000,0xbf353f80,0x3f350000,0x3f0ebf80,0xbf7b3ec4,0xbf550000,0xbe483f6d,0xbec43f80,0x3f6dbf35,0x3ec4bf80,0xbf6d3f35,0x3e48bf80,0xbf0e3f6d,0x3f7b0000,0xbf55bec4,
0x3f80,0xbf80,0x3f80,0xbf80,0xbe48bf80,0x3f0e3f6d,0xbf7b0000,0x3f55bec4,0x3ec43f80,0xbf6dbf35,0xbec4bf80,0x3f6d3f35,0xbf0ebf80,0x3f7b3ec4,0x3f550000,0x3e483f6d,
0x3f353f80,0xbf350000,0x3f353f80,0xbf350000,0xbf55bf80,0x3e48bec4,0xbf0e0000,0xbf7bbf6d,0x3f6d3f80,0x3ec43f35,0xbf6dbf80,0xbec4bf35,0xbf7bbf80,0xbf55bf6d,0x3e480000,0x3f0e3ec4,
0x3f803f80,0x3f803f80,0x3f803f80,0x3f803f80,0xbf0ebf35,0xbe48bec4,0x3f553f35,0x3f7b3f6d,0xbec40000,0xbf6dbf35,0x3ec40000,0x3f6d3f35,0x3f7b3f35,0x3f0e3f6d,0xbe48bf35,0x3f553ec4,
0xbf35bf80,0x3f350000,0xbf35bf80,0x3f350000,0xbe483f35,0xbf55bf6d,0xbf7bbf35,0x3f0ebec4,0x3f6d0000,0xbec43f35,0xbf6d0000,0x3ec4bf35,0xbf55bf35,0x3f7b3ec4,0xbf0e3f35,0x3e48bf6d,
0x3f80,0xbf80,0x3f80,0xbf80,0x3f55bf35,0xbf7b3ec4,0x3f0e3f35,0xbe48bf6d,0xbf6d0000,0x3ec43f35,0x3f6d0000,0xbec4bf35,0x3e483f35,0x3f55bf6d,0x3f7bbf35,0xbf0ebec4,
0x3f35bf80,0xbf350000,0x3f35bf80,0xbf350000,0xbf7b3f35,0xbf0e3f6d,0x3e48bf35,0xbf553ec4,0x3ec40000,0x3f6dbf35,0xbec40000,0xbf6d3f35,0x3f0ebf35,0x3e48bec4,0xbf553f35,0xbf7b3f6d,
0xbf803f80,0xbf803f80,0xbf803f80,0xbf803f80,0x3f0ebf35,0x3e48bec4,0xbf553f35,0xbf7b3f6d,0x3ec40000,0x3f6dbf35,0xbec40000,0xbf6d3f35,0xbf7b3f35,0xbf0e3f6d,0x3e48bf35,0xbf553ec4,
0x3f35bf80,0xbf350000,0x3f35bf80,0xbf350000,0x3e483f35,0x3f55bf6d,0x3f7bbf35,0xbf0ebec4,0xbf6d0000,0x3ec43f35,0x3f6d0000,0xbec4bf35,0x3f55bf35,0xbf7b3ec4,0x3f0e3f35,0xbe48bf6d,
0x3f80,0xbf80,0x3f80,0xbf80,0xbf55bf35,0x3f7b3ec4,0xbf0e3f35,0x3e48bf6d,0x3f6d0000,0xbec43f35,0xbf6d0000,0x3ec4bf35,0xbe483f35,0xbf55bf6d,0xbf7bbf35,0x3f0ebec4,
0xbf35bf80,0x3f350000,0xbf35bf80,0x3f350000,0x3f7b3f35,0x3f0e3f6d,0xbe48bf35,0x3f553ec4,0xbec40000,0xbf6dbf35,0x3ec40000,0x3f6d3f35,0xbf0ebf35,0xbe48bec4,0x3f553f35,0x3f7b3f6d,
0x0,0x0,0x0,0x0,0xbe480000,0xbf0ebec4,0xbf7bbf80,0xbf55bf6d,0x3ec40000,0x3f6d3f35,0xbec40000,0xbf6dbf35,0xbf0e0000,0xbf7bbf6d,0x3f553f80,0xbe483ec4,
0x3f350000,0x3f353f80,0x3f350000,0x3f353f80,0xbf550000,0xbe48bf6d,0xbf0ebf80,0x3f7b3ec4,0x3f6d0000,0xbec43f35,0xbf6d0000,0x3ec4bf35,0xbf7b0000,0x3f55bec4,0x3e483f80,0xbf0ebf6d,
0x3f800000,0xbf800000,0x3f800000,0xbf800000,0xbf7b0000,0x3f553ec4,0x3e48bf80,0xbf0e3f6d,0x3f6d0000,0xbec4bf35,0xbf6d0000,0x3ec43f35,0xbf550000,0xbe483f6d,0xbf0e3f80,0x3f7bbec4,
0x3f350000,0x3f35bf80,0x3f350000,0x3f35bf80,0xbf0e0000,0xbf7b3f6d,0x3f55bf80,0xbe48bec4,0x3ec40000,0x3f6dbf35,0xbec40000,0xbf6d3f35,0xbe480000,0xbf0e3ec4,0xbf7b3f80,0xbf553f6d,
0x0,0x0,0x0,0x0,0x3e480000,0x3f0ebec4,0x3f7bbf80,0x3f55bf6d,0xbec40000,0xbf6d3f35,0x3ec40000,0x3f6dbf35,0x3f0e0000,0x3f7bbf6d,0xbf553f80,0x3e483ec4,
0xbf350000,0xbf353f80,0xbf350000,0xbf353f80,0x3f550000,0x3e48bf6d,0x3f0ebf80,0xbf7b3ec4,0xbf6d0000,0x3ec43f35,0x3f6d0000,0xbec4bf35,0x3f7b0000,0xbf55bec4,0xbe483f80,0x3f0ebf6d,
0xbf800000,0x3f800000,0xbf800000,0x3f800000,0x3f7b0000,0xbf553ec4,0xbe48bf80,0x3f0e3f6d,0xbf6d0000,0x3ec4bf35,0x3f6d0000,0xbec43f35,0x3f550000,0x3e483f6d,0x3f0e3f80,0xbf7bbec4,
0xbf350000,0xbf35bf80,0xbf350000,0xbf35bf80,0x3f0e0000,0x3f7b3f6d,0xbf55bf80,0x3e48bec4,0xbec40000,0xbf6dbf35,0x3ec40000,0x3f6d3f35,0x3e480000,0x3f0e3ec4,0x3f7b3f80,0x3f553f6d,
0x0,0x0,0x0,0x0,0xbf55bf35,0xbf7bbf6d,0xbf0ebf35,0xbe48bec4,0x3f6d3f80,0x3ec43f35,0xbf6dbf80,0xbec4bf35,0xbe48bf35,0x3f553ec4,0xbf7bbf35,0xbf0ebf6d,
0xbf350000,0xbf35bf80,0xbf350000,0xbf35bf80,0x3f7b3f35,0xbf0e3ec4,0xbe483f35,0xbf55bf6d,0xbec4bf80,0x3f6d3f35,0x3ec43f80,0xbf6dbf35,0xbf0e3f35,0x3e48bf6d,0x3f553f35,0xbf7bbec4,
0x3f800000,0xbf800000,0x3f800000,0xbf800000,0xbf0ebf35,0x3e483f6d,0x3f55bf35,0xbf7b3ec4,0xbec43f80,0x3f6dbf35,0x3ec4bf80,0xbf6d3f35,0x3f7bbf35,0xbf0ebec4,0xbe48bf35,0xbf553f6d,
0xbf350000,0xbf353f80,0xbf350000,0xbf353f80,0xbe483f35,0x3f55bec4,0xbf7b3f35,0xbf0e3f6d,0x3f6dbf80,0x3ec4bf35,0xbf6d3f80,0xbec43f35,0xbf553f35,0xbf7b3f6d,0xbf0e3f35,0xbe483ec4,
0x0,0x0,0x0,0x0,0x3f55bf35,0x3f7bbf6d,0x3f0ebf35,0x3e48bec4,0xbf6d3f80,0xbec43f35,0x3f6dbf80,0x3ec4bf35,0x3e48bf35,0xbf553ec4,0x3f7bbf35,0x3f0ebf6d,
0x3f350000,0x3f35bf80,0x3f350000,0x3f35bf80,0xbf7b3f35,0x3f0e3ec4,0x3e483f35,0x3f55bf6d,0x3ec4bf80,0xbf6d3f35,0xbec43f80,0x3f6dbf35,0x3f0e3f35,0xbe48bf6d,0xbf553f35,0x3f7bbec4,
0xbf800000,0x3f800000,0xbf800000,0x3f800000,0x3f0ebf35,0xbe483f6d,0xbf55bf35,0x3f7b3ec4,0x3ec43f80,0xbf6dbf35,0xbec4bf80,0x3f6d3f35,0xbf7bbf35,0x3f0ebec4,0x3e48bf35,0x3f553f6d,
0x3f350000,0x3f353f80,0x3f350000,0x3f353f80,0x3e483f35,0xbf55bec4,0x3f7b3f35,0x3f0e3f6d,0xbf6dbf80,0xbec4bf35,0x3f6d3f80,0x3ec43f35,0x3f553f35,0x3f7b3f6d,0x3f0e3f35,0x3e483ec4,
0x35803580,0x35803580,0x35803580,0x35803580,0x357b3580,0x3555356d,0xb4480000,0xb50eb4c4,0x356d3580,0x34c43535,0xb56db580,0xb4c4b535,0x35553580,0xb44834c4,0x350e0000,0x357b356d,
0x35353580,0xb5350000,0x35353580,0xb5350000,0x350e3580,0xb57bb4c4,0xb5550000,0xb448b56d,0x34c43580,0xb56db535,0xb4c4b580,0x356d3535,0x34483580,0xb50eb56d,0x357b0000,0xb55534c4,
0x3580,0xb580,0x3580,0xb580,0xb4483580,0x350eb56d,0xb57b0000,0x355534c4,0xb4c43580,0x356db535,0x34c4b580,0xb56d3535,0xb50e3580,0x357bb4c4,0x35550000,0x3448b56d,
0xb5353580,0x35350000,0xb5353580,0x35350000,0xb5553580,0x344834c4,0xb50e0000,0xb57b356d,0xb56d3580,0xb4c43535,0x356db580,0x34c4b535,0xb57b3580,0xb555356d,0x34480000,0x350eb4c4,
0xb5803580,0xb5803580,0xb5803580,0xb5803580,0xb57b3580,0xb555356d,0x34480000,0x350eb4c4,0xb56d3580,0xb4c43535,0x356db580,0x34c4b535,0xb5553580,0x344834c4,0xb50e0000,0xb57b356d,
0xb5353580,0x35350000,0xb5353580,0x35350000,0xb50e3580,0x357bb4c4,0x35550000,0x3448b56d,0xb4c43580,0x356db535,0x34c4b580,0xb56d3535,0xb4483580,0x350eb56d,0xb57b0000,0x355534c4,
0x3580,0xb580,0x3580,0xb580,0x34483580,0xb50eb56d,0x357b0000,0xb55534c4,0x34c43580,0xb56db535,0xb4c4b580,0x356d3535,0x350e3580,0xb57bb4c4,0xb5550000,0xb448b56d,
0x35353580,0xb5350000,0x35353580,0xb5350000,0x35553580,0xb44834c4,0x350e0000,0x357b356d,0x356d3580,0x34c43535,0xb56db580,0xb4c4b535,0x357b3580,0x3555356d,0xb4480000,0xb50eb4c4,
0x35803580,0x35803580,0x35803580,0x35803580,0x350e3535,0x344834c4,0xb555b535,0xb57bb56d,0xb4c40000,0xb56db535,0x34c40000,0x356d3535,0xb57bb535,0xb50eb56d,0x34483535,0xb555b4c4,
0xb535b580,0x35350000,0xb535b580,0x35350000,0x3448b535,0x3555356d,0x357b3535,0xb50e34c4,0x356d0000,0xb4c43535,0xb56d0000,0x34c4b535,0x35553535,0xb57bb4c4,0x350eb535,0xb448356d,
0x3580,0xb580,0x3580,0xb580,0xb5553535,0x357bb4c4,0xb50eb535,0x3448356d,0xb56d0000,0x34c43535,0x356d0000,0xb4c4b535,0xb448b535,0xb555356d,0xb57b3535,0x350e34c4,
0x3535b580,0xb5350000,0x3535b580,0xb5350000,0x357bb535,0x350eb56d,0xb4483535,0x3555b4c4,0x34c40000,0x356db535,0xb4c40000,0xb56d3535,0xb50e3535,0xb44834c4,0x3555b535,0x357bb56d,
0xb5803580,0xb5803580,0xb5803580,0xb5803580,0xb50e3535,0xb44834c4,0x3555b535,0x357bb56d,0x34c40000,0x356db535,0xb4c40000,0xb56d3535,0x357bb535,0x350eb56d,0xb4483535,0x3555b4c4,
0x3535b580,0xb5350000,0x3535b580,0xb5350000,0xb448b535,0xb555356d,0xb57b3535,0x350e34c4,0xb56d0000,0x34c43535,0x356d0000,0xb4c4b535,0xb5553535,0x357bb4c4,0xb50eb535,0x3448356d,
0x3580,0xb580,0x3580,0xb580,0x35553535,0xb57bb4c4,0x350eb535,0xb448356d,0x356d0000,0xb4c43535,0xb56d0000,0x34c4b535,0x3448b535,0x3555356d,0x357b3535,0xb50e34c4,
0xb535b580,0x35350000,0xb535b580,0x35350000,0xb57bb535,0xb50eb56d,0x34483535,0xb555b4c4,0xb4c40000,0xb56db535,0x34c40000,0x356d3535,0x350e3535,0x344834c4,0xb555b535,0xb57bb56d,
0x0,0x0,0x0,0x0,0x34480000,0x350e34c4,0x357b3580,0x3555356d,0x34c40000,0x356d3535,0xb4c40000,0xb56db535,0x350e0000,0x357b356d,0xb555b580,0x3448b4c4,
0x35350000,0x35353580,0x35350000,0x35353580,0x35550000,0x3448356d,0x350e3580,0xb57bb4c4,0x356d0000,0xb4c43535,0xb56d0000,0x34c4b535,0x357b0000,0xb55534c4,0xb448b580,0x350e356d,
0x35800000,0xb5800000,0x35800000,0xb5800000,0x357b0000,0xb555b4c4,0xb4483580,0x350eb56d,0x356d0000,0xb4c4b535,0xb56d0000,0x34c43535,0x35550000,0x3448b56d,0x350eb580,0xb57b34c4,
0x35350000,0x3535b580,0x35350000,0x3535b580,0x350e0000,0x357bb56d,0xb5553580,0x344834c4,0x34c40000,0x356db535,0xb4c40000,0xb56d3535,0x34480000,0x350eb4c4,0x357bb580,0x3555b56d,
0x0,0x0,0x0,0x0,0xb4480000,0xb50e34c4,0xb57b3580,0xb555356d,0xb4c40000,0xb56d3535,0x34c40000,0x356db535,0xb50e0000,0xb57b356d,0x3555b580,0xb448b4c4,
0xb5350000,0xb5353580,0xb5350000,0xb5353580,0xb5550000,0xb448356d,0xb50e3580,0x357bb4c4,0xb56d0000,0x34c43535,0x356d0000,0xb4c4b535,0xb57b0000,0x355534c4,0x3448b580,0xb50e356d,
0xb5800000,0x35800000,0xb5800000,0x35800000,0xb57b0000,0x3555b4c4,0x34483580,0xb50eb56d,0xb56d0000,0x34c4b535,0x356d0000,0xb4c43535,0xb5550000,0xb448b56d,0xb50eb580,0x357b34c4,
0xb5350000,0xb535b580,0xb5350000,0xb535b580,0xb50e0000,0xb57bb56d,0x35553580,0xb44834c4,0xb4c40000,0xb56db535,0x34c40000,0x356d3535,0xb4480000,0xb50eb4c4,0xb57bb580,0xb555b56d,
0x0,0x0,0x0,0x0,0x35553535,0x357b356d,0x350e3535,0x344834c4,0x356d3580,0x34c43535,0xb56db580,0xb4c4b535,0x34483535,0xb555b4c4,0x357b3535,0x350e356d,
0xb5350000,0xb535b580,0xb5350000,0xb535b580,0xb57bb535,0x350eb4c4,0x3448b535,0x3555356d,0xb4c4b580,0x356d3535,0x34c43580,0xb56db535,0x350eb535,0xb448356d,0xb555b535,0x357b34c4,
0x35800000,0xb5800000,0x35800000,0xb5800000,0x350e3535,0xb448b56d,0xb5553535,0x357bb4c4,0xb4c43580,0x356db535,0x34c4b580,0xb56d3535,0xb57b3535,0x350e34c4,0x34483535,0x3555b56d,
0xb5350000,0xb5353580,0xb5350000,0xb5353580,0x3448b535,0xb55534c4,0x357bb535,0x350eb56d,0x356db580,0x34c4b535,0xb56d3580,0xb4c43535,0x3555b535,0x357bb56d,0x350eb535,0x3448b4c4,
0x0,0x0,0x0,0x0,0xb5553535,0xb57b356d,0xb50e3535,0xb44834c4,0xb56d3580,0xb4c43535,0x356db580,0x34c4b535,0xb4483535,0x3555b4c4,0xb57b3535,0xb50e356d,
0x35350000,0x3535b580,0x35350000,0x3535b580,0x357bb535,0xb50eb4c4,0xb448b535,0xb555356d,0x34c4b580,0xb56d3535,0xb4c43580,0x356db535,0xb50eb535,0x3448356d,0x3555b535,0xb57b34c4,
0xb5800000,0x35800000,0xb5800000,0x35800000,0xb50e3535,0x3448b56d,0x35553535,0xb57bb4c4,0x34c43580,0xb56db535,0xb4c4b580,0x356d3535,0x357b3535,0xb50e34c4,0xb4483535,0xb555b56d,
0x35350000,0x35353580,0x35350000,0x35353580,0xb448b535,0x355534c4,0xb57bb535,0xb50eb56d,0xb56db580,0xb4c4b535,0x356d3580,0x34c43535,0xb555b535,0xb57bb56d,0xb50eb535,0xb448b4c4,
0xbf803f80,0xbf803f80,0xbf803f80,0xbf803f80,0xbf7f3f80,0xbf753f7b,0xbf223f35,0xbef13f0e,0xbf7b3f80,0xbf553f6d,0x3e480000,0x3f0ebec4,0xbf753f80,0xbf223f55,0x3f62bf35,0x3f7fbf7b,
0xbf6d3f80,0xbec43f35,0x3f6dbf80,0x3ec4bf35,0xbf623f80,0xbdc93f0e,0x3e95bf35,0xbf223e48,0xbf553f80,0x3e483ec4,0xbf0e0000,0xbf7b3f6d,0xbf463f80,0x3ef13e48,0xbf7f3f35,0xbe953f55,
0xbf353f80,0x3f350000,0xbf353f80,0x3f350000,0xbf223f80,0x3f62be48,0x3dc93f35,0x3f75bf55,0xbf0e3f80,0x3f7bbec4,0x3f550000,0x3e48bf6d,0xbef13f80,0x3f7fbf0e,0x3f75bf35,0xbf46be48,
0xbec43f80,0x3f6dbf35,0x3ec4bf80,0xbf6d3f35,0xbe953f80,0x3f46bf55,0xbef1bf35,0xbdc93f7b,0xbe483f80,0x3f0ebf6d,0xbf7b0000,0x3f553ec4,0xbdc93f80,0x3e95bf7b,0xbf463f35,0x3f62bf0e,
0x3f80,0xbf80,0x3f80,0xbf80,0x3dc93f80,0xbe95bf7b,0x3f463f35,0xbf62bf0e,0x3e483f80,0xbf0ebf6d,0x3f7b0000,0xbf553ec4,0x3e953f80,0xbf46bf55,0x3ef1bf35,0x3dc93f7b,
0x3ec43f80,0xbf6dbf35,0xbec4bf80,0x3f6d3f35,0x3ef13f80,0xbf7fbf0e,0xbf75bf35,0x3f46be48,0x3f0e3f80,0xbf7bbec4,0xbf550000,0xbe48bf6d,0x3f223f80,0xbf62be48,0xbdc93f35,0xbf75bf55,
0x3f353f80,0xbf350000,0x3f353f80,0xbf350000,0x3f463f80,0xbef13e48,0x3f7f3f35,0x3e953f55,0x3f553f80,0xbe483ec4,0x3f0e0000,0x3f7b3f6d,0x3f623f80,0x3dc93f0e,0xbe95bf35,0x3f223e48,
0x3f6d3f80,0x3ec43f35,0xbf6dbf80,0xbec4bf35,0x3f753f80,0x3f223f55,0xbf62bf35,0xbf7fbf7b,0x3f7b3f80,0x3f553f6d,0xbe480000,0xbf0ebec4,0x3f7f3f80,0x3f753f7b,0x3f223f35,0x3ef13f0e,
0xbf803f80,0xbf803f80,0xbf803f80,0xbf803f80,0xbf623f6d,0xbf463f55,0xbe953ec4,0xbdc93e48,0xbf0e3f35,0xbe483ec4,0x3f55bf35,0x3f7bbf6d,0xbdc93ec4,0x3ef1be48,0x3f46bf6d,0x3e95bf0e,
0x3ec40000,0x3f6dbf35,0xbec40000,0xbf6d3f35,0x3f46bec4,0x3f75bf7b,0xbf7f3f6d,0xbef13f55,0x3f7bbf35,0x3f0ebf6d,0xbe483f35,0x3f55bec4,0x3f75bf6d,0xbdc9bf0e,0x3f62bec4,0x3f22bf7b,
0x3f35bf80,0xbf350000,0x3f35bf80,0xbf350000,0x3e95bf6d,0xbf7f3f0e,0xbef1bec4,0xbf463f7b,0xbe48bf35,0xbf553f6d,0xbf7b3f35,0x3f0e3ec4,0xbf22bec4,0xbe953f7b,0xbdc93f6d,0x3f62bf55,
0xbf6d0000,0x3ec43f35,0x3f6d0000,0xbec4bf35,0xbf7f3ec4,0x3f623e48,0x3f22bf6d,0xbf753f0e,0xbf553f35,0x3f7bbec4,0xbf0ebf35,0x3e483f6d,0xbef13f6d,0x3f22bf55,0xbf753ec4,0x3f7fbe48,
0x3f80,0xbf80,0x3f80,0xbf80,0x3ef13f6d,0xbf22bf55,0x3f753ec4,0xbf7fbe48,0x3f553f35,0xbf7bbec4,0x3f0ebf35,0xbe483f6d,0x3f7f3ec4,0xbf623e48,0xbf22bf6d,0x3f753f0e,
0x3f6d0000,0xbec43f35,0xbf6d0000,0x3ec4bf35,0x3f22bec4,0x3e953f7b,0x3dc93f6d,0xbf62bf55,0x3e48bf35,0x3f553f6d,0x3f7b3f35,0xbf0e3ec4,0xbe95bf6d,0x3f7f3f0e,0x3ef1bec4,0x3f463f7b,
0xbf35bf80,0x3f350000,0xbf35bf80,0x3f350000,0xbf75bf6d,0x3dc9bf0e,0xbf62bec4,0xbf22bf7b,0xbf7bbf35,0xbf0ebf6d,0x3e483f35,0xbf55bec4,0xbf46bec4,0xbf75bf7b,0x3f7f3f6d,0x3ef13f55,
0xbec40000,0xbf6dbf35,0x3ec40000,0x3f6d3f35,0x3dc93ec4,0xbef1be48,0xbf46bf6d,0xbe95bf0e,0x3f0e3f35,0x3e483ec4,0xbf55bf35,0xbf7bbf6d,0x3f623f6d,0x3f463f55,0x3e953ec4,0x3dc93e48,
0x0,0x0,0x0,0x0,0xbdc90000,0xbe953e48,0xbf463f35,0xbf623f55,0xbe480000,0xbf0e3ec4,0xbf7b3f80,0xbf553f6d,0xbe950000,0xbf463f0e,0xbef13f35,0x3dc93e48,
0xbec40000,0xbf6d3f35,0x3ec40000,0x3f6dbf35,0xbef10000,0xbf7f3f55,0x3f75bf35,0x3f46bf7b,0xbf0e0000,0xbf7b3f6d,0x3f55bf80,0xbe48bec4,0xbf220000,0xbf623f7b,0x3dc9bf35,0xbf753f0e,
0xbf350000,0xbf353f80,0xbf350000,0xbf353f80,0xbf460000,0xbef13f7b,0xbf7f3f35,0x3e953f0e,0xbf550000,0xbe483f6d,0xbf0e3f80,0x3f7bbec4,0xbf620000,0x3dc93f55,0x3e953f35,0x3f22bf7b,
0xbf6d0000,0x3ec43f35,0x3f6d0000,0xbec4bf35,0xbf750000,0x3f223f0e,0x3f62bf35,0xbf7f3e48,0xbf7b0000,0x3f553ec4,0x3e48bf80,0xbf0e3f6d,0xbf7f0000,0x3f753e48,0xbf22bf35,0x3ef13f55,
0xbf800000,0x3f800000,0xbf800000,0x3f800000,0xbf7f0000,0x3f75be48,0xbf223f35,0x3ef1bf55,0xbf7b0000,0x3f55bec4,0x3e483f80,0xbf0ebf6d,0xbf750000,0x3f22bf0e,0x3f623f35,0xbf7fbe48,
0xbf6d0000,0x3ec4bf35,0x3f6d0000,0xbec43f35,0xbf620000,0x3dc9bf55,0x3e95bf35,0x3f223f7b,0xbf550000,0xbe48bf6d,0xbf0ebf80,0x3f7b3ec4,0xbf460000,0xbef1bf7b,0xbf7fbf35,0x3e95bf0e,
0xbf350000,0xbf35bf80,0xbf350000,0xbf35bf80,0xbf220000,0xbf62bf7b,0x3dc93f35,0xbf75bf0e,0xbf0e0000,0xbf7bbf6d,0x3f553f80,0xbe483ec4,0xbef10000,0xbf7fbf55,0x3f753f35,0x3f463f7b,
0xbec40000,0xbf6dbf35,0x3ec40000,0x3f6d3f35,0xbe950000,0xbf46bf0e,0xbef1bf35,0x3dc9be48,0xbe480000,0xbf0ebec4,0xbf7bbf80,0xbf55bf6d,0xbdc90000,0xbe95be48,0xbf46bf35,0xbf62bf55,
0x0,0x0,0x0,0x0,0xbef13ec4,0xbf223f0e,0xbf753f6d,0xbf7f3f7b,0xbf553f35,0xbf7b3f6d,0xbf0e3f35,0xbe483ec4,0xbf7f3f6d,0xbf623f7b,0x3f22bec4,0x3f75bf55,
0xbf6d3f80,0xbec43f35,0x3f6dbf80,0x3ec4bf35,0xbf223f6d,0x3e953e48,0xbdc9bec4,0xbf623f0e,0xbe483f35,0x3f55bec4,0xbf7b3f35,0xbf0e3f6d,0x3e953ec4,0x3f7fbf55,0xbef13f6d,0x3f46be48,
0x3f350000,0x3f35bf80,0x3f350000,0x3f35bf80,0x3f75bec4,0x3dc9bf55,0x3f62bf6d,0xbf22be48,0x3f7bbf35,0xbf0ebec4,0xbe48bf35,0xbf553f6d,0x3f46bf6d,0xbf753e48,0xbf7f3ec4,0x3ef13f0e,
0x3ec4bf80,0xbf6d3f35,0xbec43f80,0x3f6dbf35,0xbdc9bf6d,0xbef13f7b,0x3f463ec4,0xbe95bf55,0xbf0ebf35,0x3e483f6d,0x3f55bf35,0xbf7b3ec4,0xbf62bec4,0x3f463f0e,0xbe95bf6d,0x3dc93f7b,
0xbf800000,0x3f800000,0xbf800000,0x3f800000,0xbf623ec4,0x3f46bf0e,0xbe953f6d,0x3dc9bf7b,0xbf0e3f35,0x3e48bf6d,0x3f553f35,0xbf7bbec4,0xbdc93f6d,0xbef1bf7b,0x3f46bec4,0xbe953f55,
0x3ec43f80,0xbf6dbf35,0xbec4bf80,0x3f6d3f35,0x3f463f6d,0xbf75be48,0xbf7fbec4,0x3ef1bf0e,0x3f7b3f35,0xbf0e3ec4,0xbe483f35,0xbf55bf6d,0x3f753ec4,0x3dc93f55,0x3f623f6d,0xbf223e48,
0x3f350000,0x3f353f80,0x3f350000,0x3f353f80,0x3e95bec4,0x3f7f3f55,0xbef1bf6d,0x3f463e48,0xbe48bf35,0x3f553ec4,0xbf7bbf35,0xbf0ebf6d,0xbf22bf6d,0x3e95be48,0xbdc93ec4,0xbf62bf0e,
0xbf6dbf80,0xbec4bf35,0x3f6d3f80,0x3ec43f35,0xbf7fbf6d,0xbf62bf7b,0x3f223ec4,0x3f753f55,0xbf55bf35,0xbf7bbf6d,0xbf0ebf35,0xbe48bec4,0xbef1bec4,0xbf22bf0e,0xbf75bf6d,0xbf7fbf7b,
0xbf803f80,0xbf803f80,0xbf803f80,0xbf803f80,0x3dc90000,0x3e95be48,0x3f46bf35,0x3f62bf55,0x3f7bbf80,0x3f55bf6d,0xbe480000,0xbf0e3ec4,0xbe950000,0xbf463f0e,0xbef13f35,0x3dc93e48,
0xbf6d3f80,0xbec43f35,0x3f6dbf80,0x3ec4bf35,0x3ef10000,0x3f7fbf55,0xbf753f35,0xbf463f7b,0x3f55bf80,0xbe48bec4,0x3f0e0000,0x3f7bbf6d,0xbf220000,0xbf623f7b,0x3dc9bf35,0xbf753f0e,
0xbf353f80,0x3f350000,0xbf353f80,0x3f350000,0x3f460000,0x3ef1bf7b,0x3f7fbf35,0xbe95bf0e,0x3f0ebf80,0xbf7b3ec4,0xbf550000,0xbe483f6d,0xbf620000,0x3dc93f55,0x3e953f35,0x3f22bf7b,
0xbec43f80,0x3f6dbf35,0x3ec4bf80,0xbf6d3f35,0x3f750000,0xbf22bf0e,0xbf623f35,0x3f7fbe48,0x3e48bf80,0xbf0e3f6d,0x3f7b0000,0xbf55bec4,0xbf7f0000,0x3f753e48,0xbf22bf35,0x3ef13f55,
0x3f80,0xbf80,0x3f80,0xbf80,0x3f7f0000,0xbf753e48,0x3f22bf35,0xbef13f55,0xbe48bf80,0x3f0e3f6d,0xbf7b0000,0x3f55bec4,0xbf750000,0x3f22bf0e,0x3f623f35,0xbf7fbe48,
0x3ec43f80,0xbf6dbf35,0xbec4bf80,0x3f6d3f35,0x3f620000,0xbdc93f55,0xbe953f35,0xbf22bf7b,0xbf0ebf80,0x3f7b3ec4,0x3f550000,0x3e483f6d,0xbf460000,0xbef1bf7b,0xbf7fbf35,0x3e95bf0e,
0x3f353f80,0xbf350000,0x3f353f80,0xbf350000,0x3f220000,0x3f623f7b,0xbdc9bf35,0x3f753f0e,0xbf55bf80,0x3e48bec4,0xbf0e0000,0xbf7bbf6d,0xbef10000,0xbf7fbf55,0x3f753f35,0x3f463f7b,
0x3f6d3f80,0x3ec43f35,0xbf6dbf80,0xbec4bf35,0x3e950000,0x3f463f0e,0x3ef13f35,0xbdc93e48,0xbf7bbf80,0xbf55bf6d,0x3e480000,0x3f0e3ec4,0xbdc90000,0xbe95be48,0xbf46bf35,0xbf62bf55,
0xbf803f80,0xbf803f80,0xbf803f80,0xbf803f80,0x3ef1bec4,0x3f22bf0e,0x3f75bf6d,0x3f7fbf7b,0x3f0ebf35,0x3e48bec4,0xbf553f35,0xbf7b3f6d,0xbf7f3f6d,0xbf623f7b,0x3f22bec4,0x3f75bf55,
0x3ec40000,0x3f6dbf35,0xbec40000,0xbf6d3f35,0x3f22bf6d,0xbe95be48,0x3dc93ec4,0x3f62bf0e,0xbf7b3f35,0xbf0e3f6d,0x3e48bf35,0xbf553ec4,0x3e953ec4,0x3f7fbf55,0xbef13f6d,0x3f46be48,
0x3f35bf80,0xbf350000,0x3f35bf80,0xbf350000,0xbf753ec4,0xbdc93f55,0xbf623f6d,0x3f223e48,0x3e483f35,0x3f55bf6d,0x3f7bbf35,0xbf0ebec4,0x3f46bf6d,0xbf753e48,0xbf7f3ec4,0x3ef13f0e,
0xbf6d0000,0x3ec43f35,0x3f6d0000,0xbec4bf35,0x3dc93f6d,0x3ef1bf7b,0xbf46bec4,0x3e953f55,0x3f55bf35,0xbf7b3ec4,0x3f0e3f35,0xbe48bf6d,0xbf62bec4,0x3f463f0e,0xbe95bf6d,0x3dc93f7b,
0x3f80,0xbf80,0x3f80,0xbf80,0x3f62bec4,0xbf463f0e,0x3e95bf6d,0xbdc93f7b,0xbf55bf35,0x3f7b3ec4,0xbf0e3f35,0x3e48bf6d,0xbdc93f6d,0xbef1bf7b,0x3f46bec4,0xbe953f55,
0x3f6d0000,0xbec43f35,0xbf6d0000,0x3ec4bf35,0xbf46bf6d,0x3f753e48,0x3f7f3ec4,0xbef13f0e,0xbe483f35,0xbf55bf6d,0xbf7bbf35,0x3f0ebec4,0x3f753ec4,0x3dc93f55,0x3f623f6d,0xbf223e48,
0xbf35bf80,0x3f350000,0xbf35bf80,0x3f350000,0xbe953ec4,0xbf7fbf55,0x3ef13f6d,0xbf46be48,0x3f7b3f35,0x3f0e3f6d,0xbe48bf35,0x3f553ec4,0xbf22bf6d,0x3e95be48,0xbdc93ec4,0xbf62bf0e,
0xbec40000,0xbf6dbf35,0x3ec40000,0x3f6d3f35,0x3f7f3f6d,0x3f623f7b,0xbf22bec4,0xbf75bf55,0xbf0ebf35,0xbe48bec4,0x3f553f35,0x3f7b3f6d,0xbef1bec4,0xbf22bf0e,0xbf75bf6d,0xbf7fbf7b,
0x0,0x0,0x0,0x0,0xbf7f3f80,0xbf753f7b,0xbf223f35,0xbef13f0e,0x3e480000,0x3f0ebec4,0x3f7bbf80,0x3f55bf6d,0x3f75bf80,0x3f22bf55,0xbf623f35,0xbf7f3f7b,
0xbec40000,0xbf6d3f35,0x3ec40000,0x3f6dbf35,0xbf623f80,0xbdc93f0e,0x3e95bf35,0xbf223e48,0x3f0e0000,0x3f7bbf6d,0xbf553f80,0x3e483ec4,0x3f46bf80,0xbef1be48,0x3f7fbf35,0x3e95bf55,
0xbf350000,0xbf353f80,0xbf350000,0xbf353f80,0xbf223f80,0x3f62be48,0x3dc93f35,0x3f75bf55,0x3f550000,0x3e48bf6d,0x3f0ebf80,0xbf7b3ec4,0x3ef1bf80,0xbf7f3f0e,0xbf753f35,0x3f463e48,
0xbf6d0000,0x3ec43f35,0x3f6d0000,0xbec4bf35,0xbe953f80,0x3f46bf55,0xbef1bf35,0xbdc93f7b,0x3f7b0000,0xbf55bec4,0xbe483f80,0x3f0ebf6d,0x3dc9bf80,0xbe953f7b,0x3f46bf35,0xbf623f0e,
0xbf800000,0x3f800000,0xbf800000,0x3f800000,0x3dc93f80,0xbe95bf7b,0x3f463f35,0xbf62bf0e,0x3f7b0000,0xbf553ec4,0xbe48bf80,0x3f0e3f6d,0xbe95bf80,0x3f463f55,0xbef13f35,0xbdc9bf7b,
0xbf6d0000,0x3ec4bf35,0x3f6d0000,0xbec43f35,0x3ef13f80,0xbf7fbf0e,0xbf75bf35,0x3f46be48,0x3f550000,0x3e483f6d,0x3f0e3f80,0xbf7bbec4,0xbf22bf80,0x3f623e48,0x3dc9bf35,0x3f753f55,
0xbf350000,0xbf35bf80,0xbf350000,0xbf35bf80,0x3f463f80,0xbef13e48,0x3f7f3f35,0x3e953f55,0x3f0e0000,0x3f7b3f6d,0xbf55bf80,0x3e48bec4,0xbf62bf80,0xbdc9bf0e,0x3e953f35,0xbf22be48,
0xbec40000,0xbf6dbf35,0x3ec40000,0x3f6d3f35,0x3f753f80,0x3f223f55,0xbf62bf35,0xbf7fbf7b,0x3e480000,0x3f0e3ec4,0x3f7b3f80,0x3f553f6d,0xbf7fbf80,0xbf75bf7b,0xbf22bf35,0xbef1bf0e,
0x0,0x0,0x0,0x0,0xbf623f6d,0xbf463f55,0xbe953ec4,0xbdc93e48,0x3f55bf35,0x3f7bbf6d,0x3f0ebf35,0x3e48bec4,0x3dc9bec4,0xbef13e48,0xbf463f6d,0xbe953f0e,
0xbf6d3f80,0xbec43f35,0x3f6dbf80,0x3ec4bf35,0x3f46bec4,0x3f75bf7b,0xbf7f3f6d,0xbef13f55,0x3e48bf35,0xbf553ec4,0x3f7bbf35,0x3f0ebf6d,0xbf753f6d,0x3dc93f0e,0xbf623ec4,0xbf223f7b,
0x3f350000,0x3f35bf80,0x3f350000,0x3f35bf80,0x3e95bf6d,0xbf7f3f0e,0xbef1bec4,0xbf463f7b,0xbf7b3f35,0x3f0e3ec4,0x3e483f35,0x3f55bf6d,0x3f223ec4,0x3e95bf7b,0x3dc9bf6d,0xbf623f55,
0x3ec4bf80,0xbf6d3f35,0xbec43f80,0x3f6dbf35,0xbf7f3ec4,0x3f623e48,0x3f22bf6d,0xbf753f0e,0x3f0e3f35,0xbe48bf6d,0xbf553f35,0x3f7bbec4,0x3ef1bf6d,0xbf223f55,0x3f75bec4,0xbf7f3e48,
0xbf800000,0x3f800000,0xbf800000,0x3f800000,0x3ef13f6d,0xbf22bf55,0x3f753ec4,0xbf7fbe48,0x3f0ebf35,0xbe483f6d,0xbf55bf35,0x3f7b3ec4,0xbf7fbec4,0x3f62be48,0x3f223f6d,0xbf75bf0e,
0x3ec43f80,0xbf6dbf35,0xbec4bf80,0x3f6d3f35,0x3f22bec4,0x3e953f7b,0x3dc93f6d,0xbf62bf55,0xbf7bbf35,0x3f0ebec4,0x3e48bf35,0x3f553f6d,0x3e953f6d,0xbf7fbf0e,0xbef13ec4,0xbf46bf7b,
0x3f350000,0x3f353f80,0x3f350000,0x3f353f80,0xbf75bf6d,0x3dc9bf0e,0xbf62bec4,0xbf22bf7b,0x3e483f35,0xbf55bec4,0x3f7b3f35,0x3f0e3f6d,0x3f463ec4,0x3f753f7b,0xbf7fbf6d,0xbef1bf55,
0xbf6dbf80,0xbec4bf35,0x3f6d3f80,0x3ec43f35,0x3dc93ec4,0xbef1be48,0xbf46bf6d,0xbe95bf0e,0x3f553f35,0x3f7b3f6d,0x3f0e3f35,0x3e483ec4,0xbf62bf6d,0xbf46bf55,0xbe95bec4,0xbdc9be48,
0x3f7f3f80,0x3f753f7b,0x3f223f35,0x3ef13f0e,0x3f753f80,0x3f223f55,0xbf62bf35,0xbf7fbf7b,0x3f623f80,0x3dc93f0e,0xbe95bf35,0x3f223e48,0x3f463f80,0xbef13e48,0x3f7f3f35,0x3e953f55,
0x3f223f80,0xbf62be48,0xbdc93f35,0xbf75bf55,0x3ef13f80,0xbf7fbf0e,0xbf75bf35,0x3f46be48,0x3e953f80,0xbf46bf55,0x3ef1bf35,0x3dc93f7b,0x3dc93f80,0xbe95bf7b,0x3f463f35,0xbf62bf0e,
0xbdc93f80,0x3e95bf7b,0xbf463f35,0x3f62bf0e,0xbe953f80,0x3f46bf55,0xbef1bf35,0xbdc93f7b,0xbef13f80,0x3f7fbf0e,0x3f75bf35,0xbf46be48,0xbf223f80,0x3f62be48,0x3dc93f35,0x3f75bf55,
0xbf463f80,0x3ef13e48,0xbf7f3f35,0xbe953f55,0xbf623f80,0xbdc93f0e,0x3e95bf35,0xbf223e48,0xbf753f80,0xbf223f55,0x3f62bf35,0x3f7fbf7b,0xbf7f3f80,0xbf753f7b,0xbf223f35,0xbef13f0e,
0xbf7f3f80,0xbf753f7b,0xbf223f35,0xbef13f0e,0xbf753f80,0xbf223f55,0x3f62bf35,0x3f7fbf7b,0xbf623f80,0xbdc93f0e,0x3e95bf35,0xbf223e48,0xbf463f80,0x3ef13e48,0xbf7f3f35,0xbe953f55,
0xbf223f80,0x3f62be48,0x3dc93f35,0x3f75bf55,0xbef13f80,0x3f7fbf0e,0x3f75bf35,0xbf46be48,0xbe953f80,0x3f46bf55,0xbef1bf35,0xbdc93f7b,0xbdc93f80,0x3e95bf7b,0xbf463f35,0x3f62bf0e,
0x3dc93f80,0xbe95bf7b,0x3f463f35,0xbf62bf0e,0x3e953f80,0xbf46bf55,0x3ef1bf35,0x3dc93f7b,0x3ef13f80,0xbf7fbf0e,0xbf75bf35,0x3f46be48,0x3f223f80,0xbf62be48,0xbdc93f35,0xbf75bf55,
0x3f463f80,0xbef13e48,0x3f7f3f35,0x3e953f55,0x3f623f80,0x3dc93f0e,0xbe95bf35,0x3f223e48,0x3f753f80,0x3f223f55,0xbf62bf35,0xbf7fbf7b,0x3f7f3f80,0x3f753f7b,0x3f223f35,0x3ef13f0e,
0x3f623f6d,0x3f463f55,0x3e953ec4,0x3dc93e48,0x3dc93ec4,0xbef1be48,0xbf46bf6d,0xbe95bf0e,0xbf46bec4,0xbf75bf7b,0x3f7f3f6d,0x3ef13f55,0xbf75bf6d,0x3dc9bf0e,0xbf62bec4,0xbf22bf7b,
0xbe95bf6d,0x3f7f3f0e,0x3ef1bec4,0x3f463f7b,0x3f22bec4,0x3e953f7b,0x3dc93f6d,0xbf62bf55,0x3f7f3ec4,0xbf623e48,0xbf22bf6d,0x3f753f0e,0x3ef13f6d,0xbf22bf55,0x3f753ec4,0xbf7fbe48,
0xbef13f6d,0x3f22bf55,0xbf753ec4,0x3f7fbe48,0xbf7f3ec4,0x3f623e48,0x3f22bf6d,0xbf753f0e,0xbf22bec4,0xbe953f7b,0xbdc93f6d,0x3f62bf55,0x3e95bf6d,0xbf7f3f0e,0xbef1bec4,0xbf463f7b,
0x3f75bf6d,0xbdc9bf0e,0x3f62bec4,0x3f22bf7b,0x3f46bec4,0x3f75bf7b,0xbf7f3f6d,0xbef13f55,0xbdc93ec4,0x3ef1be48,0x3f46bf6d,0x3e95bf0e,0xbf623f6d,0xbf463f55,0xbe953ec4,0xbdc93e48,
0xbf623f6d,0xbf463f55,0xbe953ec4,0xbdc93e48,0xbdc93ec4,0x3ef1be48,0x3f46bf6d,0x3e95bf0e,0x3f46bec4,0x3f75bf7b,0xbf7f3f6d,0xbef13f55,0x3f75bf6d,0xbdc9bf0e,0x3f62bec4,0x3f22bf7b,
0x3e95bf6d,0xbf7f3f0e,0xbef1bec4,0xbf463f7b,0xbf22bec4,0xbe953f7b,0xbdc93f6d,0x3f62bf55,0xbf7f3ec4,0x3f623e48,0x3f22bf6d,0xbf753f0e,0xbef13f6d,0x3f22bf55,0xbf753ec4,0x3f7fbe48,
0x3ef13f6d,0xbf22bf55,0x3f753ec4,0xbf7fbe48,0x3f7f3ec4,0xbf623e48,0xbf22bf6d,0x3f753f0e,0x3f22bec4,0x3e953f7b,0x3dc93f6d,0xbf62bf55,0xbe95bf6d,0x3f7f3f0e,0x3ef1bec4,0x3f463f7b,
0xbf75bf6d,0x3dc9bf0e,0xbf62bec4,0xbf22bf7b,0xbf46bec4,0xbf75bf7b,0x3f7f3f6d,0x3ef13f55,0x3dc93ec4,0xbef1be48,0xbf46bf6d,0xbe95bf0e,0x3f623f6d,0x3f463f55,0x3e953ec4,0x3dc93e48,
0x3dc90000,0x3e953e48,0x3f463f35,0x3f623f55,0x3e950000,0x3f463f0e,0x3ef13f35,0xbdc93e48,0x3ef10000,0x3f7f3f55,0xbf75bf35,0xbf46bf7b,0x3f220000,0x3f623f7b,0xbdc9bf35,0x3f753f0e,
0x3f460000,0x3ef13f7b,0x3f7f3f35,0xbe953f0e,0x3f620000,0xbdc93f55,0xbe953f35,0xbf22bf7b,0x3f750000,0xbf223f0e,0xbf62bf35,0x3f7f3e48,0x3f7f0000,0xbf753e48,0x3f22bf35,0xbef13f55,
0x3f7f0000,0xbf75be48,0x3f223f35,0xbef1bf55,0x3f750000,0xbf22bf0e,0xbf623f35,0x3f7fbe48,0x3f620000,0xbdc9bf55,0xbe95bf35,0xbf223f7b,0x3f460000,0x3ef1bf7b,0x3f7fbf35,0xbe95bf0e,
0x3f220000,0x3f62bf7b,0xbdc93f35,0x3f75bf0e,0x3ef10000,0x3f7fbf55,0xbf753f35,0xbf463f7b,0x3e950000,0x3f46bf0e,0x3ef1bf35,0xbdc9be48,0x3dc90000,0x3e95be48,0x3f46bf35,0x3f62bf55,
0xbdc90000,0xbe953e48,0xbf463f35,0xbf623f55,0xbe950000,0xbf463f0e,0xbef13f35,0x3dc93e48,0xbef10000,0xbf7f3f55,0x3f75bf35,0x3f46bf7b,0xbf220000,0xbf623f7b,0x3dc9bf35,0xbf753f0e,
0xbf460000,0xbef13f7b,0xbf7f3f35,0x3e953f0e,0xbf620000,0x3dc93f55,0x3e953f35,0x3f22bf7b,0xbf750000,0x3f223f0e,0x3f62bf35,0xbf7f3e48,0xbf7f0000,0x3f753e48,0xbf22bf35,0x3ef13f55,
0xbf7f0000,0x3f75be48,0xbf223f35,0x3ef1bf55,0xbf750000,0x3f22bf0e,0x3f623f35,0xbf7fbe48,0xbf620000,0x3dc9bf55,0x3e95bf35,0x3f223f7b,0xbf460000,0xbef1bf7b,0xbf7fbf35,0x3e95bf0e,
0xbf220000,0xbf62bf7b,0x3dc93f35,0xbf75bf0e,0xbef10000,0xbf7fbf55,0x3f753f35,0x3f463f7b,0xbe950000,0xbf46bf0e,0xbef1bf35,0x3dc9be48,0xbdc90000,0xbe95be48,0xbf46bf35,0xbf62bf55,
0x3ef13ec4,0x3f223f0e,0x3f753f6d,0x3f7f3f7b,0x3f7f3f6d,0x3f623f7b,0xbf22bec4,0xbf75bf55,0x3f223f6d,0xbe953e48,0x3dc9bec4,0x3f623f0e,0xbe953ec4,0xbf7fbf55,0x3ef13f6d,0xbf46be48,
0xbf75bec4,0xbdc9bf55,0xbf62bf6d,0x3f22be48,0xbf46bf6d,0x3f753e48,0x3f7f3ec4,0xbef13f0e,0x3dc9bf6d,0x3ef13f7b,0xbf463ec4,0x3e95bf55,0x3f62bec4,0xbf463f0e,0x3e95bf6d,0xbdc93f7b,
0x3f623ec4,0xbf46bf0e,0x3e953f6d,0xbdc9bf7b,0x3dc93f6d,0x3ef1bf7b,0xbf46bec4,0x3e953f55,0xbf463f6d,0x3f75be48,0x3f7fbec4,0xbef1bf0e,0xbf753ec4,0xbdc93f55,0xbf623f6d,0x3f223e48,
0xbe95bec4,0xbf7f3f55,0x3ef1bf6d,0xbf463e48,0x3f22bf6d,0xbe95be48,0x3dc93ec4,0x3f62bf0e,0x3f7fbf6d,0x3f62bf7b,0xbf223ec4,0xbf753f55,0x3ef1bec4,0x3f22bf0e,0x3f75bf6d,0x3f7fbf7b,
0xbef13ec4,0xbf223f0e,0xbf753f6d,0xbf7f3f7b,0xbf7f3f6d,0xbf623f7b,0x3f22bec4,0x3f75bf55,0xbf223f6d,0x3e953e48,0xbdc9bec4,0xbf623f0e,0x3e953ec4,0x3f7fbf55,0xbef13f6d,0x3f46be48,
0x3f75bec4,0x3dc9bf55,0x3f62bf6d,0xbf22be48,0x3f46bf6d,0xbf753e48,0xbf7f3ec4,0x3ef13f0e,0xbdc9bf6d,0xbef13f7b,0x3f463ec4,0xbe95bf55,0xbf62bec4,0x3f463f0e,0xbe95bf6d,0x3dc93f7b,
0xbf623ec4,0x3f46bf0e,0xbe953f6d,0x3dc9bf7b,0xbdc93f6d,0xbef1bf7b,0x3f46bec4,0xbe953f55,0x3f463f6d,0xbf75be48,0xbf7fbec4,0x3ef1bf0e,0x3f753ec4,0x3dc93f55,0x3f623f6d,0xbf223e48,
0x3e95bec4,0x3f7f3f55,0xbef1bf6d,0x3f463e48,0xbf22bf6d,0x3e95be48,0xbdc93ec4,0xbf62bf0e,0xbf7fbf6d,0xbf62bf7b,0x3f223ec4,0x3f753f55,0xbef1bec4,0xbf22bf0e,0xbf75bf6d,0xbf7fbf7b,
0xbdc90000,0xbe95be48,0xbf46bf35,0xbf62bf55,0x3e950000,0x3f463f0e,0x3ef13f35,0xbdc93e48,0xbef10000,0xbf7fbf55,0x3f753f35,0x3f463f7b,0x3f220000,0x3f623f7b,0xbdc9bf35,0x3f753f0e,
0xbf460000,0xbef1bf7b,0xbf7fbf35,0x3e95bf0e,0x3f620000,0xbdc93f55,0xbe953f35,0xbf22bf7b,0xbf750000,0x3f22bf0e,0x3f623f35,0xbf7fbe48,0x3f7f0000,0xbf753e48,0x3f22bf35,0xbef13f55,
0xbf7f0000,0x3f753e48,0xbf22bf35,0x3ef13f55,0x3f750000,0xbf22bf0e,0xbf623f35,0x3f7fbe48,0xbf620000,0x3dc93f55,0x3e953f35,0x3f22bf7b,0x3f460000,0x3ef1bf7b,0x3f7fbf35,0xbe95bf0e,
0xbf220000,0xbf623f7b,0x3dc9bf35,0xbf753f0e,0x3ef10000,0x3f7fbf55,0xbf753f35,0xbf463f7b,0xbe950000,0xbf463f0e,0xbef13f35,0x3dc93e48,0x3dc90000,0x3e95be48,0x3f46bf35,0x3f62bf55,
0x3dc90000,0x3e95be48,0x3f46bf35,0x3f62bf55,0xbe950000,0xbf463f0e,0xbef13f35,0x3dc93e48,0x3ef10000,0x3f7fbf55,0xbf753f35,0xbf463f7b,0xbf220000,0xbf623f7b,0x3dc9bf35,0xbf753f0e,
0x3f460000,0x3ef1bf7b,0x3f7fbf35,0xbe95bf0e,0xbf620000,0x3dc93f55,0x3e953f35,0x3f22bf7b,0x3f750000,0xbf22bf0e,0xbf623f35,0x3f7fbe48,0xbf7f0000,0x3f753e48,0xbf22bf35,0x3ef13f55,
0x3f7f0000,0xbf753e48,0x3f22bf35,0xbef13f55,0xbf750000,0x3f22bf0e,0x3f623f35,0xbf7fbe48,0x3f620000,0xbdc93f55,0xbe953f35,0xbf22bf7b,0xbf460000,0xbef1bf7b,0xbf7fbf35,0x3e95bf0e,
0x3f220000,0x3f623f7b,0xbdc9bf35,0x3f753f0e,0xbef10000,0xbf7fbf55,0x3f753f35,0x3f463f7b,0x3e950000,0x3f463f0e,0x3ef13f35,0xbdc93e48,0xbdc90000,0xbe95be48,0xbf46bf35,0xbf62bf55,
0xbef1bec4,0xbf22bf0e,0xbf75bf6d,0xbf7fbf7b,0x3f7f3f6d,0x3f623f7b,0xbf22bec4,0xbf75bf55,0xbf22bf6d,0x3e95be48,0xbdc93ec4,0xbf62bf0e,0xbe953ec4,0xbf7fbf55,0x3ef13f6d,0xbf46be48,
0x3f753ec4,0x3dc93f55,0x3f623f6d,0xbf223e48,0xbf46bf6d,0x3f753e48,0x3f7f3ec4,0xbef13f0e,0xbdc93f6d,0xbef1bf7b,0x3f46bec4,0xbe953f55,0x3f62bec4,0xbf463f0e,0x3e95bf6d,0xbdc93f7b,
0xbf62bec4,0x3f463f0e,0xbe95bf6d,0x3dc93f7b,0x3dc93f6d,0x3ef1bf7b,0xbf46bec4,0x3e953f55,0x3f46bf6d,0xbf753e48,0xbf7f3ec4,0x3ef13f0e,0xbf753ec4,0xbdc93f55,0xbf623f6d,0x3f223e48,
0x3e953ec4,0x3f7fbf55,0xbef13f6d,0x3f46be48,0x3f22bf6d,0xbe95be48,0x3dc93ec4,0x3f62bf0e,0xbf7f3f6d,0xbf623f7b,0x3f22bec4,0x3f75bf55,0x3ef1bec4,0x3f22bf0e,0x3f75bf6d,0x3f7fbf7b,
0x3ef1bec4,0x3f22bf0e,0x3f75bf6d,0x3f7fbf7b,0xbf7f3f6d,0xbf623f7b,0x3f22bec4,0x3f75bf55,0x3f22bf6d,0xbe95be48,0x3dc93ec4,0x3f62bf0e,0x3e953ec4,0x3f7fbf55,0xbef13f6d,0x3f46be48,
0xbf753ec4,0xbdc93f55,0xbf623f6d,0x3f223e48,0x3f46bf6d,0xbf753e48,0xbf7f3ec4,0x3ef13f0e,0x3dc93f6d,0x3ef1bf7b,0xbf46bec4,0x3e953f55,0xbf62bec4,0x3f463f0e,0xbe95bf6d,0x3dc93f7b,
0x3f62bec4,0xbf463f0e,0x3e95bf6d,0xbdc93f7b,0xbdc93f6d,0xbef1bf7b,0x3f46bec4,0xbe953f55,0xbf46bf6d,0x3f753e48,0x3f7f3ec4,0xbef13f0e,0x3f753ec4,0x3dc93f55,0x3f623f6d,0xbf223e48,
0xbe953ec4,0xbf7fbf55,0x3ef13f6d,0xbf46be48,0xbf22bf6d,0x3e95be48,0xbdc93ec4,0xbf62bf0e,0x3f7f3f6d,0x3f623f7b,0xbf22bec4,0xbf75bf55,0xbef1bec4,0xbf22bf0e,0xbf75bf6d,0xbf7fbf7b,
0x3f7f3f80,0x3f753f7b,0x3f223f35,0x3ef13f0e,0xbf75bf80,0xbf22bf55,0x3f623f35,0x3f7f3f7b,0x3f623f80,0x3dc93f0e,0xbe95bf35,0x3f223e48,0xbf46bf80,0x3ef1be48,0xbf7fbf35,0xbe95bf55,
0x3f223f80,0xbf62be48,0xbdc93f35,0xbf75bf55,0xbef1bf80,0x3f7f3f0e,0x3f753f35,0xbf463e48,0x3e953f80,0xbf46bf55,0x3ef1bf35,0x3dc93f7b,0xbdc9bf80,0x3e953f7b,0xbf46bf35,0x3f623f0e,
0xbdc93f80,0x3e95bf7b,0xbf463f35,0x3f62bf0e,0x3e95bf80,0xbf463f55,0x3ef13f35,0x3dc9bf7b,0xbef13f80,0x3f7fbf0e,0x3f75bf35,0xbf46be48,0x3f22bf80,0xbf623e48,0xbdc9bf35,0xbf753f55,
0xbf463f80,0x3ef13e48,0xbf7f3f35,0xbe953f55,0x3f62bf80,0x3dc9bf0e,0xbe953f35,0x3f22be48,0xbf753f80,0xbf223f55,0x3f62bf35,0x3f7fbf7b,0x3f7fbf80,0x3f75bf7b,0x3f22bf35,0x3ef1bf0e,
0xbf7f3f80,0xbf753f7b,0xbf223f35,0xbef13f0e,0x3f75bf80,0x3f22bf55,0xbf623f35,0xbf7f3f7b,0xbf623f80,0xbdc93f0e,0x3e95bf35,0xbf223e48,0x3f46bf80,0xbef1be48,0x3f7fbf35,0x3e95bf55,
0xbf223f80,0x3f62be48,0x3dc93f35,0x3f75bf55,0x3ef1bf80,0xbf7f3f0e,0xbf753f35,0x3f463e48,0xbe953f80,0x3f46bf55,0xbef1bf35,0xbdc93f7b,0x3dc9bf80,0xbe953f7b,0x3f46bf35,0xbf623f0e,
0x3dc93f80,0xbe95bf7b,0x3f463f35,0xbf62bf0e,0xbe95bf80,0x3f463f55,0xbef13f35,0xbdc9bf7b,0x3ef13f80,0xbf7fbf0e,0xbf75bf35,0x3f46be48,0xbf22bf80,0x3f623e48,0x3dc9bf35,0x3f753f55,
0x3f463f80,0xbef13e48,0x3f7f3f35,0x3e953f55,0xbf62bf80,0xbdc9bf0e,0x3e953f35,0xbf22be48,0x3f753f80,0x3f223f55,0xbf62bf35,0xbf7fbf7b,0xbf7fbf80,0xbf75bf7b,0xbf22bf35,0xbef1bf0e,
0x3f623f6d,0x3f463f55,0x3e953ec4,0x3dc93e48,0xbdc9bec4,0x3ef13e48,0x3f463f6d,0x3e953f0e,0xbf46bec4,0xbf75bf7b,0x3f7f3f6d,0x3ef13f55,0x3f753f6d,0xbdc93f0e,0x3f623ec4,0x3f223f7b,
0xbe95bf6d,0x3f7f3f0e,0x3ef1bec4,0x3f463f7b,0xbf223ec4,0xbe95bf7b,0xbdc9bf6d,0x3f623f55,0x3f7f3ec4,0xbf623e48,0xbf22bf6d,0x3f753f0e,0xbef1bf6d,0x3f223f55,0xbf75bec4,0x3f7f3e48,
0xbef13f6d,0x3f22bf55,0xbf753ec4,0x3f7fbe48,0x3f7fbec4,0xbf62be48,0xbf223f6d,0x3f75bf0e,0xbf22bec4,0xbe953f7b,0xbdc93f6d,0x3f62bf55,0xbe953f6d,0x3f7fbf0e,0x3ef13ec4,0x3f46bf7b,
0x3f75bf6d,0xbdc9bf0e,0x3f62bec4,0x3f22bf7b,0xbf463ec4,0xbf753f7b,0x3f7fbf6d,0x3ef1bf55,0xbdc93ec4,0x3ef1be48,0x3f46bf6d,0x3e95bf0e,0x3f62bf6d,0x3f46bf55,0x3e95bec4,0x3dc9be48,
0xbf623f6d,0xbf463f55,0xbe953ec4,0xbdc93e48,0x3dc9bec4,0xbef13e48,0xbf463f6d,0xbe953f0e,0x3f46bec4,0x3f75bf7b,0xbf7f3f6d,0xbef13f55,0xbf753f6d,0x3dc93f0e,0xbf623ec4,0xbf223f7b,
0x3e95bf6d,0xbf7f3f0e,0xbef1bec4,0xbf463f7b,0x3f223ec4,0x3e95bf7b,0x3dc9bf6d,0xbf623f55,0xbf7f3ec4,0x3f623e48,0x3f22bf6d,0xbf753f0e,0x3ef1bf6d,0xbf223f55,0x3f75bec4,0xbf7f3e48,
0x3ef13f6d,0xbf22bf55,0x3f753ec4,0xbf7fbe48,0xbf7fbec4,0x3f62be48,0x3f223f6d,0xbf75bf0e,0x3f22bec4,0x3e953f7b,0x3dc93f6d,0xbf62bf55,0x3e953f6d,0xbf7fbf0e,0xbef13ec4,0xbf46bf7b,
0xbf75bf6d,0x3dc9bf0e,0xbf62bec4,0xbf22bf7b,0x3f463ec4,0x3f753f7b,0xbf7fbf6d,0xbef1bf55,0x3dc93ec4,0xbef1be48,0xbf46bf6d,0xbe95bf0e,0xbf62bf6d,0xbf46bf55,0xbe95bec4,0xbdc9be48,
0x35803580,0x35803580,0x35803580,0x35803580,0x3575357f,0x35463562,0xb495b3c9,0xb522b4f1,0x3555357b,0x3448350e,0xb555b57b,0xb448b50e,0x35223575,0xb4f133c9,0x35463495,0x3562357f,
0x34c4356d,0xb56db4c4,0x34c4356d,0xb56db4c4,0x33c93562,0xb575b546,0xb57fb4f1,0x3495b522,0xb4483555,0xb50eb57b,0x3448b555,0x350e357b,0xb4f13546,0x33c9b575,0x35623522,0xb57fb495,
0xb5353535,0x3535b535,0xb5353535,0x3535b535,0xb5623522,0x357fb495,0xb4f1b546,0x33c93575,0xb57b350e,0x35553448,0x357bb50e,0xb555b448,0xb57f34f1,0x34953522,0xb3c93562,0x3575b546,
0xb56d34c4,0xb4c4356d,0xb56d34c4,0xb4c4356d,0xb5463495,0xb562357f,0x3522b575,0xb4f1b3c9,0xb50e3448,0xb57b3555,0x350eb448,0x357bb555,0xb49533c9,0xb52234f1,0xb575357f,0xb5463562,
0x0,0x0,0x0,0x0,0x3495b3c9,0x3522b4f1,0x3575b57f,0x3546b562,0x350eb448,0x357bb555,0xb50e3448,0xb57b3555,0x3546b495,0x3562b57f,0xb5223575,0x34f133c9,
0x356db4c4,0x34c4b56d,0x356db4c4,0x34c4b56d,0x357fb4f1,0xb495b522,0x33c9b562,0xb5753546,0x357bb50e,0xb555b448,0xb57b350e,0x35553448,0x3562b522,0xb57f3495,0x34f13546,0xb3c9b575,
0x3535b535,0xb5353535,0x3535b535,0xb5353535,0x34f1b546,0xb3c93575,0xb562b522,0x357f3495,0x3448b555,0x350e357b,0xb4483555,0xb50eb57b,0xb3c9b562,0x35753546,0x357f34f1,0xb4953522,
0xb4c4b56d,0x356d34c4,0xb4c4b56d,0x356d34c4,0xb522b575,0x34f1b3c9,0xb546b495,0xb562b57f,0xb555b57b,0xb448b50e,0x3555357b,0x3448350e,0xb575b57f,0xb546b562,0x349533c9,0x352234f1,
0x35803580,0x35803580,0x35803580,0x35803580,0x34f13522,0x33c93495,0xb562b546,0xb57fb575,0xb50eb448,0xb57bb555,0x350e3448,0x357b3555,0xb57fb562,0xb495b546,0xb3c934f1,0xb575b522,
0xb4c4b56d,0x356d34c4,0xb4c4b56d,0x356d34c4,0x3522b495,0x34f1357f,0x35463575,0xb562b3c9,0x357b350e,0xb5553448,0xb57bb50e,0x3555b448,0x3495357f,0xb522b562,0x3575b3c9,0xb54634f1,
0xb5353535,0x3535b535,0xb5353535,0x3535b535,0xb575b3c9,0x354634f1,0x3495b57f,0xb5223562,0xb448b555,0xb50e357b,0x34483555,0x350eb57b,0x3546b575,0xb56233c9,0xb522b495,0xb4f1357f,
0x356db4c4,0x34c4b56d,0x356db4c4,0x34c4b56d,0x33c934f1,0x3575b522,0xb57f3562,0xb4953546,0xb555357b,0xb448350e,0x3555b57b,0x3448b50e,0xb5623546,0xb57f3575,0xb4f13522,0xb3c93495,
0x0,0x0,0x0,0x0,0x3562b546,0x357fb575,0x34f1b522,0x33c9b495,0x3555b57b,0x3448b50e,0xb555357b,0xb448350e,0xb3c9b4f1,0xb5753522,0x357fb562,0x3495b546,
0xb56d34c4,0xb4c4356d,0xb56d34c4,0xb4c4356d,0xb5463575,0x3562b3c9,0x35223495,0x34f1b57f,0x34483555,0x350eb57b,0xb448b555,0xb50e357b,0x357533c9,0xb546b4f1,0xb495357f,0x3522b562,
0x3535b535,0xb5353535,0x3535b535,0xb5353535,0xb495b57f,0x35223562,0xb57533c9,0x3546b4f1,0xb57bb50e,0x3555b448,0x357b350e,0xb5553448,0xb5223495,0xb4f1b57f,0xb546b575,0x356233c9,
0x34c4356d,0xb56db4c4,0x34c4356d,0xb56db4c4,0x357f3562,0x34953546,0x33c9b4f1,0x35753522,0x350e3448,0x357b3555,0xb50eb448,0xb57bb555,0xb4f1b522,0xb3c9b495,0x35623546,0x357f3575,
0x0,0x0,0x0,0x0,0x349533c9,0x352234f1,0x3575357f,0x35463562,0x350e3448,0x357b3555,0xb50eb448,0xb57bb555,0x35463495,0x3562357f,0xb522b575,0x34f1b3c9,
0x356d34c4,0x34c4356d,0x356d34c4,0x34c4356d,0x357f34f1,0xb4953522,0x33c93562,0xb575b546,0x357b350e,0xb5553448,0xb57bb50e,0x3555b448,0x35623522,0xb57fb495,0x34f1b546,0xb3c93575,
0x35353535,0xb535b535,0x35353535,0xb535b535,0x34f13546,0xb3c9b575,0xb5623522,0x357fb495,0x34483555,0x350eb57b,0xb448b555,0xb50e357b,0xb3c93562,0x3575b546,0x357fb4f1,0xb495b522,
0xb4c4356d,0x356db4c4,0xb4c4356d,0x356db4c4,0xb5223575,0x34f133c9,0xb5463495,0xb562357f,0xb555357b,0xb448350e,0x3555b57b,0x3448b50e,0xb575357f,0xb5463562,0x3495b3c9,0x3522b4f1,
0xb5803580,0xb5803580,0xb5803580,0xb5803580,0xb575357f,0xb5463562,0x3495b3c9,0x3522b4f1,0xb555357b,0xb448350e,0x3555b57b,0x3448b50e,0xb5223575,0x34f133c9,0xb5463495,0xb562357f,
0xb4c4356d,0x356db4c4,0xb4c4356d,0x356db4c4,0xb3c93562,0x3575b546,0x357fb4f1,0xb495b522,0x34483555,0x350eb57b,0xb448b555,0xb50e357b,0x34f13546,0xb3c9b575,0xb5623522,0x357fb495,
0x35353535,0xb535b535,0x35353535,0xb535b535,0x35623522,0xb57fb495,0x34f1b546,0xb3c93575,0x357b350e,0xb5553448,0xb57bb50e,0x3555b448,0x357f34f1,0xb4953522,0x33c93562,0xb575b546,
0x356d34c4,0x34c4356d,0x356d34c4,0x34c4356d,0x35463495,0x3562357f,0xb522b575,0x34f1b3c9,0x350e3448,0x357b3555,0xb50eb448,0xb57bb555,0x349533c9,0x352234f1,0x3575357f,0x35463562,
0x0,0x0,0x0,0x0,0x35623546,0x357f3575,0x34f13522,0x33c93495,0x3555357b,0x3448350e,0xb555b57b,0xb448b50e,0xb3c934f1,0xb575b522,0x357f3562,0x34953546,
0xb56db4c4,0xb4c4b56d,0xb56db4c4,0xb4c4b56d,0xb546b575,0x356233c9,0x3522b495,0x34f1357f,0x3448b555,0x350e357b,0xb4483555,0xb50eb57b,0x3575b3c9,0xb54634f1,0xb495b57f,0x35223562,
0x35353535,0xb535b535,0x35353535,0xb535b535,0xb495357f,0x3522b562,0xb575b3c9,0x354634f1,0xb57b350e,0x35553448,0x357bb50e,0xb555b448,0xb522b495,0xb4f1357f,0xb5463575,0x3562b3c9,
0x34c4b56d,0xb56d34c4,0x34c4b56d,0xb56d34c4,0x357fb562,0x3495b546,0x33c934f1,0x3575b522,0x350eb448,0x357bb555,0xb50e3448,0xb57b3555,0xb4f13522,0xb3c93495,0x3562b546,0x357fb575,
0xb5803580,0xb5803580,0xb5803580,0xb5803580,0xb4f13522,0xb3c93495,0x3562b546,0x357fb575,0x350eb448,0x357bb555,0xb50e3448,0xb57b3555,0x357fb562,0x3495b546,0x33c934f1,0x3575b522,
0x34c4b56d,0xb56d34c4,0x34c4b56d,0xb56d34c4,0xb522b495,0xb4f1357f,0xb5463575,0x3562b3c9,0xb57b350e,0x35553448,0x357bb50e,0xb555b448,0xb495357f,0x3522b562,0xb575b3c9,0x354634f1,
0x35353535,0xb535b535,0x35353535,0xb535b535,0x3575b3c9,0xb54634f1,0xb495b57f,0x35223562,0x3448b555,0x350e357b,0xb4483555,0xb50eb57b,0xb546b575,0x356233c9,0x3522b495,0x34f1357f,
0xb56db4c4,0xb4c4b56d,0xb56db4c4,0xb4c4b56d,0xb3c934f1,0xb575b522,0x357f3562,0x34953546,0x3555357b,0x3448350e,0xb555b57b,0xb448b50e,0x35623546,0x357f3575,0x34f13522,0x33c93495,
0x3f800000,0x3f800000,0x3f800000,0x3f800000,0x3f800000,0x3f7fffb1,0x3f7ffec4,0x3f7ffd39,0x3f800000,0x3f7ffec4,0x3f7ffb11,0x3f7ff4e6,0x3f800000,0x3f7ffd39,0x3f7ff4e6,0x3f7fe705,
0x3f800000,0x3f7ffb11,0x3f7fec43,0x3f7fd397,0x3f800000,0x3f7ff84a,0x3f7fe129,0x3f7fba9e,0x3f800000,0x3f7ff4e6,0x3f7fd397,0x3f7f9c18,0x3f800000,0x3f7ff0e3,0x3f7fc38f,0x3f7f7808,
0x3f800000,0x3f7fec43,0x3f7fb10f,0x3f7f4e6d,0x3f800000,0x3f7fe705,0x3f7f9c18,0x3f7f1f49,0x3f800000,0x3f7fe129,0x3f7f84ab,0x3f7eea9d,0x3f800000,0x3f7fdaaf,0x3f7f6ac7,0x3f7eb069,
0x3f800000,0x3f7fd397,0x3f7f4e6d,0x3f7e70b0,0x3f800000,0x3f7fcbe2,0x3f7f2f9d,0x3f7e2b72,0x3f800000,0x3f7fc38f,0x3f7f0e58,0x3f7de0b1,0x3f800000,0x3f7fba9e,0x3f7eea9d,0x3f7d906e,
0x3f800000,0x3f7fb10f,0x3f7ec46d,0x3f7d3aac,0x3f800000,0x3f7fa6e3,0x3f7e9bc9,0x3f7cdf6c,0x3f800000,0x3f7f9c18,0x3f7e70b0,0x3f7c7eb0,0x3f800000,0x3f7f90b1,0x3f7e4323,0x3f7c187a,
0x3f800000,0x3f7f84ab,0x3f7e1324,0x3f7baccd,0x3f800000,0x3f7f7808,0x3f7de0b1,0x3f7b3bab,0x3f800000,0x3f7f6ac7,0x3f7dabcc,0x3f7ac516,0x3f800000,0x3f7f5ce9,0x3f7d7474,0x3f7a4910,
0x3f800000,0x3f7f4e6d,0x3f7d3aac,0x3f79c79d,0x3f800000,0x3f7f3f54,0x3f7cfe73,0x3f7940c0,0x3f800000,0x3f7f2f9d,0x3f7cbfc9,0x3f78b47b,0x3f800000,0x3f7f1f49,0x3f7c7eb0,0x3f7822d1,
0x3f800000,0x3f7f0e58,0x3f7c3b28,0x3f778bc5,0x3f800000,0x3f7efcc9,0x3f7bf531,0x3f76ef5b,0x3f800000,0x3f7eea9d,0x3f7baccd,0x3f764d97,0x3f800000,0x3f7ed7d4,0x3f7b61fc,0x3f75a67b,
0x3f800000,0x3f800000,0x3f800000,0x3f800000,0x3f7ffb11,0x3f7ff84a,0x3f7ff4e6,0x3f7ff0e3,0x3f7fec43,0x3f7fe129,0x3f7fd397,0x3f7fc38f,0x3f7fd397,0x3f7fba9e,0x3f7f9c18,0x3f7f7808,
0x3f7fb10f,0x3f7f84ab,0x3f7f4e6d,0x3f7f0e58,0x3f7f84ab,0x3f7f3f54,0x3f7eea9d,0x3f7e868b,0x3f7f4e6d,0x3f7eea9d,0x3f7e70b0,0x3f7de0b1,0x3f7f0e58,0x3f7e868b,0x3f7de0b1,0x3f7d1cdd,
0x3f7ec46d,0x3f7e1324,0x3f7d3aac,0x3f7c3b28,0x3f7e70b0,0x3f7d906e,0x3f7c7eb0,0x3f7b3bab,0x3f7e1324,0x3f7cfe73,0x3f7baccd,0x3f7a1e84,0x3f7dabcc,0x3f7c5d3a,0x3f7ac516,0x3f78e3d6,
0x3f7d3aac,0x3f7baccd,0x3f79c79d,0x3f778bc5,0x3f7cbfc9,0x3f7aed37,0x3f78b47b,0x3f76167a,0x3f7c3b28,0x3f7a1e84,0x3f778bc5,0x3f748422,0x3f7baccd,0x3f7940c0,0x3f764d97,0x3f72d4eb,
0x3f7b14be,0x3f7853f8,0x3f74fa0b,0x3f710908,0x3f7a7302,0x3f77583a,0x3f73913f,0x3f6f20b0,0x3f79c79d,0x3f764d97,0x3f721352,0x3f6d1c1d,0x3f791298,0x3f75341d,0x3f708066,0x3f6afb8c,
0x3f7853f8,0x3f740bdd,0x3f6ed89e,0x3f68bf3c,0x3f778bc5,0x3f72d4eb,0x3f6d1c1d,0x3f666771,0x3f76ba07,0x3f718f57,0x3f6b4b0c,0x3f63f473,0x3f75dec6,0x3f703b37,0x3f696591,0x3f61668a,
0x3f74fa0b,0x3f6ed89e,0x3f676bd8,0x3f5ebe05,0x3f740bdd,0x3f6d67a1,0x3f655e0b,0x3f5bfb34,0x3f731447,0x3f6be858,0x3f633c5a,0x3f591e6a,0x3f721352,0x3f6a5ad9,0x3f6106f2,0x3f5627fe,
0x3f710908,0x3f68bf3c,0x3f5ebe05,0x3f531849,0x3f6ff573,0x3f671599,0x3f5c61c7,0x3f4fefa8,0x3f6ed89e,0x3f655e0b,0x3f59f26a,0x3f4cae79,0x3f6db293,0x3f6398ac,0x3f577026,0x3f495521,
0x3f800000,0x3f800000,0x3f800000,0x3f800000,0x3f7fec43,0x3f7fe705,0x3f7fe129,0x3f7fdaaf,0x3f7fb10f,0x3f7f9c18,0x3f7f84ab,0x3f7f6ac7,0x3f7f4e6d,0x3f7f1f49,0x3f7eea9d,0x3f7eb069,
0x3f7ec46d,0x3f7e70b0,0x3f7e1324,0x3f7dabcc,0x3f7e1324,0x3f7d906e,0x3f7cfe73,0x3f7c5d3a,0x3f7d3aac,0x3f7c7eb0,0x3f7baccd,0x3f7ac516,0x3f7c3b28,0x3f7b3bab,0x3f7a1e84,0x3f78e3d6,
0x3f7b14be,0x3f79c79d,0x3f7853f8,0x3f76ba07,0x3f79c79d,0x3f7822d1,0x3f764d97,0x3f74484b,0x3f7853f8,0x3f764d97,0x3f740bdd,0x3f718f57,0x3f76ba07,0x3f74484b,0x3f718f57,0x3f6e8ff8,
0x3f74fa0b,0x3f721352,0x3f6ed89e,0x3f6b4b0c,0x3f731447,0x3f6faf1b,0x3f6be858,0x3f67c187,0x3f710908,0x3f6d1c1d,0x3f68bf3c,0x3f63f473,0x3f6ed89e,0x3f6a5ad9,0x3f655e0b,0x3f5fe4e9,
0x3f6c835e,0x3f676bd8,0x3f61c598,0x3f5b941a,0x3f6a09a7,0x3f644fac,0x3f5df6be,0x3f570348,0x3f676bd8,0x3f6106f2,0x3f59f26a,0x3f5233c6,0x3f64aa59,0x3f5d924d,0x3f55b993,0x3f4d26fd,
0x3f61c598,0x3f59f26a,0x3f514d3d,0x3f47de65,0x3f5ebe05,0x3f5627fe,0x3f4cae79,0x3f425b89,0x3f5b941a,0x3f5233c6,0x3f47de65,0x3f3ca003,0x3f584853,0x3f4e1689,0x3f42de29,0x3f36ad7f,
0x3f54db31,0x3f49d112,0x3f3daef9,0x3f3085bb,0x3f514d3d,0x3f456439,0x3f385216,0x3f2a2a80,0x3f4d9f02,0x3f40d0da,0x3f32c8c9,0x3f239da9,0x3f49d112,0x3f3c17d9,0x3f2d1469,0x3f1ce11f,
0x3f45e403,0x3f373a23,0x3f273656,0x3f15f6d9,0x3f41d870,0x3f3238aa,0x3f212ff9,0x3f0ee0db,0x3f3daef9,0x3f2d1469,0x3f1b02c6,0x3f07a136,0x3f396842,0x3f27ce61,0x3f14b039,0x3f003a06,
0x3f800000,0x3f800000,0x3f800000,0x3f800000,0x3f7fd397,0x3f7fcbe2,0x3f7fc38f,0x3f7fba9e,0x3f7f4e6d,0x3f7f2f9d,0x3f7f0e58,0x3f7eea9d,0x3f7e70b0,0x3f7e2b72,0x3f7de0b1,0x3f7d906e,
0x3f7d3aac,0x3f7cbfc9,0x3f7c3b28,0x3f7baccd,0x3f7baccd,0x3f7aed37,0x3f7a1e84,0x3f7940c0,0x3f79c79d,0x3f78b47b,0x3f778bc5,0x3f764d97,0x3f778bc5,0x3f76167a,0x3f748422,0x3f72d4eb,
0x3f74fa0b,0x3f731447,0x3f710908,0x3f6ed89e,0x3f721352,0x3f6faf1b,0x3f6d1c1d,0x3f6a5ad9,0x3f6ed89e,0x3f6be858,0x3f68bf3c,0x3f655e0b,0x3f6b4b0c,0x3f67c187,0x3f63f473,0x3f5fe4e9,
0x3f676bd8,0x3f633c5a,0x3f5ebe05,0x3f59f26a,0x3f633c5a,0x3f5e5aa6,0x3f591e6a,0x3f5389c7,0x3f5ebe05,0x3f591e6a,0x3f531849,0x3f4cae79,0x3f59f26a,0x3f5389c7,0x3f4cae79,0x3f456439,
0x3f54db31,0x3f4d9f02,0x3f45e403,0x3f3daef9,0x3f4f7a1f,0x3f476085,0x3f3ebc1b,0x3f3592e7,0x3f49d112,0x3f40d0da,0x3f373a23,0x3f2d1469,0x3f43e200,0x3f39f2ac,0x3f2f61a5,0x3f24381a,
0x3f3daef9,0x3f32c8c9,0x3f273656,0x3f1b02c6,0x3f373a23,0x3f2b561b,0x3f1ebc12,0x3f11796b,0x3f3085bb,0x3f239da9,0x3f15f6d9,0x3f07a136,0x3f299415,0x3f1ba299,0x3f0cead0,0x3efafef7,
0x3f226799,0x3f13682a,0x3f039c3d,0x3ee63375,0x3f1b02c6,0x3f0af1b7,0x3ef41f07,0x3ed0eb2a,0x3f13682a,0x3f0242b1,0x3ee0924f,0x3ebb31a0,0x3f0b9a6b,0x3ef2bd43,0x3ecc9b8b,0x3ea5129f,
0x3f039c3d,0x3ee0924f,0x3eb8442a,0x3e8e9a22,0x3ef6e0cb,0x3ece0bea,0x3ea395c5,0x3e6fa8b2,0x3ee63375,0x3ebb31a0,0x3e8e9a22,0x3e419b37,0x3ed53641,0x3ea80b1f,0x3e72b651,0x3e1324ca,
0x3f800000,0x3f800000,0x3f800000,0x3f800000,0x3f7fb10f,0x3f7fa6e3,0x3f7f9c18,0x3f7f90b1,0x3f7ec46d,0x3f7e9bc9,0x3f7e70b0,0x3f7e4323,0x3f7d3aac,0x3f7cdf6c,0x3f7c7eb0,0x3f7c187a,
0x3f7b14be,0x3f7a7302,0x3f79c79d,0x3f791298,0x3f7853f8,0x3f77583a,0x3f764d97,0x3f75341d,0x3f74fa0b,0x3f73913f,0x3f721352,0x3f708066,0x3f710908,0x3f6f20b0,0x3f6d1c1d,0x3f6afb8c,
0x3f6c835e,0x3f6a09a7,0x3f676bd8,0x3f64aa59,0x3f676bd8,0x3f644fac,0x3f6106f2,0x3f5d924d,0x3f61c598,0x3f5df6be,0x3f59f26a,0x3f55b993,0x3f5b941a,0x3f570348,0x3f5233c6,0x3f4d26fd,
0x3f54db31,0x3f4f7a1f,0x3f49d112,0x3f43e200,0x3f4d9f02,0x3f476085,0x3f40d0da,0x3f39f2ac,0x3f45e403,0x3f3ebc1b,0x3f373a23,0x3f2f61a5,0x3f3daef9,0x3f3592e7,0x3f2d1469,0x3f24381a,
0x3f3504f3,0x3f2beb4a,0x3f226799,0x3f187fc0,0x3f2beb4a,0x3f21cbfb,0x3f173c07,0x3f0c42c9,0x3f226799,0x3f173c07,0x3f0b9a6b,0x3eff17b2,0x3f187fc0,0x3f0c42c9,0x3eff17b2,0x3ee4cbfe,
0x3f0e39da,0x3f00e7e4,0x3ee63375,0x3ec9b953,0x3f039c3d,0x3eea6681,0x3ecc9b8b,0x3eadf73c,0x3ef15aea,0x3ed25a09,0x3eb263ef,0x3e919ddd,0x3edae880,0x3eb9bb1e,0x3e97a117,0x3e698bba,
0x3ec3ef15,0x3ea09ae5,0x3e78cfcc,0x3e2f10a2,0x3eac7cd4,0x3e870ada,0x3e419b37,0x3de7fa9a,0x3e94a031,0x3e5a3997,0x3e09cf86,0x3d621469,0x3e78cfcc,0x3e25c58c,0x3da3308c,0xbb490fc6,
0x3e47c5c2,0x3de1bc2e,0x3cc90ab0,0xbd7b2b74,0x3e164083,0x3d6ea038,0xbcfb49ba,0xbdf475c0,0x3dc8bd36,0x3bc90f88,0xbdafb680,0xbe354098,0x3d48fb30,0xbd3c6dd5,0xbe1008b7,0xbe6fa8b2,
0x3f800000,0x3f800000,0x3f800000,0x3f800000,0x3f7f84ab,0x3f7f7808,0x3f7f6ac7,0x3f7f5ce9,0x3f7e1324,0x3f7de0b1,0x3f7dabcc,0x3f7d7474,0x3f7baccd,0x3f7b3bab,0x3f7ac516,0x3f7a4910,
0x3f7853f8,0x3f778bc5,0x3f76ba07,0x3f75dec6,0x3f740bdd,0x3f72d4eb,0x3f718f57,0x3f703b37,0x3f6ed89e,0x3f6d1c1d,0x3f6b4b0c,0x3f696591,0x3f68bf3c,0x3f666771,0x3f63f473,0x3f61668a,
0x3f61c598,0x3f5ebe05,0x3f5b941a,0x3f584853,0x3f59f26a,0x3f5627fe,0x3f5233c6,0x3f4e1689,0x3f514d3d,0x3f4cae79,0x3f47de65,0x3f42de29,0x3f47de65,0x3f425b89,0x3f3ca003,0x3f36ad7f,
0x3f3daef9,0x3f373a23,0x3f3085bb,0x3f299415,0x3f32c8c9,0x3f2b561b,0x3f239da9,0x3f1ba299,0x3f273656,0x3f1ebc12,0x3f15f6d9,0x3f0cead0,0x3f1b02c6,0x3f11796b,0x3f07a136,0x3efafef7,
0x3f0e39da,0x3f039c3d,0x3ef15aea,0x3edae880,0x3f00e7e4,0x3eea6681,0x3ed25a09,0x3eb9bb1e,0x3ee63375,0x3ecc9b8b,0x3eb263ef,0x3e97a117,0x3ec9b953,0x3eadf73c,0x3e919ddd,0x3e698bba,
0x3eac7cd4,0x3e8e9a22,0x3e605c13,0x3e22abb6,0x3e8e9a22,0x3e5d4b1a,0x3e1c76de,0x3db5f8da,0x3e605c13,0x3e1c76de,0x3dafb680,0x3c96c9b6,0x3e22abb6,0x3db5f8da,0x3c96c9b6,0xbd55880e,
0x3dc8bd36,0x3cc90ab0,0xbd48fb30,0xbdfab273,0x3d16c32c,0xbd2351cb,0xbdee3876,0xbe44b0b9,0xbcc90ab0,0xbdd53db9,0xbe3b6ecf,0xbe8586ce,0xbdafb680,0xbe2bf804,0xbe7ee6e1,0xbea80b1f,
0xbe164083,0xbe6c9a7f,0xbea09ae5,0xbec9b953,0xbe541501,0xbe9620d2,0xbec1071e,0xbeea6681,0xbe888e93,0xbeb554ec,0xbee0924f,0xbf04f484,0xbea68f12,0xbed3c867,0xbeff17b2,0xbf140c5f,
0x3f800000,0x3f800000,0x3f800000,0x3f800000,0x3f7f4e6d,0x3f7f3f54,0x3f7f2f9d,0x3f7f1f49,0x3f7d3aac,0x3f7cfe73,0x3f7cbfc9,0x3f7c7eb0,0x3f79c79d,0x3f7940c0,0x3f78b47b,0x3f7822d1,
0x3f74fa0b,0x3f740bdd,0x3f731447,0x3f721352,0x3f6ed89e,0x3f6d67a1,0x3f6be858,0x3f6a5ad9,0x3f676bd8,0x3f655e0b,0x3f633c5a,0x3f6106f2,0x3f5ebe05,0x3f5bfb34,0x3f591e6a,0x3f5627fe,
0x3f54db31,0x3f514d3d,0x3f4d9f02,0x3f49d112,0x3f49d112,0x3f456439,0x3f40d0da,0x3f3c17d9,0x3f3daef9,0x3f385216,0x3f32c8c9,0x3f2d1469,0x3f3085bb,0x3f2a2a80,0x3f239da9,0x3f1ce11f,
0x3f226799,0x3f1b02c6,0x3f13682a,0x3f0b9a6b,0x3f13682a,0x3f0af1b7,0x3f0242b1,0x3ef2bd43,0x3f039c3d,0x3ef41f07,0x3ee0924f,0x3ecc9b8b,0x3ee63375,0x3ed0eb2a,0x3ebb31a0,0x3ea5129f,
0x3ec3ef15,0x3eac7cd4,0x3e94a031,0x3e78cfcc,0x3ea09ae5,0x3e870ada,0x3e5a3997,0x3e25c58c,0x3e78cfcc,0x3e419b37,0x3e09cf86,0x3da3308c,0x3e2f10a2,0x3de7fa9a,0x3d621469,0xbb490fc6,
0x3dc8bd36,0x3d16c32c,0xbcc90ab0,0xbdafb680,0x3cc90ab0,0xbd2351cb,0xbdd53db9,0xbe2bf804,0xbd48fb30,0xbdee3876,0xbe3b6ecf,0xbe7ee6e1,0xbdfab273,0xbe44b0b9,0xbe8586ce,0xbea80b1f,
0xbe47c5c2,0xbe888e93,0xbeac7cd4,0xbecf7bca,0xbe888e93,0xbeadf73c,0xbed25a09,0xbef58035,0xbeac7cd4,0xbed25a09,0xbef6e0cb,0xbf0cead0,0xbecf7bca,0xbef58035,0xbf0cead0,0xbf1e1e22,
0xbef15aea,0xbf0b9a6b,0xbf1d7fd1,0xbf2e3bde,0xbf08f59b,0xbf1ba299,0xbf2d1469,0xbf3d27b8,0xbf187fc0,0xbf2ac082,0xbf3b8f3b,0xbf4ac77f,0xbf273656,0xbf38dd65,0xbf48d8b3,0xbf570348,
0x3f800000,0x3f800000,0x3f800000,0x3f800000,0x3f7f0e58,0x3f7efcc9,0x3f7eea9d,0x3f7ed7d4,0x3f7c3b28,0x3f7bf531,0x3f7baccd,0x3f7b61fc,0x3f778bc5,0x3f76ef5b,0x3f764d97,0x3f75a67b,
0x3f710908,0x3f6ff573,0x3f6ed89e,0x3f6db293,0x3f68bf3c,0x3f671599,0x3f655e0b,0x3f6398ac,0x3f5ebe05,0x3f5c61c7,0x3f59f26a,0x3f577026,0x3f531849,0x3f4fefa8,0x3f4cae79,0x3f495521,
0x3f45e403,0x3f41d870,0x3f3daef9,0x3f396842,0x3f373a23,0x3f3238aa,0x3f2d1469,0x3f27ce61,0x3f273656,0x3f212ff9,0x3f1b02c6,0x3f14b039,0x3f15f6d9,0x3f0ee0db,0x3f07a136,0x3f003a06,
0x3f039c3d,0x3ef6e0cb,0x3ee63375,0x3ed53641,0x3ee0924f,0x3ece0bea,0x3ebb31a0,0x3ea80b1f,0x3eb8442a,0x3ea395c5,0x3e8e9a22,0x3e72b651,0x3e8e9a22,0x3e6fa8b2,0x3e419b37,0x3e1324ca,
0x3e47c5c2,0x3e164083,0x3dc8bd36,0x3d48fb30,0x3de1bc2e,0x3d6ea038,0x3bc90f88,0xbd3c6dd5,0x3cc90ab0,0xbcfb49ba,0xbdafb680,0xbe1008b7,0xbd7b2b74,0xbdf475c0,0xbe354098,0xbe6fa8b2,
0xbe164083,0xbe541501,0xbe888e93,0xbea68f12,0xbe6c9a7f,0xbe9620d2,0xbeb554ec,0xbed3c867,0xbea09ae5,0xbec1071e,0xbee0924f,0xbeff17b2,0xbec9b953,0xbeea6681,0xbf04f484,0xbf140c5f,
0xbef15aea,0xbf08f59b,0xbf187fc0,0xbf273656,0xbf0b9a6b,0xbf1ba299,0xbf2ac082,0xbf38dd65,0xbf1d7fd1,0xbf2d1469,0xbf3b8f3b,0xbf48d8b3,0xbf2e3bde,0xbf3d27b8,0xbf4ac77f,0xbf570348,
0xbf3daef9,0xbf4bbbf8,0xbf584853,0xbf633c5a,0xbf4bbbf8,0xbf58b3a1,0xbf63f473,0xbf6d67a1,0xbf584853,0xbf63f473,0xbf6db293,0xbf756d97,0xbf633c5a,0xbf6d67a1,0xbf756d97,0xbf7b3bab,
0x0,0x0,0x0,0x0,0x0,0x3b490fc6,0x3bc90f88,0x3c16cb58,0x0,0x3bc90f88,0x3c490e90,0x3c96c9b6,0x0,0x3c16cb58,0x3c96c9b6,0x3ce22a7a,
0x0,0x3c490e90,0x3cc90ab0,0x3d16c32c,0x0,0x3c7b514b,0x3cfb49ba,0x3d3c6dd5,0x0,0x3c96c9b6,0x3d16c32c,0x3d621469,0x0,0x3cafea69,0x3d2fe007,0x3d83db0a,
0x0,0x3cc90ab0,0x3d48fb30,0x3d96a905,0x0,0x3ce22a7a,0x3d621469,0x3da973ba,0x0,0x3cfb49ba,0x3d7b2b74,0x3dbc3ac3,0x0,0x3d0a342f,0x3d8a200a,0x3dcefdb7,
0x0,0x3d16c32c,0x3d96a905,0x3de1bc2e,0x0,0x3d2351cb,0x3da3308c,0x3df475c0,0x0,0x3d2fe007,0x3dafb680,0x3e039502,0x0,0x3d3c6dd5,0x3dbc3ac3,0x3e0cec4a,
0x0,0x3d48fb30,0x3dc8bd36,0x3e164083,0x0,0x3d55880e,0x3dd53db9,0x3e1f917b,0x0,0x3d621469,0x3de1bc2e,0x3e28defc,0x0,0x3d6ea038,0x3dee3876,0x3e3228d4,
0x0,0x3d7b2b74,0x3dfab273,0x3e3b6ecf,0x0,0x3d83db0a,0x3e039502,0x3e44b0b9,0x0,0x3d8a200a,0x3e09cf86,0x3e4dee60,0x0,0x3d9064b4,0x3e1008b7,0x3e57278f,
0x0,0x3d96a905,0x3e164083,0x3e605c13,0x0,0x3d9cecf9,0x3e1c76de,0x3e698bba,0x0,0x3da3308c,0x3e22abb6,0x3e72b651,0x0,0x3da973ba,0x3e28defc,0x3e7bdba4,
0x0,0x3dafb680,0x3e2f10a2,0x3e827dc0,0x0,0x3db5f8da,0x3e354098,0x3e870ada,0x0,0x3dbc3ac3,0x3e3b6ecf,0x3e8b9507,0x0,0x3dc27c39,0x3e419b37,0x3e901c2c,
0x0,0x0,0x0,0x0,0x3c490e90,0x3c7b514b,0x3c96c9b6,0x3cafea69,0x3cc90ab0,0x3cfb49ba,0x3d16c32c,0x3d2fe007,0x3d16c32c,0x3d3c6dd5,0x3d621469,0x3d83db0a,
0x3d48fb30,0x3d7b2b74,0x3d96a905,0x3dafb680,0x3d7b2b74,0x3d9cecf9,0x3dbc3ac3,0x3ddb7d37,0x3d96a905,0x3dbc3ac3,0x3de1bc2e,0x3e039502,0x3dafb680,0x3ddb7d37,0x3e039502,0x3e195be0,
0x3dc8bd36,0x3dfab273,0x3e164083,0x3e2f10a2,0x3de1bc2e,0x3e0cec4a,0x3e28defc,0x3e44b0b9,0x3dfab273,0x3e1c76de,0x3e3b6ecf,0x3e5a3997,0x3e09cf86,0x3e2bf804,0x3e4dee60,0x3e6fa8b2,
0x3e164083,0x3e3b6ecf,0x3e605c13,0x3e827dc0,0x3e22abb6,0x3e4ada4f,0x3e72b651,0x3e8d17c0,0x3e2f10a2,0x3e5a3997,0x3e827dc0,0x3e97a117,0x3e3b6ecf,0x3e698bba,0x3e8b9507,0x3ea21887,
0x3e47c5c2,0x3e78cfcc,0x3e94a031,0x3eac7cd4,0x3e541501,0x3e840270,0x3e9d9e78,0x3eb6ccc3,0x3e605c13,0x3e8b9507,0x3ea68f12,0x3ec1071e,0x3e6c9a7f,0x3e931f35,0x3eaf713a,0x3ecb2aae,
0x3e78cfcc,0x3e9aa086,0x3eb8442a,0x3ed53641,0x3e827dc0,0x3ea21887,0x3ec1071e,0x3edf28a9,0x3e888e93,0x3ea986c4,0x3ec9b953,0x3ee900b7,0x3e8e9a22,0x3eb0eacb,0x3ed25a09,0x3ef2bd43,
0x3e94a031,0x3eb8442a,0x3edae880,0x3efc5d27,0x3e9aa086,0x3ebf926f,0x3ee363fa,0x3f02ef9f,0x3ea09ae5,0x3ec6d529,0x3eebcbbb,0x3f07a136,0x3ea68f12,0x3ece0bea,0x3ef41f07,0x3f0c42c9,
0x3eac7cd4,0x3ed53641,0x3efc5d27,0x3f10d3cd,0x3eb263ef,0x3edc53c1,0x3f0242b1,0x3f1553b7,0x3eb8442a,0x3ee363fa,0x3f064b82,0x3f19c200,0x3ebe1d4a,0x3eea6681,0x3f0a48ad,0x3f1e1e22,
0x0,0x0,0x0,0x0,0x3cc90ab0,0x3ce22a7a,0x3cfb49ba,0x3d0a342f,0x3d48fb30,0x3d621469,0x3d7b2b74,0x3d8a200a,0x3d96a905,0x3da973ba,0x3dbc3ac3,0x3dcefdb7,
0x3dc8bd36,0x3de1bc2e,0x3dfab273,0x3e09cf86,0x3dfab273,0x3e0cec4a,0x3e1c76de,0x3e2bf804,0x3e164083,0x3e28defc,0x3e3b6ecf,0x3e4dee60,0x3e2f10a2,0x3e44b0b9,0x3e5a3997,0x3e6fa8b2,
0x3e47c5c2,0x3e605c13,0x3e78cfcc,0x3e888e93,0x3e605c13,0x3e7bdba4,0x3e8b9507,0x3e9920fe,0x3e78cfcc,0x3e8b9507,0x3e9aa086,0x3ea986c4,0x3e888e93,0x3e9920fe,0x3ea986c4,0x3eb9bb1e,
0x3e94a031,0x3ea68f12,0x3eb8442a,0x3ec9b953,0x3ea09ae5,0x3eb3dca5,0x3ec6d529,0x3ed97cb9,0x3eac7cd4,0x3ec1071e,0x3ed53641,0x3ee900b7,0x3eb8442a,0x3ece0bea,0x3ee363fa,0x3ef840c8,
0x3ec3ef15,0x3edae880,0x3ef15aea,0x3f039c3d,0x3ecf7bca,0x3ee79a5d,0x3eff17b2,0x3f0af1b7,0x3edae880,0x3ef41f07,0x3f064b82,0x3f121eb0,0x3ee63375,0x3f003a06,0x3f0cead0,0x3f19210f,
0x3ef15aea,0x3f064b82,0x3f13682a,0x3f1ff6cb,0x3efc5d27,0x3f0c42c9,0x3f19c200,0x3f269de3,0x3f039c3d,0x3f121eb0,0x3f1ff6cb,0x3f2d1469,0x3f08f59b,0x3f17de12,0x3f26050a,0x3f33587a,
0x3f0e39da,0x3f1d7fd1,0x3f2beb4a,0x3f396842,0x3f13682a,0x3f2302d3,0x3f31a81d,0x3f3f41fc,0x3f187fc0,0x3f286605,0x3f373a23,0x3f44e3f5,0x3f1d7fd1,0x3f2da859,0x3f3ca003,0x3f4a4c87,
0x3f226799,0x3f32c8c9,0x3f41d870,0x3f4f7a1f,0x3f273656,0x3f37c655,0x3f46e22a,0x3f546b3b,0x3f2beb4a,0x3f3ca003,0x3f4bbbf8,0x3f591e6a,0x3f3085bb,0x3f4154e1,0x3f5064af,0x3f5d924d,
0x0,0x0,0x0,0x0,0x3d16c32c,0x3d2351cb,0x3d2fe007,0x3d3c6dd5,0x3d96a905,0x3da3308c,0x3dafb680,0x3dbc3ac3,0x3de1bc2e,0x3df475c0,0x3e039502,0x3e0cec4a,
0x3e164083,0x3e22abb6,0x3e2f10a2,0x3e3b6ecf,0x3e3b6ecf,0x3e4ada4f,0x3e5a3997,0x3e698bba,0x3e605c13,0x3e72b651,0x3e827dc0,0x3e8b9507,0x3e827dc0,0x3e8d17c0,0x3e97a117,0x3ea21887,
0x3e94a031,0x3ea09ae5,0x3eac7cd4,0x3eb8442a,0x3ea68f12,0x3eb3dca5,0x3ec1071e,0x3ece0bea,0x3eb8442a,0x3ec6d529,0x3ed53641,0x3ee363fa,0x3ec9b953,0x3ed97cb9,0x3ee900b7,0x3ef840c8,
0x3edae880,0x3eebcbbb,0x3efc5d27,0x3f064b82,0x3eebcbbb,0x3efdbabb,0x3f07a136,0x3f102dd5,0x3efc5d27,0x3f07a136,0x3f10d3cd,0x3f19c200,0x3f064b82,0x3f102dd5,0x3f19c200,0x3f2302d3,
0x3f0e39da,0x3f187fc0,0x3f226799,0x3f2beb4a,0x3f15f6d9,0x3f209393,0x3f2ac082,0x3f34768f,0x3f1d7fd1,0x3f286605,0x3f32c8c9,0x3f3ca003,0x3f24d225,0x3f2ff3e6,0x3f3a7ca4,0x3f446337,
0x3f2beb4a,0x3f373a23,0x3f41d870,0x3f4bbbf8,0x3f32c8c9,0x3f3e35c5,0x3f48d8b3,0x3f52a649,0x3f396842,0x3f44e3f5,0x3f4f7a1f,0x3f591e6a,0x3f3fc767,0x3f4b41fa,0x3f55b993,0x3f5f20db,
0x3f45e403,0x3f514d3d,0x3f5b941a,0x3f64aa59,0x3f4bbbf8,0x3f570348,0x3f6106f2,0x3f69b7e4,0x3f514d3d,0x3f5c61c7,0x3f660f88,0x3f6e46be,0x3f5695e5,0x3f61668a,0x3f6aab7b,0x3f725470,
0x3f5b941a,0x3f660f88,0x3f6ed89e,0x3f75dec6,0x3f604621,0x3f6a5ad9,0x3f7294f8,0x3f78e3d6,0x3f64aa59,0x3f6e46be,0x3f75dec6,0x3f7b61fc,0x3f68bf3c,0x3f71d19f,0x3f78b47b,0x3f7d57de,
0x0,0x0,0x0,0x0,0x3d48fb30,0x3d55880e,0x3d621469,0x3d6ea038,0x3dc8bd36,0x3dd53db9,0x3de1bc2e,0x3dee3876,0x3e164083,0x3e1f917b,0x3e28defc,0x3e3228d4,
0x3e47c5c2,0x3e541501,0x3e605c13,0x3e6c9a7f,0x3e78cfcc,0x3e840270,0x3e8b9507,0x3e931f35,0x3e94a031,0x3e9d9e78,0x3ea68f12,0x3eaf713a,0x3eac7cd4,0x3eb6ccc3,0x3ec1071e,0x3ecb2aae,
0x3ec3ef15,0x3ecf7bca,0x3edae880,0x3ee63375,0x3edae880,0x3ee79a5d,0x3ef41f07,0x3f003a06,0x3ef15aea,0x3eff17b2,0x3f064b82,0x3f0cead0,0x3f039c3d,0x3f0af1b7,0x3f121eb0,0x3f19210f,
0x3f0e39da,0x3f15f6d9,0x3f1d7fd1,0x3f24d225,0x3f187fc0,0x3f209393,0x3f286605,0x3f2ff3e6,0x3f226799,0x3f2ac082,0x3f32c8c9,0x3f3a7ca4,0x3f2beb4a,0x3f34768f,0x3f3ca003,0x3f446337,
0x3f3504f3,0x3f3daef9,0x3f45e403,0x3f4d9f02,0x3f3daef9,0x3f466354,0x3f4e8d90,0x3f5627fe,0x3f45e403,0x3f4e8d90,0x3f5695e5,0x3f5df6be,0x3f4d9f02,0x3f5627fe,0x3f5df6be,0x3f650479,
0x3f54db31,0x3f5d2d53,0x3f64aa59,0x3f6b4b0c,0x3f5b941a,0x3f6398ac,0x3f6aab7b,0x3f70c501,0x3f61c598,0x3f696591,0x3f6ff573,0x3f756d97,0x3f676bd8,0x3f6e8ff8,0x3f748422,0x3f7940c0,
0x3f6c835e,0x3f731447,0x3f7853f8,0x3f7c3b28,0x3f710908,0x3f76ef5b,0x3f7b61fc,0x3f7e5a38,0x3f74fa0b,0x3f7a1e84,0x3f7dabcc,0x3f7f9c18,0x3f7853f8,0x3f7c9f8a,0x3f7f2f9d,0x3f7fffb1,
0x3f7b14be,0x3f7e70b0,0x3f7fec43,0x3f7f84ab,0x3f7d3aac,0x3f7f90b1,0x3f7fe129,0x3f7e2b72,0x3f7ec46d,0x3f7ffec4,0x3f7f0e58,0x3f7bf531,0x3f7fb10f,0x3f7fba9e,0x3f7d7474,0x3f78e3d6,
0x0,0x0,0x0,0x0,0x3d7b2b74,0x3d83db0a,0x3d8a200a,0x3d9064b4,0x3dfab273,0x3e039502,0x3e09cf86,0x3e1008b7,0x3e3b6ecf,0x3e44b0b9,0x3e4dee60,0x3e57278f,
0x3e78cfcc,0x3e827dc0,0x3e888e93,0x3e8e9a22,0x3e9aa086,0x3ea21887,0x3ea986c4,0x3eb0eacb,0x3eb8442a,0x3ec1071e,0x3ec9b953,0x3ed25a09,0x3ed53641,0x3edf28a9,0x3ee900b7,0x3ef2bd43,
0x3ef15aea,0x3efc5d27,0x3f039c3d,0x3f08f59b,0x3f064b82,0x3f0c42c9,0x3f121eb0,0x3f17de12,0x3f13682a,0x3f19c200,0x3f1ff6cb,0x3f26050a,0x3f1ff6cb,0x3f269de3,0x3f2d1469,0x3f33587a,
0x3f2beb4a,0x3f32c8c9,0x3f396842,0x3f3fc767,0x3f373a23,0x3f3e35c5,0x3f44e3f5,0x3f4b41fa,0x3f41d870,0x3f48d8b3,0x3f4f7a1f,0x3f55b993,0x3f4bbbf8,0x3f52a649,0x3f591e6a,0x3f5f20db,
0x3f54db31,0x3f5b941a,0x3f61c598,0x3f676bd8,0x3f5d2d53,0x3f6398ac,0x3f696591,0x3f6e8ff8,0x3f64aa59,0x3f6aab7b,0x3f6ff573,0x3f748422,0x3f6b4b0c,0x3f70c501,0x3f756d97,0x3f7940c0,
0x3f710908,0x3f75dec6,0x3f79c79d,0x3f7cbfc9,0x3f75dec6,0x3f79f35e,0x3f7cfe73,0x3f7efcc9,0x3f79c79d,0x3f7cfe73,0x3f7f0e58,0x3f7ff4e6,0x3f7cbfc9,0x3f7efcc9,0x3f7ff4e6,0x3f7fa6e3,
0x3f7ec46d,0x3f7fec43,0x3f7fb10f,0x3f7e1324,0x3f7fd397,0x3f7fcbe2,0x3f7e4323,0x3f7b3bab,0x3f7fec43,0x3f7e9bc9,0x3f7baccd,0x3f772417,0x3f7f0e58,0x3f7c5d3a,0x3f77f110,0x3f71d19f,
0x3f7d3aac,0x3f791298,0x3f731447,0x3f6b4b0c,0x3f7a7302,0x3f74bf62,0x3f6d1c1d,0x3f6398ac,0x3f76ba07,0x3f6f6830,0x3f660f88,0x3f5ac450,0x3f721352,0x3f6912ae,0x3f5df6be,0x3f50d937,
0x0,0x0,0x0,0x0,0x3d96a905,0x3d9cecf9,0x3da3308c,0x3da973ba,0x3e164083,0x3e1c76de,0x3e22abb6,0x3e28defc,0x3e605c13,0x3e698bba,0x3e72b651,0x3e7bdba4,
0x3e94a031,0x3e9aa086,0x3ea09ae5,0x3ea68f12,0x3eb8442a,0x3ebf926f,0x3ec6d529,0x3ece0bea,0x3edae880,0x3ee363fa,0x3eebcbbb,0x3ef41f07,0x3efc5d27,0x3f02ef9f,0x3f07a136,0x3f0c42c9,
0x3f0e39da,0x3f13682a,0x3f187fc0,0x3f1d7fd1,0x3f1d7fd1,0x3f2302d3,0x3f286605,0x3f2da859,0x3f2beb4a,0x3f31a81d,0x3f373a23,0x3f3ca003,0x3f396842,0x3f3f41fc,0x3f44e3f5,0x3f4a4c87,
0x3f45e403,0x3f4bbbf8,0x3f514d3d,0x3f5695e5,0x3f514d3d,0x3f570348,0x3f5c61c7,0x3f61668a,0x3f5b941a,0x3f6106f2,0x3f660f88,0x3f6aab7b,0x3f64aa59,0x3f69b7e4,0x3f6e46be,0x3f725470,
0x3f6c835e,0x3f710908,0x3f74fa0b,0x3f7853f8,0x3f731447,0x3f76ef5b,0x3f7a1e84,0x3f7c9f8a,0x3f7853f8,0x3f7b61fc,0x3f7dabcc,0x3f7f2f9d,0x3f7c3b28,0x3f7e5a38,0x3f7f9c18,0x3f7fffb1,
0x3f7ec46d,0x3f7fd397,0x3f7fec43,0x3f7f0e58,0x3f7fec43,0x3f7fcbe2,0x3f7e9bc9,0x3f7c5d3a,0x3f7fb10f,0x3f7e4323,0x3f7baccd,0x3f77f110,0x3f7e1324,0x3f7b3bab,0x3f772417,0x3f71d19f,
0x3f7b14be,0x3f76ba07,0x3f710908,0x3f6a09a7,0x3f76ba07,0x3f70c501,0x3f696591,0x3f60a6cf,0x3f710908,0x3f696591,0x3f604621,0x3f55b993,0x3f6a09a7,0x3f60a6cf,0x3f55b993,0x3f495521,
0x3f61c598,0x3f5695e5,0x3f49d112,0x3f3b8f3b,0x3f584853,0x3f4b41fa,0x3f3ca003,0x3f2c800f,0x3f4d9f02,0x3f3ebc1b,0x3f2e3bde,0x3f1c420c,0x3f41d870,0x3f311722,0x3f1ebc12,0x3f0af1b7,
0x0,0x0,0x0,0x0,0x3dafb680,0x3db5f8da,0x3dbc3ac3,0x3dc27c39,0x3e2f10a2,0x3e354098,0x3e3b6ecf,0x3e419b37,0x3e827dc0,0x3e870ada,0x3e8b9507,0x3e901c2c,
0x3eac7cd4,0x3eb263ef,0x3eb8442a,0x3ebe1d4a,0x3ed53641,0x3edc53c1,0x3ee363fa,0x3eea6681,0x3efc5d27,0x3f0242b1,0x3f064b82,0x3f0a48ad,0x3f10d3cd,0x3f1553b7,0x3f19c200,0x3f1e1e22,
0x3f226799,0x3f273656,0x3f2beb4a,0x3f3085bb,0x3f32c8c9,0x3f37c655,0x3f3ca003,0x3f4154e1,0x3f41d870,0x3f46e22a,0x3f4bbbf8,0x3f5064af,0x3f4f7a1f,0x3f546b3b,0x3f591e6a,0x3f5d924d,
0x3f5b941a,0x3f604621,0x3f64aa59,0x3f68bf3c,0x3f660f88,0x3f6a5ad9,0x3f6e46be,0x3f71d19f,0x3f6ed89e,0x3f7294f8,0x3f75dec6,0x3f78b47b,0x3f75dec6,0x3f78e3d6,0x3f7b61fc,0x3f7d57de,
0x3f7b14be,0x3f7d3aac,0x3f7ec46d,0x3f7fb10f,0x3f7e70b0,0x3f7f90b1,0x3f7ffec4,0x3f7fba9e,0x3f7fec43,0x3f7fe129,0x3f7f0e58,0x3f7d7474,0x3f7f84ab,0x3f7e2b72,0x3f7bf531,0x3f78e3d6,
0x3f7d3aac,0x3f7a7302,0x3f76ba07,0x3f721352,0x3f791298,0x3f74bf62,0x3f6f6830,0x3f6912ae,0x3f731447,0x3f6d1c1d,0x3f660f88,0x3f5df6be,0x3f6b4b0c,0x3f6398ac,0x3f5ac450,0x3f50d937,
0x3f61c598,0x3f584853,0x3f4d9f02,0x3f41d870,0x3f5695e5,0x3f4b41fa,0x3f3ebc1b,0x3f311722,0x3f49d112,0x3f3ca003,0x3f2e3bde,0x3f1ebc12,0x3f3b8f3b,0x3f2c800f,0x3f1c420c,0x3f0af1b7,
0x3f2beb4a,0x3f1b02c6,0x3f08f59b,0x3eebcbbb,0x3f1b02c6,0x3f084b92,0x3ee900b7,0x3ebf926f,0x3f08f59b,0x3ee900b7,0x3ebe1d4a,0x3e919ddd,0x3eebcbbb,0x3ebf926f,0x3e919ddd,0x3e44b0b9,
0x3f803f80,0x3f803f80,0x3f803f80,0x3f803f80,0x3f7b3f80,0x3f553f6d,0xbe480000,0xbf0ebec4,0x3f6d3f80,0x3ec43f35,0xbf6dbf80,0xbec4bf35,0x3f553f80,0xbe483ec4,0x3f0e0000,0x3f7b3f6d,
0x3f353f80,0xbf350000,0x3f353f80,0xbf350000,0x3f0e3f80,0xbf7bbec4,0xbf550000,0xbe48bf6d,0x3ec43f80,0xbf6dbf35,0xbec4bf80,0x3f6d3f35,0x3e483f80,0xbf0ebf6d,0x3f7b0000,0xbf553ec4,
0x3f80,0xbf80,0x3f80,0xbf80,0xbe483f80,0x3f0ebf6d,0xbf7b0000,0x3f553ec4,0xbec43f80,0x3f6dbf35,0x3ec4bf80,0xbf6d3f35,0xbf0e3f80,0x3f7bbec4,0x3f550000,0x3e48bf6d,
0xbf353f80,0x3f350000,0xbf353f80,0x3f350000,0xbf553f80,0x3e483ec4,0xbf0e0000,0xbf7b3f6d,0xbf6d3f80,0xbec43f35,0x3f6dbf80,0x3ec4bf35,0xbf7b3f80,0xbf553f6d,0x3e480000,0x3f0ebec4,
0xbf803f80,0xbf803f80,0xbf803f80,0xbf803f80,0xbf7b3f80,0xbf553f6d,0x3e480000,0x3f0ebec4,0xbf6d3f80,0xbec43f35,0x3f6dbf80,0x3ec4bf35,0xbf553f80,0x3e483ec4,0xbf0e0000,0xbf7b3f6d,
0xbf353f80,0x3f350000,0xbf353f80,0x3f350000,0xbf0e3f80,0x3f7bbec4,0x3f550000,0x3e48bf6d,0xbec43f80,0x3f6dbf35,0x3ec4bf80,0xbf6d3f35,0xbe483f80,0x3f0ebf6d,0xbf7b0000,0x3f553ec4,
0x3f80,0xbf80,0x3f80,0xbf80,0x3e483f80,0xbf0ebf6d,0x3f7b0000,0xbf553ec4,0x3ec43f80,0xbf6dbf35,0xbec4bf80,0x3f6d3f35,0x3f0e3f80,0xbf7bbec4,0xbf550000,0xbe48bf6d,
0x3f353f80,0xbf350000,0x3f353f80,0xbf350000,0x3f553f80,0xbe483ec4,0x3f0e0000,0x3f7b3f6d,0x3f6d3f80,0x3ec43f35,0xbf6dbf80,0xbec4bf35,0x3f7b3f80,0x3f553f6d,0xbe480000,0xbf0ebec4,
0x3f803f80,0x3f803f80,0x3f803f80,0x3f803f80,0x3f0e3f35,0x3e483ec4,0xbf55bf35,0xbf7bbf6d,0xbec40000,0xbf6dbf35,0x3ec40000,0x3f6d3f35,0xbf7bbf35,0xbf0ebf6d,0x3e483f35,0xbf55bec4,
0xbf35bf80,0x3f350000,0xbf35bf80,0x3f350000,0x3e48bf35,0x3f553f6d,0x3f7b3f35,0xbf0e3ec4,0x3f6d0000,0xbec43f35,0xbf6d0000,0x3ec4bf35,0x3f553f35,0xbf7bbec4,0x3f0ebf35,0xbe483f6d,
0x3f80,0xbf80,0x3f80,0xbf80,0xbf553f35,0x3f7bbec4,0xbf0ebf35,0x3e483f6d,0xbf6d0000,0x3ec43f35,0x3f6d0000,0xbec4bf35,0xbe48bf35,0xbf553f6d,0xbf7b3f35,0x3f0e3ec4,
0x3f35bf80,0xbf350000,0x3f35bf80,0xbf350000,0x3f7bbf35,0x3f0ebf6d,0xbe483f35,0x3f55bec4,0x3ec40000,0x3f6dbf35,0xbec40000,0xbf6d3f35,0xbf0e3f35,0xbe483ec4,0x3f55bf35,0x3f7bbf6d,
0xbf803f80,0xbf803f80,0xbf803f80,0xbf803f80,0xbf0e3f35,0xbe483ec4,0x3f55bf35,0x3f7bbf6d,0x3ec40000,0x3f6dbf35,0xbec40000,0xbf6d3f35,0x3f7bbf35,0x3f0ebf6d,0xbe483f35,0x3f55bec4,
0x3f35bf80,0xbf350000,0x3f35bf80,0xbf350000,0xbe48bf35,0xbf553f6d,0xbf7b3f35,0x3f0e3ec4,0xbf6d0000,0x3ec43f35,0x3f6d0000,0xbec4bf35,0xbf553f35,0x3f7bbec4,0xbf0ebf35,0x3e483f6d,
0x3f80,0xbf80,0x3f80,0xbf80,0x3f553f35,0xbf7bbec4,0x3f0ebf35,0xbe483f6d,0x3f6d0000,0xbec43f35,0xbf6d0000,0x3ec4bf35,0x3e48bf35,0x3f553f6d,0x3f7b3f35,0xbf0e3ec4,
0xbf35bf80,0x3f350000,0xbf35bf80,0x3f350000,0xbf7bbf35,0xbf0ebf6d,0x3e483f35,0xbf55bec4,0xbec40000,0xbf6dbf35,0x3ec40000,0x3f6d3f35,0x3f0e3f35,0x3e483ec4,0xbf55bf35,0xbf7bbf6d,
0x0,0x0,0x0,0x0,0x3e480000,0x3f0e3ec4,0x3f7b3f80,0x3f553f6d,0x3ec40000,0x3f6d3f35,0xbec40000,0xbf6dbf35,0x3f0e0000,0x3f7b3f6d,0xbf55bf80,0x3e48bec4,
0x3f350000,0x3f353f80,0x3f350000,0x3f353f80,0x3f550000,0x3e483f6d,0x3f0e3f80,0xbf7bbec4,0x3f6d0000,0xbec43f35,0xbf6d0000,0x3ec4bf35,0x3f7b0000,0xbf553ec4,0xbe48bf80,0x3f0e3f6d,
0x3f800000,0xbf800000,0x3f800000,0xbf800000,0x3f7b0000,0xbf55bec4,0xbe483f80,0x3f0ebf6d,0x3f6d0000,0xbec4bf35,0xbf6d0000,0x3ec43f35,0x3f550000,0x3e48bf6d,0x3f0ebf80,0xbf7b3ec4,
0x3f350000,0x3f35bf80,0x3f350000,0x3f35bf80,0x3f0e0000,0x3f7bbf6d,0xbf553f80,0x3e483ec4,0x3ec40000,0x3f6dbf35,0xbec40000,0xbf6d3f35,0x3e480000,0x3f0ebec4,0x3f7bbf80,0x3f55bf6d,
0x0,0x0,0x0,0x0,0xbe480000,0xbf0e3ec4,0xbf7b3f80,0xbf553f6d,0xbec40000,0xbf6d3f35,0x3ec40000,0x3f6dbf35,0xbf0e0000,0xbf7b3f6d,0x3f55bf80,0xbe48bec4,
0xbf350000,0xbf353f80,0xbf350000,0xbf353f80,0xbf550000,0xbe483f6d,0xbf0e3f80,0x3f7bbec4,0xbf6d0000,0x3ec43f35,0x3f6d0000,0xbec4bf35,0xbf7b0000,0x3f553ec4,0x3e48bf80,0xbf0e3f6d,
0xbf800000,0x3f800000,0xbf800000,0x3f800000,0xbf7b0000,0x3f55bec4,0x3e483f80,0xbf0ebf6d,0xbf6d0000,0x3ec4bf35,0x3f6d0000,0xbec43f35,0xbf550000,0xbe48bf6d,0xbf0ebf80,0x3f7b3ec4,
0xbf350000,0xbf35bf80,0xbf350000,0xbf35bf80,0xbf0e0000,0xbf7bbf6d,0x3f553f80,0xbe483ec4,0xbec40000,0xbf6dbf35,0x3ec40000,0x3f6d3f35,0xbe480000,0xbf0ebec4,0xbf7bbf80,0xbf55bf6d,
0x0,0x0,0x0,0x0,0x3f553f35,0x3f7b3f6d,0x3f0e3f35,0x3e483ec4,0x3f6d3f80,0x3ec43f35,0xbf6dbf80,0xbec4bf35,0x3e483f35,0xbf55bec4,0x3f7b3f35,0x3f0e3f6d,
0xbf350000,0xbf35bf80,0xbf350000,0xbf35bf80,0xbf7bbf35,0x3f0ebec4,0x3e48bf35,0x3f553f6d,0xbec4bf80,0x3f6d3f35,0x3ec43f80,0xbf6dbf35,0x3f0ebf35,0xbe483f6d,0xbf55bf35,0x3f7b3ec4,
0x3f800000,0xbf800000,0x3f800000,0xbf800000,0x3f0e3f35,0xbe48bf6d,0xbf553f35,0x3f7bbec4,0xbec43f80,0x3f6dbf35,0x3ec4bf80,0xbf6d3f35,0xbf7b3f35,0x3f0e3ec4,0x3e483f35,0x3f55bf6d,
0xbf350000,0xbf353f80,0xbf350000,0xbf353f80,0x3e48bf35,0xbf553ec4,0x3f7bbf35,0x3f0ebf6d,0x3f6dbf80,0x3ec4bf35,0xbf6d3f80,0xbec43f35,0x3f55bf35,0x3f7bbf6d,0x3f0ebf35,0x3e48bec4,
0x0,0x0,0x0,0x0,0xbf553f35,0xbf7b3f6d,0xbf0e3f35,0xbe483ec4,0xbf6d3f80,0xbec43f35,0x3f6dbf80,0x3ec4bf35,0xbe483f35,0x3f55bec4,0xbf7b3f35,0xbf0e3f6d,
0x3f350000,0x3f35bf80,0x3f350000,0x3f35bf80,0x3f7bbf35,0xbf0ebec4,0xbe48bf35,0xbf553f6d,0x3ec4bf80,0xbf6d3f35,0xbec43f80,0x3f6dbf35,0xbf0ebf35,0x3e483f6d,0x3f55bf35,0xbf7b3ec4,
0xbf800000,0x3f800000,0xbf800000,0x3f800000,0xbf0e3f35,0x3e48bf6d,0x3f553f35,0xbf7bbec4,0x3ec43f80,0xbf6dbf35,0xbec4bf80,0x3f6d3f35,0x3f7b3f35,0xbf0e3ec4,0xbe483f35,0xbf55bf6d,
0x3f350000,0x3f353f80,0x3f350000,0x3f353f80,0xbe48bf35,0x3f553ec4,0xbf7bbf35,0xbf0ebf6d,0xbf6dbf80,0xbec4bf35,0x3f6d3f80,0x3ec43f35,0xbf55bf35,0xbf7bbf6d,0xbf0ebf35,0xbe48bec4,
0x3f803f80,0x3f803f80,0x3f803f80,0x3f803f80,0x3f6d3f80,0x3ec43f35,0xbec40000,0xbf6dbf35,0x3f353f80,0xbf350000,0xbf35bf80,0x3f350000,0x3ec43f80,0xbf6dbf35,0x3f6d0000,0xbec43f35,
0x3f80,0xbf80,0x3f80,0xbf80,0xbec43f80,0x3f6dbf35,0xbf6d0000,0x3ec43f35,0xbf353f80,0x3f350000,0x3f35bf80,0xbf350000,0xbf6d3f80,0xbec43f35,0x3ec40000,0x3f6dbf35,
0xbf803f80,0xbf803f80,0xbf803f80,0xbf803f80,0xbf6d3f80,0xbec43f35,0x3ec40000,0x3f6dbf35,0xbf353f80,0x3f350000,0x3f35bf80,0xbf350000,0xbec43f80,0x3f6dbf35,0xbf6d0000,0x3ec43f35,
0x3f80,0xbf80,0x3f80,0xbf80,0x3ec43f80,0xbf6dbf35,0x3f6d0000,0xbec43f35,0x3f353f80,0xbf350000,0xbf35bf80,0x3f350000,0x3f6d3f80,0x3ec43f35,0xbec40000,0xbf6dbf35,
0x3f803f80,0x3f803f80,0x3f803f80,0x3f803f80,0x3f6d3f80,0x3ec43f35,0xbec40000,0xbf6dbf35,0x3f353f80,0xbf350000,0xbf35bf80,0x3f350000,0x3ec43f80,0xbf6dbf35,0x3f6d0000,0xbec43f35,
0x3f80,0xbf80,0x3f80,0xbf80,0xbec43f80,0x3f6dbf35,0xbf6d0000,0x3ec43f35,0xbf353f80,0x3f350000,0x3f35bf80,0xbf350000,0xbf6d3f80,0xbec43f35,0x3ec40000,0x3f6dbf35,
0xbf803f80,0xbf803f80,0xbf803f80,0xbf803f80,0xbf6d3f80,0xbec43f35,0x3ec40000,0x3f6dbf35,0xbf353f80,0x3f350000,0x3f35bf80,0xbf350000,0xbec43f80,0x3f6dbf35,0xbf6d0000,0x3ec43f35,
0x3f80,0xbf80,0x3f80,0xbf80,0x3ec43f80,0xbf6dbf35,0x3f6d0000,0xbec43f35,0x3f353f80,0xbf350000,0xbf35bf80,0x3f350000,0x3f6d3f80,0x3ec43f35,0xbec40000,0xbf6dbf35,
0x3f803f80,0x3f803f80,0x3f803f80,0x3f803f80,0x3f553f7b,0x3e483f0e,0xbf0ebe48,0xbf7bbf55,0x3ec43f6d,0xbf6dbec4,0xbec4bf6d,0x3f6d3ec4,0xbe483f55,0xbf0ebf7b,0x3f7b3f0e,0xbf553e48,
0xbf353f35,0x3f35bf35,0xbf353f35,0x3f35bf35,0xbf7b3f0e,0x3f553e48,0xbe48bf55,0xbf0e3f7b,0xbf6d3ec4,0xbec43f6d,0x3f6dbec4,0x3ec4bf6d,0xbf0e3e48,0xbf7b3f55,0xbf553f7b,0xbe483f0e,
0x0,0x0,0x0,0x0,0x3f0ebe48,0x3f7bbf55,0x3f55bf7b,0x3e48bf0e,0x3f6dbec4,0x3ec4bf6d,0xbf6d3ec4,0xbec43f6d,0x3f7bbf0e,0xbf55be48,0x3e483f55,0x3f0ebf7b,
0x3f35bf35,0xbf353f35,0x3f35bf35,0xbf353f35,0x3e48bf55,0x3f0e3f7b,0xbf7bbf0e,0x3f55be48,0xbec4bf6d,0x3f6d3ec4,0x3ec43f6d,0xbf6dbec4,0xbf55bf7b,0xbe48bf0e,0x3f0e3e48,0x3f7b3f55,
0xbf80bf80,0xbf80bf80,0xbf80bf80,0xbf80bf80,0xbf55bf7b,0xbe48bf0e,0x3f0e3e48,0x3f7b3f55,0xbec4bf6d,0x3f6d3ec4,0x3ec43f6d,0xbf6dbec4,0x3e48bf55,0x3f0e3f7b,0xbf7bbf0e,0x3f55be48,
0x3f35bf35,0xbf353f35,0x3f35bf35,0xbf353f35,0x3f7bbf0e,0xbf55be48,0x3e483f55,0x3f0ebf7b,0x3f6dbec4,0x3ec4bf6d,0xbf6d3ec4,0xbec43f6d,0x3f0ebe48,0x3f7bbf55,0x3f55bf7b,0x3e48bf0e,
0x0,0x0,0x0,0x0,0xbf0e3e48,0xbf7b3f55,0xbf553f7b,0xbe483f0e,0xbf6d3ec4,0xbec43f6d,0x3f6dbec4,0x3ec4bf6d,0xbf7b3f0e,0x3f553e48,0xbe48bf55,0xbf0e3f7b,
0xbf353f35,0x3f35bf35,0xbf353f35,0x3f35bf35,0xbe483f55,0xbf0ebf7b,0x3f7b3f0e,0xbf553e48,0x3ec43f6d,0xbf6dbec4,0xbec4bf6d,0x3f6d3ec4,0x3f553f7b,0x3e483f0e,0xbf0ebe48,0xbf7bbf55,
0x80008000,0x80008000,0x80008000,0x80008000,0xbec48000,0xbf6dbf35,0xbf6dbf80,0xbec4bf35,0xbf358000,0xbf35bf80,0x3f358000,0x3f353f80,0xbf6d8000,0x3ec4bf35,0x3ec43f80,0xbf6dbf35,
0xbf808000,0x3f808000,0xbf808000,0x3f808000,0xbf6d8000,0x3ec43f35,0x3ec4bf80,0xbf6d3f35,0xbf358000,0xbf353f80,0x3f358000,0x3f35bf80,0xbec48000,0xbf6d3f35,0xbf6d3f80,0xbec43f35,
0x80008000,0x80008000,0x80008000,0x80008000,0x3ec48000,0x3f6dbf35,0x3f6dbf80,0x3ec4bf35,0x3f358000,0x3f35bf80,0xbf358000,0xbf353f80,0x3f6d8000,0xbec4bf35,0xbec43f80,0x3f6dbf35,
0x3f808000,0xbf808000,0x3f808000,0xbf808000,0x3f6d8000,0xbec43f35,0xbec4bf80,0x3f6d3f35,0x3f358000,0x3f353f80,0xbf358000,0xbf35bf80,0x3ec48000,0x3f6d3f35,0x3f6d3f80,0x3ec43f35,
0x80008000,0x80008000,0x80008000,0x80008000,0xbec48000,0xbf6dbf35,0xbf6dbf80,0xbec4bf35,0xbf358000,0xbf35bf80,0x3f358000,0x3f353f80,0xbf6d8000,0x3ec4bf35,0x3ec43f80,0xbf6dbf35,
0xbf808000,0x3f808000,0xbf808000,0x3f808000,0xbf6d8000,0x3ec43f35,0x3ec4bf80,0xbf6d3f35,0xbf358000,0xbf353f80,0x3f358000,0x3f35bf80,0xbec48000,0xbf6d3f35,0xbf6d3f80,0xbec43f35,
0x80008000,0x80008000,0x80008000,0x80008000,0x3ec48000,0x3f6dbf35,0x3f6dbf80,0x3ec4bf35,0x3f358000,0x3f35bf80,0xbf358000,0xbf353f80,0x3f6d8000,0xbec4bf35,0xbec43f80,0x3f6dbf35,
0x3f808000,0xbf808000,0x3f808000,0xbf808000,0x3f6d8000,0xbec43f35,0xbec4bf80,0x3f6d3f35,0x3f358000,0x3f353f80,0xbf358000,0xbf35bf80,0x3ec48000,0x3f6d3f35,0x3f6d3f80,0x3ec43f35,
0x80008000,0x80008000,0x80008000,0x80008000,0xbf0ebe48,0xbf7bbf55,0xbf55bf7b,0xbe48bf0e,0xbf6dbec4,0xbec4bf6d,0x3f6d3ec4,0x3ec43f6d,0xbf7bbf0e,0x3f55be48,0xbe483f55,0xbf0ebf7b,
0xbf35bf35,0x3f353f35,0xbf35bf35,0x3f353f35,0xbe48bf55,0xbf0e3f7b,0x3f7bbf0e,0xbf55be48,0x3ec4bf6d,0xbf6d3ec4,0xbec43f6d,0x3f6dbec4,0x3f55bf7b,0x3e48bf0e,0xbf0e3e48,0xbf7b3f55,
0x3f80bf80,0x3f80bf80,0x3f80bf80,0x3f80bf80,0x3f55bf7b,0x3e48bf0e,0xbf0e3e48,0xbf7b3f55,0x3ec4bf6d,0xbf6d3ec4,0xbec43f6d,0x3f6dbec4,0xbe48bf55,0xbf0e3f7b,0x3f7bbf0e,0xbf55be48,
0xbf35bf35,0x3f353f35,0xbf35bf35,0x3f353f35,0xbf7bbf0e,0x3f55be48,0xbe483f55,0xbf0ebf7b,0xbf6dbec4,0xbec4bf6d,0x3f6d3ec4,0x3ec43f6d,0xbf0ebe48,0xbf7bbf55,0xbf55bf7b,0xbe48bf0e,
0x80008000,0x80008000,0x80008000,0x80008000,0x3f0e3e48,0x3f7b3f55,0x3f553f7b,0x3e483f0e,0x3f6d3ec4,0x3ec43f6d,0xbf6dbec4,0xbec4bf6d,0x3f7b3f0e,0xbf553e48,0x3e48bf55,0x3f0e3f7b,
0x3f353f35,0xbf35bf35,0x3f353f35,0xbf35bf35,0x3e483f55,0x3f0ebf7b,0xbf7b3f0e,0x3f553e48,0xbec43f6d,0x3f6dbec4,0x3ec4bf6d,0xbf6d3ec4,0xbf553f7b,0xbe483f0e,0x3f0ebe48,0x3f7bbf55,
0xbf803f80,0xbf803f80,0xbf803f80,0xbf803f80,0xbf553f7b,0xbe483f0e,0x3f0ebe48,0x3f7bbf55,0xbec43f6d,0x3f6dbec4,0x3ec4bf6d,0xbf6d3ec4,0x3e483f55,0x3f0ebf7b,0xbf7b3f0e,0x3f553e48,
0x3f353f35,0xbf35bf35,0x3f353f35,0xbf35bf35,0x3f7b3f0e,0xbf553e48,0x3e48bf55,0x3f0e3f7b,0x3f6d3ec4,0x3ec43f6d,0xbf6dbec4,0xbec4bf6d,0x3f0e3e48,0x3f7b3f55,0x3f553f7b,0x3e483f0e,
0x3f800000,0x3f800000,0x3f800000,0x3f800000,0x3f800000,0x3f7ffb11,0x3f7fec43,0x3f7fd397,0x3f800000,0x3f7fec43,0x3f7fb10f,0x3f7f4e6d,0x3f800000,0x3f7fd397,0x3f7f4e6d,0x3f7e70b0,
0x3f800000,0x3f7fb10f,0x3f7ec46d,0x3f7d3aac,0x3f800000,0x3f7f84ab,0x3f7e1324,0x3f7baccd,0x3f800000,0x3f7f4e6d,0x3f7d3aac,0x3f79c79d,0x3f800000,0x3f7f0e58,0x3f7c3b28,0x3f778bc5,
0x3f800000,0x3f7ec46d,0x3f7b14be,0x3f74fa0b,0x3f800000,0x3f7e70b0,0x3f79c79d,0x3f721352,0x3f800000,0x3f7e1324,0x3f7853f8,0x3f6ed89e,0x3f800000,0x3f7dabcc,0x3f76ba07,0x3f6b4b0c,
0x3f800000,0x3f7d3aac,0x3f74fa0b,0x3f676bd8,0x3f800000,0x3f7cbfc9,0x3f731447,0x3f633c5a,0x3f800000,0x3f7c3b28,0x3f710908,0x3f5ebe05,0x3f800000,0x3f7baccd,0x3f6ed89e,0x3f59f26a,
0x3f800000,0x3f7b14be,0x3f6c835e,0x3f54db31,0x3f800000,0x3f7a7302,0x3f6a09a7,0x3f4f7a1f,0x3f800000,0x3f79c79d,0x3f676bd8,0x3f49d112,0x3f800000,0x3f791298,0x3f64aa59,0x3f43e200,
0x3f800000,0x3f7853f8,0x3f61c598,0x3f3daef9,0x3f800000,0x3f778bc5,0x3f5ebe05,0x3f373a23,0x3f800000,0x3f76ba07,0x3f5b941a,0x3f3085bb,0x3f800000,0x3f75dec6,0x3f584853,0x3f299415,
0x3f800000,0x3f74fa0b,0x3f54db31,0x3f226799,0x3f800000,0x3f740bdd,0x3f514d3d,0x3f1b02c6,0x3f800000,0x3f731447,0x3f4d9f02,0x3f13682a,0x3f800000,0x3f721352,0x3f49d112,0x3f0b9a6b,
0x3f800000,0x3f710908,0x3f45e403,0x3f039c3d,0x3f800000,0x3f6ff573,0x3f41d870,0x3ef6e0cb,0x3f800000,0x3f6ed89e,0x3f3daef9,0x3ee63375,0x3f800000,0x3f6db293,0x3f396842,0x3ed53641,
0x3f800000,0x3f800000,0x3f800000,0x3f800000,0x3f7ffec4,0x3f7ff4e6,0x3f7fe129,0x3f7fc38f,0x3f7ffb11,0x3f7fd397,0x3f7f84ab,0x3f7f0e58,0x3f7ff4e6,0x3f7f9c18,0x3f7eea9d,0x3f7de0b1,
0x3f7fec43,0x3f7f4e6d,0x3f7e1324,0x3f7c3b28,0x3f7fe129,0x3f7eea9d,0x3f7cfe73,0x3f7a1e84,0x3f7fd397,0x3f7e70b0,0x3f7baccd,0x3f778bc5,0x3f7fc38f,0x3f7de0b1,0x3f7a1e84,0x3f748422,
0x3f7fb10f,0x3f7d3aac,0x3f7853f8,0x3f710908,0x3f7f9c18,0x3f7c7eb0,0x3f764d97,0x3f6d1c1d,0x3f7f84ab,0x3f7baccd,0x3f740bdd,0x3f68bf3c,0x3f7f6ac7,0x3f7ac516,0x3f718f57,0x3f63f473,
0x3f7f4e6d,0x3f79c79d,0x3f6ed89e,0x3f5ebe05,0x3f7f2f9d,0x3f78b47b,0x3f6be858,0x3f591e6a,0x3f7f0e58,0x3f778bc5,0x3f68bf3c,0x3f531849,0x3f7eea9d,0x3f764d97,0x3f655e0b,0x3f4cae79,
0x3f7ec46d,0x3f74fa0b,0x3f61c598,0x3f45e403,0x3f7e9bc9,0x3f73913f,0x3f5df6be,0x3f3ebc1b,0x3f7e70b0,0x3f721352,0x3f59f26a,0x3f373a23,0x3f7e4323,0x3f708066,0x3f55b993,0x3f2f61a5,
0x3f7e1324,0x3f6ed89e,0x3f514d3d,0x3f273656,0x3f7de0b1,0x3f6d1c1d,0x3f4cae79,0x3f1ebc12,0x3f7dabcc,0x3f6b4b0c,0x3f47de65,0x3f15f6d9,0x3f7d7474,0x3f696591,0x3f42de29,0x3f0cead0,
0x3f7d3aac,0x3f676bd8,0x3f3daef9,0x3f039c3d,0x3f7cfe73,0x3f655e0b,0x3f385216,0x3ef41f07,0x3f7cbfc9,0x3f633c5a,0x3f32c8c9,0x3ee0924f,0x3f7c7eb0,0x3f6106f2,0x3f2d1469,0x3ecc9b8b,
0x3f7c3b28,0x3f5ebe05,0x3f273656,0x3eb8442a,0x3f7bf531,0x3f5c61c7,0x3f212ff9,0x3ea395c5,0x3f7baccd,0x3f59f26a,0x3f1b02c6,0x3e8e9a22,0x3f7b61fc,0x3f577026,0x3f14b039,0x3e72b651,
0x3f800000,0x3f800000,0x3f800000,0x3f800000,0x3f7fb10f,0x3f7f84ab,0x3f7f4e6d,0x3f7f0e58,0x3f7ec46d,0x3f7e1324,0x3f7d3aac,0x3f7c3b28,0x3f7d3aac,0x3f7baccd,0x3f79c79d,0x3f778bc5,
0x3f7b14be,0x3f7853f8,0x3f74fa0b,0x3f710908,0x3f7853f8,0x3f740bdd,0x3f6ed89e,0x3f68bf3c,0x3f74fa0b,0x3f6ed89e,0x3f676bd8,0x3f5ebe05,0x3f710908,0x3f68bf3c,0x3f5ebe05,0x3f531849,
0x3f6c835e,0x3f61c598,0x3f54db31,0x3f45e403,0x3f676bd8,0x3f59f26a,0x3f49d112,0x3f373a23,0x3f61c598,0x3f514d3d,0x3f3daef9,0x3f273656,0x3f5b941a,0x3f47de65,0x3f3085bb,0x3f15f6d9,
0x3f54db31,0x3f3daef9,0x3f226799,0x3f039c3d,0x3f4d9f02,0x3f32c8c9,0x3f13682a,0x3ee0924f,0x3f45e403,0x3f273656,0x3f039c3d,0x3eb8442a,0x3f3daef9,0x3f1b02c6,0x3ee63375,0x3e8e9a22,
0x3f3504f3,0x3f0e39da,0x3ec3ef15,0x3e47c5c2,0x3f2beb4a,0x3f00e7e4,0x3ea09ae5,0x3de1bc2e,0x3f226799,0x3ee63375,0x3e78cfcc,0x3cc90ab0,0x3f187fc0,0x3ec9b953,0x3e2f10a2,0xbd7b2b74,
0x3f0e39da,0x3eac7cd4,0x3dc8bd36,0xbe164083,0x3f039c3d,0x3e8e9a22,0x3cc90ab0,0xbe6c9a7f,0x3ef15aea,0x3e605c13,0xbd48fb30,0xbea09ae5,0x3edae880,0x3e22abb6,0xbdfab273,0xbec9b953,
0x3ec3ef15,0x3dc8bd36,0xbe47c5c2,0xbef15aea,0x3eac7cd4,0x3d16c32c,0xbe888e93,0xbf0b9a6b,0x3e94a031,0xbcc90ab0,0xbeac7cd4,0xbf1d7fd1,0x3e78cfcc,0xbdafb680,0xbecf7bca,0xbf2e3bde,
0x3e47c5c2,0xbe164083,0xbef15aea,0xbf3daef9,0x3e164083,0xbe541501,0xbf08f59b,0xbf4bbbf8,0x3dc8bd36,0xbe888e93,0xbf187fc0,0xbf584853,0x3d48fb30,0xbea68f12,0xbf273656,0xbf633c5a,
0x3f800000,0x3f800000,0x3f800000,0x3f800000,0x3f7f9c18,0x3f7f6ac7,0x3f7f2f9d,0x3f7eea9d,0x3f7e70b0,0x3f7dabcc,0x3f7cbfc9,0x3f7baccd,0x3f7c7eb0,0x3f7ac516,0x3f78b47b,0x3f764d97,
0x3f79c79d,0x3f76ba07,0x3f731447,0x3f6ed89e,0x3f764d97,0x3f718f57,0x3f6be858,0x3f655e0b,0x3f721352,0x3f6b4b0c,0x3f633c5a,0x3f59f26a,0x3f6d1c1d,0x3f63f473,0x3f591e6a,0x3f4cae79,
0x3f676bd8,0x3f5b941a,0x3f4d9f02,0x3f3daef9,0x3f6106f2,0x3f5233c6,0x3f40d0da,0x3f2d1469,0x3f59f26a,0x3f47de65,0x3f32c8c9,0x3f1b02c6,0x3f5233c6,0x3f3ca003,0x3f239da9,0x3f07a136,
0x3f49d112,0x3f3085bb,0x3f13682a,0x3ee63375,0x3f40d0da,0x3f239da9,0x3f0242b1,0x3ebb31a0,0x3f373a23,0x3f15f6d9,0x3ee0924f,0x3e8e9a22,0x3f2d1469,0x3f07a136,0x3ebb31a0,0x3e419b37,
0x3f226799,0x3ef15aea,0x3e94a031,0x3dc8bd36,0x3f173c07,0x3ed25a09,0x3e5a3997,0x3bc90f88,0x3f0b9a6b,0x3eb263ef,0x3e09cf86,0xbdafb680,0x3eff17b2,0x3e919ddd,0x3d621469,0xbe354098,
0x3ee63375,0x3e605c13,0xbcc90ab0,0xbe888e93,0x3ecc9b8b,0x3e1c76de,0xbdd53db9,0xbeb554ec,0x3eb263ef,0x3dafb680,0xbe3b6ecf,0xbee0924f,0x3e97a117,0x3c96c9b6,0xbe8586ce,0xbf04f484,
0x3e78cfcc,0xbd48fb30,0xbeac7cd4,0xbf187fc0,0x3e419b37,0xbdee3876,0xbed25a09,0xbf2ac082,0x3e09cf86,0xbe3b6ecf,0xbef6e0cb,0xbf3b8f3b,0x3da3308c,0xbe7ee6e1,0xbf0cead0,0xbf4ac77f,
0x3cc90ab0,0xbea09ae5,0xbf1d7fd1,0xbf584853,0xbcfb49ba,0xbec1071e,0xbf2d1469,0xbf63f473,0xbdafb680,0xbee0924f,0xbf3b8f3b,0xbf6db293,0xbe1008b7,0xbeff17b2,0xbf48d8b3,0xbf756d97,
0x3f800000,0x3f800000,0x3f800000,0x3f800000,0x3f7ec46d,0x3f7e70b0,0x3f7e1324,0x3f7dabcc,0x3f7b14be,0x3f79c79d,0x3f7853f8,0x3f76ba07,0x3f74fa0b,0x3f721352,0x3f6ed89e,0x3f6b4b0c,
0x3f6c835e,0x3f676bd8,0x3f61c598,0x3f5b941a,0x3f61c598,0x3f59f26a,0x3f514d3d,0x3f47de65,0x3f54db31,0x3f49d112,0x3f3daef9,0x3f3085bb,0x3f45e403,0x3f373a23,0x3f273656,0x3f15f6d9,
0x3f3504f3,0x3f226799,0x3f0e39da,0x3ef15aea,0x3f226799,0x3f0b9a6b,0x3ee63375,0x3eb263ef,0x3f0e39da,0x3ee63375,0x3eac7cd4,0x3e605c13,0x3ef15aea,0x3eb263ef,0x3e605c13,0x3dafb680,
0x3ec3ef15,0x3e78cfcc,0x3dc8bd36,0xbd48fb30,0x3e94a031,0x3e09cf86,0xbcc90ab0,0xbe3b6ecf,0x3e47c5c2,0x3cc90ab0,0xbe164083,0xbea09ae5,0x3dc8bd36,0xbdafb680,0xbe888e93,0xbee0924f,
0x0,0xbe47c5c2,0xbec3ef15,0xbf0e39da,0xbdc8bd36,0xbe9aa086,0xbefc5d27,0xbf299415,0xbe47c5c2,0xbecf7bca,0xbf187fc0,0xbf41d870,0xbe94a031,0xbf00e7e4,0xbf3085bb,0xbf5695e5,
0xbec3ef15,0xbf187fc0,0xbf45e403,0xbf676bd8,0xbef15aea,0xbf2e3bde,0xbf584853,0xbf740bdd,0xbf0e39da,0xbf41d870,0xbf676bd8,0xbf7c3b28,0xbf226799,0xbf531849,0xbf731447,0xbf7fd397,
0xbf3504f3,0xbf61c598,0xbf7b14be,0xbf7ec46d,0xbf45e403,0xbf6db293,0xbf7f4e6d,0xbf791298,0xbf54db31,0xbf76ba07,0xbf7fb10f,0xbf6ed89e,0xbf61c598,0xbf7cbfc9,0xbf7c3b28,0xbf604621,
0xbf6c835e,0xbf7fb10f,0xbf74fa0b,0xbf4d9f02,0xbf74fa0b,0xbf7f84ab,0xbf6a09a7,0xbf373a23,0xbf7b14be,0xbf7c3b28,0xbf5b941a,0xbf1d7fd1,0xbf7ec46d,0xbf75dec6,0xbf49d112,0xbf00e7e4,
0x3f800000,0x3f800000,0x3f800000,0x3f800000,0x3f7e9bc9,0x3f7e4323,0x3f7de0b1,0x3f7d7474,0x3f7a7302,0x3f791298,0x3f778bc5,0x3f75dec6,0x3f73913f,0x3f708066,0x3f6d1c1d,0x3f696591,
0x3f6a09a7,0x3f64aa59,0x3f5ebe05,0x3f584853,0x3f5df6be,0x3f55b993,0x3f4cae79,0x3f42de29,0x3f4f7a1f,0x3f43e200,0x3f373a23,0x3f299415,0x3f3ebc1b,0x3f2f61a5,0x3f1ebc12,0x3f0cead0,
0x3f2beb4a,0x3f187fc0,0x3f039c3d,0x3edae880,0x3f173c07,0x3eff17b2,0x3ecc9b8b,0x3e97a117,0x3f00e7e4,0x3ec9b953,0x3e8e9a22,0x3e22abb6,0x3ed25a09,0x3e919ddd,0x3e1c76de,0x3c96c9b6,
0x3ea09ae5,0x3e2f10a2,0x3cc90ab0,0xbdfab273,0x3e5a3997,0x3d621469,0xbdd53db9,0xbe8586ce,0x3de1bc2e,0xbd7b2b74,0xbe6c9a7f,0xbec9b953,0x3bc90f88,0xbe354098,0xbeb554ec,0xbf04f484,
0xbdc8bd36,0xbe94a031,0xbef15aea,0xbf226799,0xbe4dee60,0xbecc9b8b,0xbf14b039,0xbf3ca003,0xbe9aa086,0xbf00e7e4,0xbf2e3bde,0xbf531849,0xbecc9b8b,0xbf19c200,0xbf44e3f5,0xbf655e0b,
0xbefc5d27,0xbf3085bb,0xbf584853,0xbf731447,0xbf14b039,0xbf44e3f5,0xbf6816a8,0xbf7bf531,0xbf299415,0xbf5695e5,0xbf740bdd,0xbf7fd397,0xbf3ca003,0xbf655e0b,0xbf7bf531,0xbf7e9bc9,
0xbf4d9f02,0xbf710908,0xbf7fb10f,0xbf7853f8,0xbf5c61c7,0xbf796e4e,0xbf7f2f9d,0xbf6d1c1d,0xbf68bf3c,0xbf7e70b0,0xbf7a7302,0xbf5d2d53,0xbf7294f8,0xbf7ffec4,0xbf718f57,0xbf48d8b3,
0xbf79c79d,0xbf7e1324,0xbf64aa59,0xbf3085bb,0xbf7e4323,0xbf78b47b,0xbf53fac3,0xbf14b039,0xbf7ffb11,0xbf6ff573,0xbf3fc767,0xbeebcbbb,0xbf7eea9d,0xbf63f473,0xbf286605,0xbea986c4,
0x3f800000,0x3f800000,0x3f800000,0x3f800000,0x3f7d3aac,0x3f7cbfc9,0x3f7c3b28,0x3f7baccd,0x3f74fa0b,0x3f731447,0x3f710908,0x3f6ed89e,0x3f676bd8,0x3f633c5a,0x3f5ebe05,0x3f59f26a,
0x3f54db31,0x3f4d9f02,0x3f45e403,0x3f3daef9,0x3f3daef9,0x3f32c8c9,0x3f273656,0x3f1b02c6,0x3f226799,0x3f13682a,0x3f039c3d,0x3ee63375,0x3f039c3d,0x3ee0924f,0x3eb8442a,0x3e8e9a22,
0x3ec3ef15,0x3e94a031,0x3e47c5c2,0x3dc8bd36,0x3e78cfcc,0x3e09cf86,0x3cc90ab0,0xbdafb680,0x3dc8bd36,0xbcc90ab0,0xbe164083,0xbe888e93,0xbd48fb30,0xbe3b6ecf,0xbea09ae5,0xbee0924f,
0xbe47c5c2,0xbeac7cd4,0xbef15aea,0xbf187fc0,0xbeac7cd4,0xbef6e0cb,0xbf1d7fd1,0xbf3b8f3b,0xbef15aea,0xbf1d7fd1,0xbf3daef9,0xbf584853,0xbf187fc0,0xbf3b8f3b,0xbf584853,0xbf6db293,
0xbf3504f3,0xbf54db31,0xbf6c835e,0xbf7b14be,0xbf4d9f02,0xbf68bf3c,0xbf79c79d,0xbf7ffb11,0xbf61c598,0xbf76ba07,0xbf7fb10f,0xbf7c3b28,0xbf710908,0xbf7e70b0,0xbf7e1324,0xbf6ff573,
0xbf7b14be,0xbf7fb10f,0xbf74fa0b,0xbf5b941a,0xbf7fb10f,0xbf7a7302,0xbf64aa59,0xbf3fc767,0xbf7ec46d,0xbf6ed89e,0xbf4d9f02,0xbf1d7fd1,0xbf7853f8,0xbf5d2d53,0xbf3085bb,0xbeebcbbb,
0xbf6c835e,0xbf45e403,0xbf0e39da,0xbe94a031,0xbf5b941a,0xbf299415,0xbecf7bca,0xbde1bc2e,0xbf45e403,0xbf08f59b,0xbe78cfcc,0x3d96a905,0xbf2beb4a,0xbec9b953,0xbd96a905,0x3e827dc0,
0xbf0e39da,0xbe78cfcc,0x3dc8bd36,0x3edae880,0xbedae880,0xbdafb680,0x3e888e93,0x3f15f6d9,0xbe94a031,0x3d96a905,0x3edae880,0x3f396842,0xbe164083,0x3e6c9a7f,0x3f13682a,0x3f5695e5,
0x3f800000,0x3f800000,0x3f800000,0x3f800000,0x3f7cfe73,0x3f7c7eb0,0x3f7bf531,0x3f7b61fc,0x3f740bdd,0x3f721352,0x3f6ff573,0x3f6db293,0x3f655e0b,0x3f6106f2,0x3f5c61c7,0x3f577026,
0x3f514d3d,0x3f49d112,0x3f41d870,0x3f396842,0x3f385216,0x3f2d1469,0x3f212ff9,0x3f14b039,0x3f1b02c6,0x3f0b9a6b,0x3ef6e0cb,0x3ed53641,0x3ef41f07,0x3ecc9b8b,0x3ea395c5,0x3e72b651,
0x3eac7cd4,0x3e78cfcc,0x3e164083,0x3d48fb30,0x3e419b37,0x3da3308c,0xbcfb49ba,0xbe1008b7,0x3d16c32c,0xbdafb680,0xbe541501,0xbea68f12,0xbdee3876,0xbe7ee6e1,0xbec1071e,0xbeff17b2,
0xbe888e93,0xbecf7bca,0xbf08f59b,0xbf273656,0xbed25a09,0xbf0cead0,0xbf2d1469,0xbf48d8b3,0xbf0b9a6b,0xbf2e3bde,0xbf4bbbf8,0xbf633c5a,0xbf2ac082,0xbf4ac77f,0xbf63f473,0xbf756d97,
0xbf45e403,0xbf61c598,0xbf74fa0b,0xbf7ec46d,0xbf5c61c7,0xbf7294f8,0xbf7e4323,0xbf7eea9d,0xbf6db293,0xbf7cbfc9,0xbf7f84ab,0xbf75dec6,0xbf796e4e,0xbf7ffec4,0xbf78b47b,0xbf63f473,
0xbf7f4e6d,0xbf7c3b28,0xbf6a09a7,0xbf49d112,0xbf7f2f9d,0xbf718f57,0xbf53fac3,0xbf286605,0xbf791298,0xbf604621,0xbf373a23,0xbf00e7e4,0xbf6d1c1d,0xbf48d8b3,0xbf14b039,0xbea986c4,
0xbf5b941a,0xbf2beb4a,0xbedae880,0xbe164083,0xbf44e3f5,0xbf0a48ad,0xbe8586ce,0x3d2fe007,0xbf299415,0xbec9b953,0xbdafb680,0x3e6c9a7f,0xbf0a48ad,0xbe72b651,0x3dbc3ac3,0x3ed25a09,
0xbecf7bca,0xbd96a905,0x3e888e93,0x3f13682a,0xbe8586ce,0x3dbc3ac3,0x3eddbe79,0x3f385216,0xbde1bc2e,0x3e827dc0,0x3f15f6d9,0x3f5695e5,0x3d2fe007,0x3ed25a09,0x3f385216,0x3f6d1c1d,
0x0,0x0,0x0,0x0,0x0,0x3c490e90,0x3cc90ab0,0x3d16c32c,0x0,0x3cc90ab0,0x3d48fb30,0x3d96a905,0x0,0x3d16c32c,0x3d96a905,0x3de1bc2e,
0x0,0x3d48fb30,0x3dc8bd36,0x3e164083,0x0,0x3d7b2b74,0x3dfab273,0x3e3b6ecf,0x0,0x3d96a905,0x3e164083,0x3e605c13,0x0,0x3dafb680,0x3e2f10a2,0x3e827dc0,
0x0,0x3dc8bd36,0x3e47c5c2,0x3e94a031,0x0,0x3de1bc2e,0x3e605c13,0x3ea68f12,0x0,0x3dfab273,0x3e78cfcc,0x3eb8442a,0x0,0x3e09cf86,0x3e888e93,0x3ec9b953,
0x0,0x3e164083,0x3e94a031,0x3edae880,0x0,0x3e22abb6,0x3ea09ae5,0x3eebcbbb,0x0,0x3e2f10a2,0x3eac7cd4,0x3efc5d27,0x0,0x3e3b6ecf,0x3eb8442a,0x3f064b82,
0x0,0x3e47c5c2,0x3ec3ef15,0x3f0e39da,0x0,0x3e541501,0x3ecf7bca,0x3f15f6d9,0x0,0x3e605c13,0x3edae880,0x3f1d7fd1,0x0,0x3e6c9a7f,0x3ee63375,0x3f24d225,
0x0,0x3e78cfcc,0x3ef15aea,0x3f2beb4a,0x0,0x3e827dc0,0x3efc5d27,0x3f32c8c9,0x0,0x3e888e93,0x3f039c3d,0x3f396842,0x0,0x3e8e9a22,0x3f08f59b,0x3f3fc767,
0x0,0x3e94a031,0x3f0e39da,0x3f45e403,0x0,0x3e9aa086,0x3f13682a,0x3f4bbbf8,0x0,0x3ea09ae5,0x3f187fc0,0x3f514d3d,0x0,0x3ea68f12,0x3f1d7fd1,0x3f5695e5,
0x0,0x3eac7cd4,0x3f226799,0x3f5b941a,0x0,0x3eb263ef,0x3f273656,0x3f604621,0x0,0x3eb8442a,0x3f2beb4a,0x3f64aa59,0x0,0x3ebe1d4a,0x3f3085bb,0x3f68bf3c,
0x0,0x0,0x0,0x0,0x3bc90f88,0x3c96c9b6,0x3cfb49ba,0x3d2fe007,0x3c490e90,0x3d16c32c,0x3d7b2b74,0x3dafb680,0x3c96c9b6,0x3d621469,0x3dbc3ac3,0x3e039502,
0x3cc90ab0,0x3d96a905,0x3dfab273,0x3e2f10a2,0x3cfb49ba,0x3dbc3ac3,0x3e1c76de,0x3e5a3997,0x3d16c32c,0x3de1bc2e,0x3e3b6ecf,0x3e827dc0,0x3d2fe007,0x3e039502,0x3e5a3997,0x3e97a117,
0x3d48fb30,0x3e164083,0x3e78cfcc,0x3eac7cd4,0x3d621469,0x3e28defc,0x3e8b9507,0x3ec1071e,0x3d7b2b74,0x3e3b6ecf,0x3e9aa086,0x3ed53641,0x3d8a200a,0x3e4dee60,0x3ea986c4,0x3ee900b7,
0x3d96a905,0x3e605c13,0x3eb8442a,0x3efc5d27,0x3da3308c,0x3e72b651,0x3ec6d529,0x3f07a136,0x3dafb680,0x3e827dc0,0x3ed53641,0x3f10d3cd,0x3dbc3ac3,0x3e8b9507,0x3ee363fa,0x3f19c200,
0x3dc8bd36,0x3e94a031,0x3ef15aea,0x3f226799,0x3dd53db9,0x3e9d9e78,0x3eff17b2,0x3f2ac082,0x3de1bc2e,0x3ea68f12,0x3f064b82,0x3f32c8c9,0x3dee3876,0x3eaf713a,0x3f0cead0,0x3f3a7ca4,
0x3dfab273,0x3eb8442a,0x3f13682a,0x3f41d870,0x3e039502,0x3ec1071e,0x3f19c200,0x3f48d8b3,0x3e09cf86,0x3ec9b953,0x3f1ff6cb,0x3f4f7a1f,0x3e1008b7,0x3ed25a09,0x3f26050a,0x3f55b993,
0x3e164083,0x3edae880,0x3f2beb4a,0x3f5b941a,0x3e1c76de,0x3ee363fa,0x3f31a81d,0x3f6106f2,0x3e22abb6,0x3eebcbbb,0x3f373a23,0x3f660f88,0x3e28defc,0x3ef41f07,0x3f3ca003,0x3f6aab7b,
0x3e2f10a2,0x3efc5d27,0x3f41d870,0x3f6ed89e,0x3e354098,0x3f0242b1,0x3f46e22a,0x3f7294f8,0x3e3b6ecf,0x3f064b82,0x3f4bbbf8,0x3f75dec6,0x3e419b37,0x3f0a48ad,0x3f5064af,0x3f78b47b,
0x0,0x0,0x0,0x0,0x3d48fb30,0x3d7b2b74,0x3d96a905,0x3dafb680,0x3dc8bd36,0x3dfab273,0x3e164083,0x3e2f10a2,0x3e164083,0x3e3b6ecf,0x3e605c13,0x3e827dc0,
0x3e47c5c2,0x3e78cfcc,0x3e94a031,0x3eac7cd4,0x3e78cfcc,0x3e9aa086,0x3eb8442a,0x3ed53641,0x3e94a031,0x3eb8442a,0x3edae880,0x3efc5d27,0x3eac7cd4,0x3ed53641,0x3efc5d27,0x3f10d3cd,
0x3ec3ef15,0x3ef15aea,0x3f0e39da,0x3f226799,0x3edae880,0x3f064b82,0x3f1d7fd1,0x3f32c8c9,0x3ef15aea,0x3f13682a,0x3f2beb4a,0x3f41d870,0x3f039c3d,0x3f1ff6cb,0x3f396842,0x3f4f7a1f,
0x3f0e39da,0x3f2beb4a,0x3f45e403,0x3f5b941a,0x3f187fc0,0x3f373a23,0x3f514d3d,0x3f660f88,0x3f226799,0x3f41d870,0x3f5b941a,0x3f6ed89e,0x3f2beb4a,0x3f4bbbf8,0x3f64aa59,0x3f75dec6,
0x3f3504f3,0x3f54db31,0x3f6c835e,0x3f7b14be,0x3f3daef9,0x3f5d2d53,0x3f731447,0x3f7e70b0,0x3f45e403,0x3f64aa59,0x3f7853f8,0x3f7fec43,0x3f4d9f02,0x3f6b4b0c,0x3f7c3b28,0x3f7f84ab,
0x3f54db31,0x3f710908,0x3f7ec46d,0x3f7d3aac,0x3f5b941a,0x3f75dec6,0x3f7fec43,0x3f791298,0x3f61c598,0x3f79c79d,0x3f7fb10f,0x3f731447,0x3f676bd8,0x3f7cbfc9,0x3f7e1324,0x3f6b4b0c,
0x3f6c835e,0x3f7ec46d,0x3f7b14be,0x3f61c598,0x3f710908,0x3f7fd397,0x3f76ba07,0x3f5695e5,0x3f74fa0b,0x3f7fec43,0x3f710908,0x3f49d112,0x3f7853f8,0x3f7f0e58,0x3f6a09a7,0x3f3b8f3b,
0x3f7b14be,0x3f7d3aac,0x3f61c598,0x3f2beb4a,0x3f7d3aac,0x3f7a7302,0x3f584853,0x3f1b02c6,0x3f7ec46d,0x3f76ba07,0x3f4d9f02,0x3f08f59b,0x3f7fb10f,0x3f721352,0x3f41d870,0x3eebcbbb,
0x0,0x0,0x0,0x0,0x3d621469,0x3d8a200a,0x3da3308c,0x3dbc3ac3,0x3de1bc2e,0x3e09cf86,0x3e22abb6,0x3e3b6ecf,0x3e28defc,0x3e4dee60,0x3e72b651,0x3e8b9507,
0x3e605c13,0x3e888e93,0x3ea09ae5,0x3eb8442a,0x3e8b9507,0x3ea986c4,0x3ec6d529,0x3ee363fa,0x3ea68f12,0x3ec9b953,0x3eebcbbb,0x3f064b82,0x3ec1071e,0x3ee900b7,0x3f07a136,0x3f19c200,
0x3edae880,0x3f039c3d,0x3f187fc0,0x3f2beb4a,0x3ef41f07,0x3f121eb0,0x3f286605,0x3f3ca003,0x3f064b82,0x3f1ff6cb,0x3f373a23,0x3f4bbbf8,0x3f121eb0,0x3f2d1469,0x3f44e3f5,0x3f591e6a,
0x3f1d7fd1,0x3f396842,0x3f514d3d,0x3f64aa59,0x3f286605,0x3f44e3f5,0x3f5c61c7,0x3f6e46be,0x3f32c8c9,0x3f4f7a1f,0x3f660f88,0x3f75dec6,0x3f3ca003,0x3f591e6a,0x3f6e46be,0x3f7b61fc,
0x3f45e403,0x3f61c598,0x3f74fa0b,0x3f7ec46d,0x3f4e8d90,0x3f696591,0x3f7a1e84,0x3f7ffec4,0x3f5695e5,0x3f6ff573,0x3f7dabcc,0x3f7f0e58,0x3f5df6be,0x3f756d97,0x3f7f9c18,0x3f7bf531,
0x3f64aa59,0x3f79c79d,0x3f7fec43,0x3f76ba07,0x3f6aab7b,0x3f7cfe73,0x3f7e9bc9,0x3f6f6830,0x3f6ff573,0x3f7f0e58,0x3f7baccd,0x3f660f88,0x3f748422,0x3f7ff4e6,0x3f772417,0x3f5ac450,
0x3f7853f8,0x3f7fb10f,0x3f710908,0x3f4d9f02,0x3f7b61fc,0x3f7e4323,0x3f696591,0x3f3ebc1b,0x3f7dabcc,0x3f7baccd,0x3f604621,0x3f2e3bde,0x3f7f2f9d,0x3f77f110,0x3f55b993,0x3f1c420c,
0x3f7fec43,0x3f731447,0x3f49d112,0x3f08f59b,0x3f7fe129,0x3f6d1c1d,0x3f3ca003,0x3ee900b7,0x3f7f0e58,0x3f660f88,0x3f2e3bde,0x3ebe1d4a,0x3f7d7474,0x3f5df6be,0x3f1ebc12,0x3e919ddd,
0x0,0x0,0x0,0x0,0x3dc8bd36,0x3de1bc2e,0x3dfab273,0x3e09cf86,0x3e47c5c2,0x3e605c13,0x3e78cfcc,0x3e888e93,0x3e94a031,0x3ea68f12,0x3eb8442a,0x3ec9b953,
0x3ec3ef15,0x3edae880,0x3ef15aea,0x3f039c3d,0x3ef15aea,0x3f064b82,0x3f13682a,0x3f1ff6cb,0x3f0e39da,0x3f1d7fd1,0x3f2beb4a,0x3f396842,0x3f226799,0x3f32c8c9,0x3f41d870,0x3f4f7a1f,
0x3f3504f3,0x3f45e403,0x3f54db31,0x3f61c598,0x3f45e403,0x3f5695e5,0x3f64aa59,0x3f6ff573,0x3f54db31,0x3f64aa59,0x3f710908,0x3f79c79d,0x3f61c598,0x3f6ff573,0x3f79c79d,0x3f7f0e58,
0x3f6c835e,0x3f7853f8,0x3f7ec46d,0x3f7fb10f,0x3f74fa0b,0x3f7dabcc,0x3f7fec43,0x3f7baccd,0x3f7b14be,0x3f7fec43,0x3f7d3aac,0x3f731447,0x3f7ec46d,0x3f7f0e58,0x3f76ba07,0x3f660f88,
0x3f800000,0x3f7b14be,0x3f6c835e,0x3f54db31,0x3f7ec46d,0x3f740bdd,0x3f5ebe05,0x3f3fc767,0x3f7b14be,0x3f6a09a7,0x3f4d9f02,0x3f273656,0x3f74fa0b,0x3f5d2d53,0x3f396842,0x3f0b9a6b,
0x3f6c835e,0x3f4d9f02,0x3f226799,0x3edae880,0x3f61c598,0x3f3b8f3b,0x3f08f59b,0x3e9aa086,0x3f54db31,0x3f273656,0x3edae880,0x3e2f10a2,0x3f45e403,0x3f10d3cd,0x3ea09ae5,0x3d16c32c,
0x3f3504f3,0x3ef15aea,0x3e47c5c2,0xbdc8bd36,0x3f226799,0x3ebe1d4a,0x3d96a905,0xbe6c9a7f,0x3f0e39da,0x3e888e93,0xbd48fb30,0xbeb8442a,0x3ef15aea,0x3e22abb6,0xbe2f10a2,0xbef6e0cb,
0x3ec3ef15,0x3d48fb30,0xbe94a031,0xbf187fc0,0x3e94a031,0xbd7b2b74,0xbecf7bca,0xbf32c8c9,0x3e47c5c2,0xbe2f10a2,0xbf039c3d,0xbf49d112,0x3dc8bd36,0xbe8e9a22,0xbf1d7fd1,0xbf5d2d53,
0x0,0x0,0x0,0x0,0x3dd53db9,0x3dee3876,0x3e039502,0x3e1008b7,0x3e541501,0x3e6c9a7f,0x3e827dc0,0x3e8e9a22,0x3e9d9e78,0x3eaf713a,0x3ec1071e,0x3ed25a09,
0x3ecf7bca,0x3ee63375,0x3efc5d27,0x3f08f59b,0x3eff17b2,0x3f0cead0,0x3f19c200,0x3f26050a,0x3f15f6d9,0x3f24d225,0x3f32c8c9,0x3f3fc767,0x3f2ac082,0x3f3a7ca4,0x3f48d8b3,0x3f55b993,
0x3f3daef9,0x3f4d9f02,0x3f5b941a,0x3f676bd8,0x3f4e8d90,0x3f5df6be,0x3f6aab7b,0x3f748422,0x3f5d2d53,0x3f6b4b0c,0x3f75dec6,0x3f7cbfc9,0x3f696591,0x3f756d97,0x3f7cfe73,0x3f7ff4e6,
0x3f731447,0x3f7c3b28,0x3f7fec43,0x3f7e1324,0x3f7a1e84,0x3f7f9c18,0x3f7e9bc9,0x3f772417,0x3f7e70b0,0x3f7f84ab,0x3f791298,0x3f6b4b0c,0x3f7ffec4,0x3f7bf531,0x3f6f6830,0x3f5ac450,
0x3f7ec46d,0x3f74fa0b,0x3f61c598,0x3f45e403,0x3f7ac516,0x3f6aab7b,0x3f5064af,0x3f2d1469,0x3f740bdd,0x3f5d2d53,0x3f3b8f3b,0x3f10d3cd,0x3f6aab7b,0x3f4cae79,0x3f239da9,0x3ee363fa,
0x3f5ebe05,0x3f396842,0x3f08f59b,0x3ea09ae5,0x3f5064af,0x3f239da9,0x3ed8106b,0x3e354098,0x3f3fc767,0x3f0b9a6b,0x3e9aa086,0x3d16c32c,0x3f2d1469,0x3ee363fa,0x3e354098,0xbdd53db9,
0x3f187fc0,0x3eac7cd4,0x3d48fb30,0xbe78cfcc,0x3f0242b1,0x3e667c66,0xbda3308c,0xbec1071e,0x3ed53641,0x3de1bc2e,0xbe541501,0xbf00e7e4,0x3ea395c5,0xbbc90f88,0xbea986c4,0xbf1ebc12,
0x3e605c13,0xbdfab273,0xbee63375,0xbf396842,0x3dee3876,0xbe72b651,0xbf0f8784,0xbf5064af,0x3c490e90,0xbeb263ef,0xbf299415,0xbf633c5a,0xbdbc3ac3,0xbee900b7,0xbf40d0da,0xbf718f57,
0x0,0x0,0x0,0x0,0x3e164083,0x3e22abb6,0x3e2f10a2,0x3e3b6ecf,0x3e94a031,0x3ea09ae5,0x3eac7cd4,0x3eb8442a,0x3edae880,0x3eebcbbb,0x3efc5d27,0x3f064b82,
0x3f0e39da,0x3f187fc0,0x3f226799,0x3f2beb4a,0x3f2beb4a,0x3f373a23,0x3f41d870,0x3f4bbbf8,0x3f45e403,0x3f514d3d,0x3f5b941a,0x3f64aa59,0x3f5b941a,0x3f660f88,0x3f6ed89e,0x3f75dec6,
0x3f6c835e,0x3f74fa0b,0x3f7b14be,0x3f7ec46d,0x3f7853f8,0x3f7dabcc,0x3f7fec43,0x3f7f0e58,0x3f7ec46d,0x3f7fec43,0x3f7d3aac,0x3f76ba07,0x3f7fb10f,0x3f7baccd,0x3f731447,0x3f660f88,
0x3f7b14be,0x3f710908,0x3f61c598,0x3f4d9f02,0x3f710908,0x3f604621,0x3f49d112,0x3f2e3bde,0x3f61c598,0x3f49d112,0x3f2beb4a,0x3f08f59b,0x3f4d9f02,0x3f2e3bde,0x3f08f59b,0x3ebe1d4a,
0x3f3504f3,0x3f0e39da,0x3ec3ef15,0x3e47c5c2,0x3f187fc0,0x3ed53641,0x3e605c13,0x3c490e90,0x3ef15aea,0x3e888e93,0x3d48fb30,0xbe2f10a2,0x3eac7cd4,0x3de1bc2e,0xbdfab273,0xbeb263ef,
0x3e47c5c2,0xbd48fb30,0xbe94a031,0xbf039c3d,0x3d48fb30,0xbe541501,0xbee63375,0xbf299415,0xbdc8bd36,0xbeb8442a,0xbf187fc0,0xbf49d112,0xbe78cfcc,0xbf00e7e4,0xbf396842,0xbf633c5a,
0xbec3ef15,0xbf226799,0xbf54db31,0xbf74fa0b,0xbf039c3d,0xbf3fc767,0xbf6a09a7,0xbf7e70b0,0xbf226799,0xbf584853,0xbf7853f8,0xbf7f4e6d,0xbf3daef9,0xbf6b4b0c,0xbf7f4e6d,0xbf778bc5,
0xbf54db31,0xbf7853f8,0xbf7ec46d,0xbf676bd8,0xbf676bd8,0xbf7f0e58,0xbf76ba07,0xbf4f7a1f,0xbf74fa0b,0xbf7f4e6d,0xbf676bd8,0xbf3085bb,0xbf7d3aac,0xbf791298,0xbf514d3d,0xbf0b9a6b,
0x0,0x0,0x0,0x0,0x3e1c76de,0x3e28defc,0x3e354098,0x3e419b37,0x3e9aa086,0x3ea68f12,0x3eb263ef,0x3ebe1d4a,0x3ee363fa,0x3ef41f07,0x3f0242b1,0x3f0a48ad,
0x3f13682a,0x3f1d7fd1,0x3f273656,0x3f3085bb,0x3f31a81d,0x3f3ca003,0x3f46e22a,0x3f5064af,0x3f4bbbf8,0x3f5695e5,0x3f604621,0x3f68bf3c,0x3f6106f2,0x3f6aab7b,0x3f7294f8,0x3f78b47b,
0x3f710908,0x3f7853f8,0x3f7d3aac,0x3f7fb10f,0x3f7b61fc,0x3f7f2f9d,0x3f7fe129,0x3f7d7474,0x3f7fd397,0x3f7f0e58,0x3f7a7302,0x3f721352,0x3f7e4323,0x3f77f110,0x3f6d1c1d,0x3f5df6be,
0x3f76ba07,0x3f6a09a7,0x3f584853,0x3f41d870,0x3f696591,0x3f55b993,0x3f3ca003,0x3f1ebc12,0x3f5695e5,0x3f3b8f3b,0x3f1b02c6,0x3eebcbbb,0x3f3ebc1b,0x3f1c420c,0x3ee900b7,0x3e919ddd,
0x3f226799,0x3ef15aea,0x3e94a031,0x3dc8bd36,0x3f0242b1,0x3ea395c5,0x3dee3876,0xbdbc3ac3,0x3ebe1d4a,0x3e22abb6,0xbd7b2b74,0xbe8e9a22,0x3e667c66,0xbbc90f88,0xbe72b651,0xbee900b7,
0x3d96a905,0xbe2f10a2,0xbecf7bca,0xbf1d7fd1,0xbda3308c,0xbea986c4,0xbf0f8784,0xbf40d0da,0xbe6c9a7f,0xbef6e0cb,0xbf32c8c9,0xbf5d2d53,0xbec1071e,0xbf1ebc12,0xbf5064af,0xbf718f57,
0xbf039c3d,0xbf3daef9,0xbf676bd8,0xbf7d3aac,0xbf239da9,0xbf577026,0xbf772417,0xbf7fc38f,0xbf3fc767,0xbf6b4b0c,0xbf7f0e58,0xbf791298,0xbf577026,0xbf78b47b,0xbf7eea9d,0xbf696591,
0xbf6a09a7,0xbf7f4e6d,0xbf76ba07,0xbf514d3d,0xbf772417,0xbf7eea9d,0xbf66becc,0xbf31a81d,0xbf7e70b0,0xbf778bc5,0xbf4f7a1f,0xbf0b9a6b,0xbf7fc38f,0xbf696591,0xbf31a81d,0xbec1071e,
};
constexpr unsigned WS_H    = 0;
constexpr unsigned WS_ZW   = 16u<<20;
constexpr unsigned WS_Z2   = 24u<<20;
constexpr unsigned WS_PART = 28u<<20;
constexpr unsigned WS_CS   = 36u<<20;
constexpr unsigned WS_STAT = 37u<<20;
constexpr unsigned WS_FLAG = 38u<<20;
constexpr unsigned WS_E = WS_FLAG, WS_F0 = WS_FLAG + 12288, WS_F1 = WS_FLAG + 4096, WS_F2 = WS_FLAG + 8192, WS_F3 = WS_FLAG + 65536, WS_F4 = WS_FLAG + 131072;
constexpr unsigned WS_USED = 40u<<20;

constexpr int A_SLOT = 16384, B_SLOT = 14336;
constexpr int G1_A = 0, G1_B = 3*A_SLOT, L_EX = G1_B + 6*B_SLOT;
constexpr int L_MISC = L_EX + 18432;
constexpr int L_BN = L_MISC, L_CN = L_MISC + 1024, L_ST = L_MISC + 2048, L_RS = L_MISC + 3072, L_FL = L_MISC + 3200, L_ODD = L_MISC + 4096, L_DUMP = L_MISC + 8192;
constexpr int L_TOT = 163840;
constexpr int C_IN0 = L_EX, C_IN1 = 81920, C_EXC = 65536, C_OUT = L_EX, C_TW = 98304, C_MAT = 114688, E_K = 32768;
constexpr int G2_SLOT = 32768, G2_A = 0, G2_B = 16384, E_Z = 131072, E_Y = 65536;

#define TS(slot)
#define CY(var)

DI void dma16(i32x4 rsrc, unsigned voff, unsigned soff, unsigned ldsdst) {
  asm volatile("s_mov_b32 m0, %3\n\ts_nop 0\n\tbuffer_load_dwordx4 %0, %1, %2 offen lds" :: "v"(voff), "s"(rsrc), "s"(soff), "s"(ldsdst) : "memory");
}
DI void dma16s(i32x4 rsrc, unsigned voff, unsigned soff, unsigned ldsdst) {
  asm volatile("s_mov_b32 m0, %3\n\ts_nop 0\n\tbuffer_load_dwordx4 %0, %1, %2 offen sc1 lds" :: "v"(voff), "s"(rsrc), "s"(soff), "s"(ldsdst) : "memory");
}
DI void touch(i32x4 rsrc, unsigned voff, unsigned soff, unsigned ldsdst) {
  asm volatile("s_mov_b32 m0, %3\n\ts_nop 0\n\tbuffer_load_dword %0, %1, %2 offen lds" :: "v"(voff), "s"(rsrc), "s"(soff), "s"(ldsdst) : "memory");
}
DI i32x4 mkrsrc(const void* p, unsigned bytes) {
  u64 a = (u64)p; i32x4 r; r[0] = __builtin_amdgcn_readfirstlane((unsigned)a); r[1] = __builtin_amdgcn_readfirstlane((unsigned)(a>>32)) & 0xffff; r[2] = bytes; r[3] = 0x00020000; return r;
}
typedef __amdgpu_buffer_rsrc_t rsrc_t;
DI void st16(rsrc_t r, unsigned off, u32x4 v) { __builtin_amdgcn_raw_buffer_store_b128(v, r, off, 0, 16); }
DI void st8(rsrc_t r, unsigned off, u32x2 v) { __builtin_amdgcn_raw_buffer_store_b64(v, r, off, 0, 16); }
DI u32x4 ld16(rsrc_t r, unsigned off) { return __builtin_amdgcn_raw_buffer_load_b128(r, off, 0, 16); }
DI u32x2 ld8(rsrc_t r, unsigned off) { return __builtin_amdgcn_raw_buffer_load_b64(r, off, 0, 16); }
DI unsigned cvtpk(float lo, float hi) { f32x2 v = {lo, hi}; return __builtin_bit_cast(unsigned, __builtin_convertvector(v, bf16x2)); }
DI float bflo(unsigned u) { return __uint_as_float(u << 16); }
DI float bfhi(unsigned u) { return __uint_as_float(u & 0xffff0000u); }
#define WAITV(n) asm volatile("s_waitcnt vmcnt(%0)" :: "n"(n) : "memory")
#define BAR() do { asm volatile("" ::: "memory"); __builtin_amdgcn_s_barrier(); asm volatile("" ::: "memory"); } while(0)
#define SB() __builtin_amdgcn_sched_barrier(0)
#define WAITL0() __builtin_amdgcn_s_waitcnt(0xC07F)
#define LBAR() do { WAITL0(); BAR(); } while(0)
#define PIN(x) asm volatile("" : "+v"(x))
DI s16x4 trrd(unsigned a) { return __builtin_amdgcn_ds_read_tr16_b64_v4i16((LAS s16x4*)(size_t)a); }
DI bf16x8 cat8(s16x4 lo, s16x4 hi) { return __builtin_bit_cast(bf16x8, __builtin_shufflevector(lo, hi, 0,1,2,3,4,5,6,7)); }
DI f32x16 mfma32(bf16x8 a, bf16x8 b, f32x16 c) { return __builtin_amdgcn_mfma_f32_32x32x16_bf16(a, b, c, 0,0,0); }
DI float cosr(float r) { return __builtin_amdgcn_cosf(r); }
DI float sinr(float r) { return __builtin_amdgcn_sinf(r); }
DI int crow(int r, int h) { return (r & 3) + 8*(r >> 2) + 4*h; }
DI bf16x8 pack8(const float* v) { u32x4 p = { cvtpk(v[0],v[1]), cvtpk(v[2],v[3]), cvtpk(v[4],v[5]), cvtpk(v[6],v[7]) }; return __builtin_bit_cast(bf16x8, p); }
DI f32x16 zero16() { f32x16 z;
  #pragma unroll
  for (int i=0;i<16;i++) z[i]=0.f; return z; }
DI bf16x8 xor8(bf16x8 v, unsigned m) { u32x4 p = __builtin_bit_cast(u32x4, v); p ^= m; return __builtin_bit_cast(bf16x8, p); }
DI bf16x8 neg8(bf16x8 v) { return xor8(v, 0x80008000u); }
template<typename T> DI T ldsr(unsigned a) { return *(LAS T*)(size_t)a; }
template<typename T> DI void ldsw(unsigned a, T v) { *(LAS T*)(size_t)a = v; }

template<int CTRL> DI float dppadd(float v) { return v + __builtin_bit_cast(float, __builtin_amdgcn_update_dpp(0, __builtin_bit_cast(int, v), CTRL, 0xf, 0xf, true)); }
DI float wave_sum(float v) {
  v = dppadd<0xB1>(v); v = dppadd<0x4E>(v); v = dppadd<0x141>(v); v = dppadd<0x140>(v);
  { auto r = __builtin_amdgcn_permlane16_swap(__float_as_uint(v), __float_as_uint(v), false, false); v = __uint_as_float(r[0]) + __uint_as_float(r[1]); }
  { auto r = __builtin_amdgcn_permlane32_swap(__float_as_uint(v), __float_as_uint(v), false, false); v = __uint_as_float(r[0]) + __uint_as_float(r[1]); }
  return v;
}
DI void trig_frag(float step, float base, bf16x8& c, bf16x8& s) {
  float cv[8], sv[8];
  #pragma unroll
  for (int j=0;j<8;j++) { float r = fmaf(step, (float)(8*(j>>2) + (j&3)), base); cv[j] = cosr(r); sv[j] = sinr(r); }
  c = pack8(cv); s = pack8(sv);
}
DI u32x4 mish8(f32x4 u0, f32x4 u1) {
  f32x2 u[4] = {{u0[0],u0[1]}, {u0[2],u0[3]}, {u1[0],u1[1]}, {u1[2],u1[3]}}, t[4], d[4];
  #pragma unroll
  for (int i=0;i<4;i++) { t[i].x = __builtin_amdgcn_exp2f(u[i].x); t[i].y = __builtin_amdgcn_exp2f(u[i].y); }
  #pragma unroll
  for (int i=0;i<4;i++) d[i] = t[i]*0.5f + 1.0f;
  #pragma unroll
  for (int i=0;i<4;i++) d[i] = t[i]*d[i] + 1.0f;
  #pragma unroll
  for (int i=0;i<4;i++) { t[i].x = __builtin_amdgcn_rcpf(d[i].x); t[i].y = __builtin_amdgcn_rcpf(d[i].y); }
  #pragma unroll
  for (int i=0;i<4;i++) d[i] = u[i] - u[i]*t[i];
  return (u32x4){cvtpk(d[0].x, d[0].y), cvtpk(d[1].x, d[1].y), cvtpk(d[2].x, d[2].y), cvtpk(d[3].x, d[3].y)};
}
constexpr int SPIN_MAX = 1 << 17;
DI void poll1(rsrc_t r, unsigned off, bool act, u32x2 tag) {
  #pragma unroll 1
  for (int it = 0; it < SPIN_MAX; it++) {
    u32x2 v = ld8(r, off);
    if (__builtin_amdgcn_ballot_w64(act && (v[0] != tag[0] || v[1] != tag[1])) == 0) break;
    __builtin_amdgcn_s_sleep(2);
  }
  asm volatile("" ::: "memory");
}
DI void poll256(rsrc_t r, unsigned base, int lane, u32x2 tag) {
  #pragma unroll 1
  for (int it = 0; it < SPIN_MAX; it++) {
    u32x4 a = ld16(r, base + lane*16), b = ld16(r, base + 1024 + lane*16);
    bool bad = a[0] != tag[0] || a[1] != tag[1] || a[2] != tag[0] || a[3] != tag[1] || b[0] != tag[0] || b[1] != tag[1] || b[2] != tag[0] || b[3] != tag[1];
    if (__builtin_amdgcn_ballot_w64(bad) == 0) break;
    __builtin_amdgcn_s_sleep(4);
  }
  asm volatile("" ::: "memory");
}
DI bool bad256(u32x4 a, u32x4 b, u32x2 tag) {
  return __builtin_amdgcn_ballot_w64(a[0] != tag[0] || a[1] != tag[1] || a[2] != tag[0] || a[3] != tag[1] || b[0] != tag[0] || b[1] != tag[1] || b[2] != tag[0] || b[3] != tag[1]) != 0;
}
DI bool last_arriver(unsigned addr, int lane, unsigned n) {
  unsigned old = 0;
  if (lane == 0) old = __hip_atomic_fetch_add((LAS unsigned*)(size_t)addr, 1u, __ATOMIC_RELAXED, __HIP_MEMORY_SCOPE_WORKGROUP);
  return (unsigned)__builtin_amdgcn_readfirstlane(old) == n - 1;
}
DI void lwait(unsigned flbase, int lane, unsigned phase, unsigned mask) {
  asm volatile("" ::: "memory");
  #pragma unroll 1
  for (int it = 0; it < (1 << 22); it++) {
    unsigned v = *(volatile LAS unsigned*)(size_t)(flbase + (lane & 3)*4);
    if (__builtin_amdgcn_ballot_w64(((mask >> (lane & 3)) & 1) && v < phase) == 0) break;
    __builtin_amdgcn_s_sleep(1);
  }
  asm volatile("" ::: "memory");
}
DI void lsync(unsigned flbase, int me, int lane, unsigned phase, unsigned mask) {
  WAITL0(); asm volatile("" ::: "memory");
  if (lane == 0) *(volatile LAS unsigned*)(size_t)(flbase + me*4) = phase;
  #pragma unroll 1
  for (int it = 0; it < (1 << 22); it++) {
    unsigned v = *(volatile LAS unsigned*)(size_t)(flbase + (lane & 3)*4);
    if (__builtin_amdgcn_ballot_w64(((mask >> (lane & 3)) & 1) && v < phase) == 0) break;
    __builtin_amdgcn_s_sleep(1);
  }
  asm volatile("" ::: "memory");
}

__global__ __launch_bounds__(512, 2) void k_all(const u16* __restrict__ x, const u16* __restrict__ wr, const u16* __restrict__ wi,
    const u16* __restrict__ w1, const u16* __restrict__ b1, const u16* __restrict__ w2, char* __restrict__ ws, u16* __restrict__ out) {
  extern __shared__ __attribute__((aligned(16))) char smem[];
  const int wid = __builtin_amdgcn_readfirstlane(threadIdx.x >> 6), lane = threadIdx.x & 63;
  const int b = blockIdx.x, xcd = b & 7, idx = b >> 3;
  const int tm = (xcd >> 1)*4 + (idx & 3), ks = xcd & 1, tn2 = idx >> 2, tn = ks*8 + tn2;
  const int gid = tm*16 + tn;
  const int rot = ROT ? (7*tn2 + 1) >> 1 : 0;
  const int gi = lane >> 4, q = (lane & 15) >> 2, p = lane & 3, h = lane >> 5, l31 = lane & 31;
  TS(0);
  const i32x4 rx = mkrsrc(x, S_*D_*2), rb = mkrsrc(w1, D_*F_*2), rt = mkrsrc(TAB, 40960);
  if (wid < 4) {
    #pragma unroll
    for (int i=0;i<4;i++) dma16(rx, lane*16, gid*16384 + (wid*4+i)*1024, L_EX + (wid*4+i)*1024);
  } else if (wid == 7) {
    if (lane < 16) *(volatile LAS unsigned*)(size_t)(L_FL + lane*4) = 0u;
    u64 pc; asm volatile("s_getpc_b64 %0" : "=s"(pc));
    touch(mkrsrc((const void*)pc, CODE_BYTES), (unsigned)((idx*16 + (lane & 15))*128), 0, L_DUMP);
    touch(rt, (unsigned)((idx*16 + (lane & 15))*128), 0, L_DUMP);
  }
  u64 ep = *(const __attribute__((address_space(4))) u64*)(size_t)(ws + WS_E);
  const rsrc_t rw_ = __builtin_amdgcn_make_buffer_rsrc(ws, 0, WS_USED, 0x00020000);
  const i32x4 rz = mkrsrc(ws, WS_USED);
  u32x2 tag;
  #define GETTAG() do { asm volatile("" : "+s"(ep) :: "memory"); u64 t_ = ep * 0x9E3779B97F4A7C15ull + 0x632BE59BD9B4E019ull; tag = (u32x2){(unsigned)t_, (unsigned)(t_ >> 32)}; } while (0)

  if (wid < 4) {
    {
      bf16x8 Cp[2], Sp[2], M1[2], M2[2];
      { float f = (float)l31 * (1.0f/32); trig_frag(f, f*(float)(4*h), Cp[0], Sp[0]); Sp[0] = neg8(Sp[0]);
        unsigned m = (l31 & 1) ? 0x80008000u : 0u; Cp[1] = xor8(Cp[0], m); Sp[1] = xor8(Sp[0], m);
        float f2 = (float)(l31 & 15) * (1.0f/32); trig_frag(f2, f2*(float)(4*h) + ((l31 >= 16) ? 0.25f : 0.f), M1[0], M2[0]);
        M1[1] = xor8(M1[0], m); M2[1] = xor8(M2[0], m); }
      float twc[16], tws[16];
      { float f = (float)l31 * (1.0f/1024), bs = f*(float)(4*h);
        #pragma unroll
        for (int r=0;r<16;r++) { float a = fmaf(f, (float)crow(r,0), bs); twc[r] = cosr(a); tws[r] = sinr(a); PIN(twc[r]); PIN(tws[r]); } }
      TS(9);
      GETTAG();
      WAITV(0);
      TS(1);
      bf16x8 xf[2][2];
      #pragma unroll
      for (int rr=0; rr<2; rr++)
        #pragma unroll
        for (int s=0;s<2;s++) { unsigned base = L_EX + (wid*2+rr)*2048 + (16*s + 4*(gi>>1) + q)*64 + (gi&1)*32 + 8*p; xf[rr][s] = cat8(trrd(base), trrd(base + 512)); }
      f32x16 X[2];
      f32x2 stat[2];
      #pragma unroll
      for (int rr=0; rr<2; rr++) {
        f32x16 Pr = zero16(), Pi = zero16();
        #pragma unroll
        for (int s=0;s<2;s++) { Pr = mfma32(xf[rr][s], Cp[s], Pr); Pi = mfma32(xf[rr][s], Sp[s], Pi); }
        float s1 = 0.f, s2 = 0.f;
        const bf16x2 one2 = __builtin_bit_cast(bf16x2, 0x3f803f80u);
        #pragma unroll
        for (int s=0;s<2;s++) { u32x4 u = __builtin_bit_cast(u32x4, xf[rr][s]);
          #pragma unroll
          for (int e=0;e<4;e++) { unsigned ue = u[e]; bf16x2 v = __builtin_bit_cast(bf16x2, ue); s1 = __builtin_amdgcn_fdot2_f32_bf16(v, one2, s1, false); s2 = __builtin_amdgcn_fdot2_f32_bf16(v, v, s2, false); } }
        float ny = wave_sum((lane & 1) ? -s1 : s1);
        s1 = wave_sum(s1); s2 = wave_sum(s2);
        float mean = s1 * (1.0f/D_), var = fmaxf(s2 * (1.0f/D_) - mean*mean, 0.f);
        float rstd = __builtin_amdgcn_rsqf(var + 1e-5f);
        stat[rr] = (f32x2){rstd * 1.4426950408889634f, mean * rstd * 1.4426950408889634f};
        float sub = (l31 == 0) ? 32.f*mean : 0.f;
        float tr_[16], ti_[16];
        #pragma unroll
        for (int r=0;r<16;r++) { float a = Pr[r] - sub, bb = Pi[r]; tr_[r] = a*twc[r] + bb*tws[r]; ti_[r] = bb*twc[r] - a*tws[r]; }
        f32x16 Y = zero16();
        #pragma unroll
        for (int s=0;s<2;s++) { Y = mfma32(M1[s], pack8(tr_ + 8*s), Y); Y = mfma32(M2[s], pack8(ti_ + 8*s), Y); }
        if (lane == 0) { Y[0] = ny; Y[8] = 0.f; }
        X[rr] = Y * rstd;
      }
      PIN(X[0]); PIN(X[1]);
      TS(10);
      #pragma unroll
      for (int r=0;r<8;r++) ldsw<u32x2>(L_EX + wid*4096 + (l31 + 32*crow(r,h))*8, (u32x2){cvtpk(X[0][r], X[1][r]), cvtpk(X[0][r+8], X[1][r+8])});
      if (lane == 0) ldsw<f32x4>(L_RS + wid*16, (f32x4){stat[0].x, stat[0].y, stat[1].x, stat[1].y});
    }
    const int wm = wid & 1, wn = wid >> 1;
    const int g = gi, c = lane & 15;
    {
      f32x4 acc[7][4];
      #pragma unroll
      for (int i=0;i<7;i++)
        #pragma unroll
        for (int j=0;j<4;j++) { acc[i][j] = (f32x4){0,0,0,0}; PIN(acc[i][j]); }
      unsigned xak[2];
      xak[0] = G1_A + ((wm*64 + c)>>3)*1024 + (c&7)*128 + ((g ^ (c&7))*16);
      xak[1] = G1_A + ((wm*64 + c)>>3)*1024 + (c&7)*128 + (((4+g) ^ (c&7))*16);
      const unsigned wbase = G1_B + (8*g + q)*448 + 8*p;
      const unsigned sgn = (g&1)*32;
      unsigned wb[2][3];
      wb[0][0] = wbase + wn*256 + sgn;
      wb[0][1] = wbase + wn*256 - sgn;
      wb[0][2] = wn ? wbase + 32 - sgn : wbase + sgn;
      #pragma unroll
      for (int i=0;i<3;i++) { wb[1][i] = wb[0][i] + 3*B_SLOT; PIN(wb[0][i]); PIN(wb[1][i]); }
      PIN(xak[0]); PIN(xak[1]);
      bf16x8 xf[2][4], wf[2][7];
      s16x4 lo_[7];
      auto rd = [&](int set, int r, int n) {
        if (n >= 2 && n < 6) { xf[set][n-2] = ldsr<bf16x8>(xak[r&1] + (r>>1)*A_SLOT + (n-2)*2048); return; }
        int i = n < 2 ? 0 : (n-6)/2+1, t = n < 2 ? n : (n-6)&1;
        s16x4 v = trrd(wb[r/3][i == 6 ? 2 : (i&1)] + (r%3)*B_SLOT + i*32 + t*1792);
        if (t==0) lo_[i] = v; else wf[set][i] = cat8(lo_[i], v);
      };
      auto mm = [&](int set, int n) { int i = n>>2, j = (i&1) ? 3-(n&3) : (n&3);
        asm volatile("v_mfma_f32_16x16x32_bf16 %0, %1, %2, %0" : "+v"(acc[i][j]) : "v"(wf[set][i]), "v"(xf[set][j])); };
      WAITL0(); TS(11);
      LBAR();
      TS(2); CY(cy0);
      #pragma unroll
      for (int n=0;n<18;n++) rd(0, 0, n);
      WAITL0();
      auto step = [&](int set, int r1, bool bar) {
        SB();
        #pragma unroll
        for (int n=0;n<28;n++) { mm(set, n); SB(); if (n < 18) { rd(set^1, r1, n); SB(); }
          if (n == 25) { WAITL0(); SB(); }
          if (n == 26 && bar) { BAR(); SB(); } }
      };
      #pragma unroll 1
      for (int s = 0; s < 30; s += 6) { step(0, 1, true); step(1, 2, false); step(0, 3, true); step(1, 4, false); step(0, 5, true); step(1, 0, false); }
      step(0, 1, true);
      f32x4 bl[7], cl[7]; f32x2 st[4];
      #pragma unroll
      for (int n=0;n<28;n++) { mm(1, n); SB();
        if (n < 7) bl[n] = ldsr<f32x4>(L_BN + ((n == 6 ? wn*16 : wn*128) + n*16 + 4*g)*4);
        else if (n < 14) cl[n-7] = ldsr<f32x4>(L_CN + ((n == 13 ? wn*16 : wn*128) + (n-7)*16 + 4*g)*4);
        else if (n < 18) st[n-14] = ldsr<f32x2>(L_ST + (wm*64 + (n-14)*16 + c)*8);
        SB(); }
      asm volatile("s_nop 7\n\ts_nop 7" ::: "memory");
      TS(3); CY(cy1);
      SB();
      auto pre = [&](int i, int j) { return acc[i][j] * st[j].x + (bl[i] - cl[i] * st[j].y); };
      u32x4 keep;
      { u32x4 o01 = mish8(pre(6,0), pre(6,1)), o23 = mish8(pre(6,2), pre(6,3));
        keep = wn ? o23 : o01;
        ldsw<u32x4>(L_ODD + wid*1024 + lane*16, wn ? o01 : o23);
        if (lane == 0) *(volatile LAS unsigned*)(size_t)(L_FL + 16 + wid*4) = 1u; }
      const unsigned hb0 = WS_H + (unsigned)(((tm*8 + wm*4)*112 + tn*7)*1024 + lane*16);
      #pragma unroll
      for (int j=0;j<4;j++) {
        #pragma unroll
        for (int ip=0; ip<3; ip++) st16(rw_, hb0 + j*112*1024 + (wn*4 + ip)*1024, mish8(pre(2*ip,j), pre(2*ip+1,j)));
        if (j == 1) {
          #pragma unroll 1
          for (int it = 0; it < (1 << 22); it++) { if (*(volatile LAS unsigned*)(size_t)(L_FL + 16 + (wid ^ 2)*4) != 0u) break; }
          asm volatile("" ::: "memory");
          u32x4 rcv = ldsr<u32x4>(L_ODD + (wid ^ 2)*1024 + lane*16);
          u32x4 lo = wn ? rcv : keep, hi = wn ? keep : rcv;
          st16(rw_, hb0 + (2*wn)*112*1024 + 3*1024, (u32x4){lo[0], lo[1], hi[0], hi[1]});
          st16(rw_, hb0 + (2*wn+1)*112*1024 + 3*1024, (u32x4){lo[2], lo[3], hi[2], hi[3]});
        }
      }
      TS(4);
      WAITV(0);
      TS(12);
      if (last_arriver(L_FL + 36, lane, 4)) {
        if (lane == 0) st8(rw_, WS_F3 + gid*128, tag);
        if (!ROT) poll1(rw_, WS_F3 + (unsigned)((tm*16 + ks*8 + (lane & 7))*128), true, tag);
        #pragma unroll 1
        for (int k=0;k<2;k++)
          #pragma unroll
          for (int pc=0;pc<16;pc++) dma16s(rz, lane*16, WS_H + ((tm*8 + (pc>>1))*112 + ks*56 + (pc&1) + 2*(k + rot))*1024, G2_A + k*G2_SLOT + pc*1024);
      }
    }
    {
      f32x4 acc[4][4];
      #pragma unroll
      for (int i=0;i<4;i++)
        #pragma unroll
        for (int j=0;j<4;j++) { acc[i][j] = (f32x4){0,0,0,0}; PIN(acc[i][j]); }
      unsigned xa[2], wb[2][4];
      xa[0] = G2_A + wm*8192 + lane*16; xa[1] = xa[0] + 2*G2_SLOT; PIN(xa[0]); PIN(xa[1]);
      { int rho = 4*(g&1) + q;
        #pragma unroll
        for (int i=0;i<4;i++) { wb[0][i] = G2_B + (4*g + q)*256 + (((wn*4 + i) ^ rho)*32) + 8*p; wb[1][i] = wb[0][i] + 2*G2_SLOT; PIN(wb[0][i]); PIN(wb[1][i]); } }
      bf16x8 xf[2][2][4], wf[2][2][4];
      s16x4 lo_;
      auto rd = [&](int set, int r, int n) {
        int sub = n / 12, m = n % 12;
        if (m >= 2 && m < 6) { xf[set][sub][m-2] = ldsr<bf16x8>(xa[r>>1] + (r&1)*G2_SLOT + (m-2)*2048 + sub*1024); return; }
        int i = m < 2 ? 0 : (m-6)/2+1, t = m < 2 ? m : (m-6)&1;
        s16x4 v = trrd(wb[r>>1][i] + (r&1)*G2_SLOT + sub*8192 + t*4096);
        if (t==0) lo_ = v; else wf[set][sub][i] = cat8(lo_, v);
      };
      auto mm = [&](int set, int n) { int sub = n & 1, m = n >> 1; int i = m>>2, j = (i&1) ? 3-(m&3) : (m&3);
        asm volatile("v_mfma_f32_16x16x32_bf16 %0, %1, %2, %0" : "+v"(acc[i][j]) : "v"(wf[set][sub][i]), "v"(xf[set][sub][j])); };
      WAITV(16); BAR();
      TS(5); CY(cy2);
      #pragma unroll
      for (int n=0;n<24;n++) rd(0, 0, n);
      WAITV(0); WAITL0(); BAR();
      auto step = [&](int set, int r1, bool bar) {
        SB();
        #pragma unroll
        for (int n=0;n<32;n++) { mm(set, n); SB(); if (n < 24) { rd(set^1, r1, n); SB(); }
          if (n == 29) { WAITL0(); SB(); }
          if (n == 30 && bar) { BAR(); SB(); } }
      };
      #pragma unroll 1
      for (int s = 0; s < 24; s += 4) { step(0, 1, true); step(1, 2, true); step(0, 3, true); step(1, 0, true); }
      step(0, 1, true); step(1, 2, true); step(0, 3, false);
      #pragma unroll
      for (int n=0;n<32;n++) { mm(1, n); SB(); if (n == 27) { BAR(); SB(); } }
      asm volatile("s_nop 7\n\ts_nop 7" ::: "memory");
      TS(6); CY(cy3);
      SB();
      const unsigned pb = G2_A + wid*8192;
      #pragma unroll
      for (int j=0;j<4;j++)
        #pragma unroll
        for (int i=0;i<4;i++) ldsw<u32x2>(pb + (j*16 + c)*128 + (((i*2 + (g>>1)) ^ (c & 7))*16) + (g&1)*8, (u32x2){cvtpk(acc[i][j][0], acc[i][j][1]), cvtpk(acc[i][j][2], acc[i][j][3])});
      asm volatile("" ::: "memory");
      #pragma unroll
      for (int i=0;i<8;i++) { int r = i*8 + (lane >> 3); u32x4 v = ldsr<u32x4>(pb + r*128 + (((lane & 7) ^ (r & 7))*16));
        st16(rw_, WS_PART + ks*(4u<<20) + (unsigned)((tm*128 + wm*64 + r)*2048 + tn2*256 + wn*128 + (lane & 7)*16), v); }
      TS(9);
      WAITV(0);
      TS(10);
      if (last_arriver(L_FL + 40 + wm*4, lane, 2) && lane == 0) st8(rw_, WS_F4 + (gid*2 + wm)*128, tag);
    }
  } else {
    const int w = wid - 4;
    const int col = w >> 1, hf = w & 1;
    auto cs_load = [&]() {
      #pragma unroll 2
      for (int pc=0; pc<14; pc++) { int ci = pc*64 + lane; int r = (ci*9363)>>18, c = ci - r*28; c ^= ((r>>3)&1)<<1;
        dma16(rb, (unsigned)(r*F_*2 + tn*448 + c*16), ((w-2)*512 + tm*32)*F_*2, G1_B + (2+w)*B_SLOT + pc*1024); } };
    const int a = w - 2, t = a*64 + lane;
    f32x2 csn = {0.f, 0.f}, bn = {0.f, 0.f}; u32x2 stv = {0u, 0u};
    const char* pfp = (const char*)w2 + (w & 1)*(1792*2048) + b*8064;
    #define PF(n) do { _Pragma("unroll") for (int i_=0;i_<14;i_++) asm volatile("s_load_dword s99, %0, %1" :: "s"(pfp), "n"(((n)*14 + i_)*64) : "memory", "s99"); } while (0)
    if (w < 2) {
      __builtin_amdgcn_s_setprio(2);
      unsigned va[8], vb[14];
      #pragma unroll
      for (int i=0;i<8;i++) { int pc = w*8+i; int r = pc*8 + (lane>>3); va[i] = (unsigned)((tm*128 + r)*D_*2 + (((lane&7) ^ (r&7))*16)); }
      #pragma unroll
      for (int i=0;i<14;i++) { int pc = w + 2*i; int ci = pc*64 + lane; int r = (ci*9363)>>18, c = ci - r*28; c ^= ((r>>3)&1)<<1; vb[i] = (unsigned)(r*F_*2 + tn*448 + c*16); }
      auto issueA = [&](int k, int slot) {
        #pragma unroll
        for (int i=0;i<8;i++) dma16(rx, va[i], k*128, G1_A + w*8192 + slot*A_SLOT + i*1024); };
      auto issueB = [&](int k, int slot, int i0 = 0, int i1 = 14) {
        #pragma unroll
        for (int i=i0;i<i1;i++) dma16(rb, vb[i], k*64*F_*2, G1_B + w*1024 + slot*2*B_SLOT + i*2048); };
      #pragma unroll 1
      for (int k=0;k<2;k++) { issueB(k,k); issueA(k,k); }
      issueA(2,2);
      TS(9);
      GETTAG();
      WAITV(30); BAR();
      issueB(2,2);
      int slot = 0;
      #pragma unroll 1
      for (int k = 0; k < 13; k++) { WAITV(22); BAR(); issueA(k+3, slot); issueB(k+3, slot); slot = slot == 2 ? 0 : slot+1; }
      WAITV(22); BAR();
      #pragma unroll
      for (int i=0;i<4;i++) dma16(rt, lane*16, (20 + w*4 + i)*1024, C_TW + (w*4 + i)*1024);
      { u32x4 fa = ld16(rw_, WS_F1 + lane*16), fb = ld16(rw_, WS_F1 + 1024 + lane*16);
        bool bad = bad256(fa, fb, tag);
        WAITV(0); BAR();
        if (bad) poll256(rw_, WS_F1, lane, tag); asm volatile("" ::: "memory"); }
      if (b == 0 && w == 0 && lane == 0) { u64 e1 = ep + 1; st8(rw_, WS_E, (u32x2){(unsigned)e1, (unsigned)(e1 >> 32)}); }
      __builtin_amdgcn_s_sleep(GDELAY);
      const unsigned gv = (unsigned)((lane >> 3)*32768);
      #pragma unroll
      for (int i=0;i<16;i++) { int pc = w*16 + i; dma16s(rz, gv + (((lane & 7) ^ (((i&1)*4 + (lane >> 4)) & 7))*16), WS_ZW + pc*8*32768 + gid*128, (w ? C_IN1 : C_IN0) + i*1024);
        if (i == 11) BAR(); }
      #pragma unroll
      for (int i=0;i<10;i++) dma16(rt, lane*16, (w*10 + i)*1024, C_MAT + (w*10 + i)*1024);
    } else {
      const i32x4 rwt = mkrsrc(a ? wi : wr, S_*DF_*2);
      auto cs_sum = [&](f32x4 res) {
        const u32x4 onesu = {0x3f803f80u,0x3f803f80u,0x3f803f80u,0x3f803f80u};
        const bf16x8 ones = __builtin_bit_cast(bf16x8, onesu);
        const unsigned base = G1_B + (4+a)*B_SLOT + (8*gi + q)*448 + 8*p, sg = (gi&1)*32;
        bf16x8 f[14];
        #pragma unroll
        for (int u=0; u<14; u++) { unsigned ad = ((u&1) ? base - sg : base + sg) + u*32; f[u] = cat8(trrd(ad), trrd(ad + 1792)); }
        f32x4 sel = {0,0,0,0};
        #pragma unroll
        for (int u=0; u<14; u++) { f32x4 d = __builtin_amdgcn_mfma_f32_16x16x32_bf16(ones, f[u], (f32x4){0,0,0,0}, 0,0,0); if (gi == (u & 3)) sel[u >> 2] = d[0]; }
        return res + sel; };
      if (a == 1) __builtin_amdgcn_s_sleep(CS2_DELAY);
      cs_load();
      TS(9);
      GETTAG();
      unsigned zo = WS_ZW + gid*32768 + (t >> 2)*128 + (t & 3)*16, zl = L_EX + t*4;
      unsigned wl = L_EX + ((t & 1)*4*DF_ + (t >> 1))*2, wl0 = t < 2 ? wl + 1024 : wl;
      PIN(zo); PIN(zl); PIN(wl); PIN(wl0);
      u32x4 zv[8];
      f32x4 res = {0,0,0,0};
      auto z_read = [&]() {
        #pragma unroll
        for (int i=0;i<8;i++)
          #pragma unroll
          for (int e=0;e<4;e++) zv[i][e] = ldsr<unsigned>(zl + e*4096 + i*512); };
      touch(rwt, lane*128, gid*8208, L_DUMP + 256 + a*512); touch(rwt, lane*128, gid*8208 + 128, L_DUMP + 512 + a*512);
      __builtin_amdgcn_s_setprio(HPRIO);
      WAITV(2);
      res = cs_sum(res);
      if (a == 0) ldsw<f32x4>(L_ODD + lane*16, res);
      TS(11);
      LBAR();
      z_read();
      if (a == 1) res += ldsr<f32x4>(L_ODD + lane*16);
      #pragma unroll
      for (int i=0;i<3;i++) st16(rw_, zo + i*4096, zv[i]);
      if (t < 4) st16(rw_, WS_STAT + gid*64 + t*16, ldsr<u32x4>(L_RS + t*16));
      #pragma unroll
      for (int i=3;i<8;i++) PIN(zv[i]);
      LBAR(); PF(0);
      #pragma unroll
      for (int i=0;i<5;i++) dma16(rwt, lane*16, gid*8208 + i*1024, L_EX + a*9216 + i*1024);
      if (a == 1) { const unsigned co = WS_CS + (unsigned)((tm*F_ + tn*224 + lane)*4);
        #pragma unroll
        for (int i=0;i<4;i++) if (i < 3 || lane < 32) __builtin_amdgcn_raw_buffer_store_b32(__float_as_uint(res[i]), rw_, co + i*256, 0, 16); }
      BAR(); PF(1);
      #pragma unroll
      for (int i=5;i<9;i++) dma16(rwt, lane*16, gid*8208 + i*1024, L_EX + a*9216 + i*1024);
      st16(rw_, zo + 3*4096, zv[3]);
      BAR(); PF(2);
      WAITV(0);
      st16(rw_, zo + 4*4096, zv[4]); st16(rw_, zo + 5*4096, zv[5]);
      BAR();
      if (t == 0) st8(rw_, WS_F0 + gid*8, tag);
      #pragma unroll
      for (int pr=0; pr<4; pr++) {
        u32x4 v[2];
        #pragma unroll
        for (int i=0;i<2;i++)
          #pragma unroll
          for (int e=0;e<4;e++) { unsigned off = ((pr == 0 && i == 0) ? wl0 : wl) + e*DF_*2 + (pr*2+i)*128; v[i][e] = (unsigned)ldsr<u16>(off) | ((unsigned)ldsr<u16>(off + 9216) << 16); }
        #pragma unroll
        for (int i=0;i<2;i++) st16(rw_, zo + (pr*2+i)*4096 + 64, v[i]);
        if (pr < 2) st16(rw_, zo + (6+pr)*4096, zv[6+pr]);
        BAR();
      }
      PF(3);
      WAITV(0); BAR(); PF(4);
      if (t == 0) st8(rw_, WS_F1 + gid*8, tag);
      BAR(); PF(5);
      { const unsigned fo = WS_F0 + (unsigned)(((lane & 16) ? (lane & 15)*16 + tn : tm*16 + (lane & 15))*8);
        u32x2 fv = ld8(rw_, fo);
        BAR(); PF(6);
        if (__builtin_amdgcn_ballot_w64(fv[0] != tag[0] || fv[1] != tag[1]) != 0) poll1(rw_, fo, true, tag);
        asm volatile("" ::: "memory"); }
      u32x2 cp[16]; const int tt = t < 112 ? t : 111; const unsigned co = WS_CS + (unsigned)((tn*224 + tt*2)*4);
      #pragma unroll
      for (int i=0;i<8;i++) cp[i] = __builtin_amdgcn_raw_buffer_load_b64(rw_, co, i*F_*4, 16);
      BAR(); PF(7);
      #pragma unroll
      for (int i=8;i<16;i++) cp[i] = __builtin_amdgcn_raw_buffer_load_b64(rw_, co, i*F_*4, 16);
      stv = ld8(rw_, WS_STAT + (unsigned)((tm*128 + t)*8));
      unsigned bv = *(const unsigned*)(b1 + tn*224 + tt*2);
      BAR(); PF(8);
      #pragma unroll
      for (int i=0;i<16;i++) csn += __builtin_bit_cast(f32x2, cp[i]);
      bn = (f32x2){bflo(bv), bfhi(bv)} * 1.4426950408889634f; PIN(bn); PIN(csn); PIN(stv);
      BAR();
      BAR();
      if (t < 112) { ldsw<f32x2>(L_BN + t*8, bn); ldsw<f32x2>(L_CN + t*8, csn); }
      ldsw<u32x2>(L_ST + t*8, stv);
      LBAR();
    }
    { const i32x4 r2 = mkrsrc(w2, F_*D_*2);
      const unsigned vb0 = (unsigned)((lane>>4)*D_*2 + (((lane&15) ^ ((lane>>4)<<1))*16)), vb1 = (unsigned)((lane>>4)*D_*2 + (((lane&15) ^ ((4 + (lane>>4))<<1))*16));
      #pragma unroll
      for (int i=0;i<8;i++) dma16(r2, (i&1) ? vb1 : vb0, (ks*1792 + (col + rot)*64 + (hf*8+i)*4)*D_*2 + tn2*256, G2_B + col*G2_SLOT + (hf*8+i)*1024);
      WAITV(8); }
    __builtin_amdgcn_s_setprio(FPRIO);
    lsync(L_FL, w, lane, 1u, 3u);
    TS(3);
    {
      const unsigned mt = C_MAT + hf*10240 + lane*16, sm = (l31 & 1) ? 0x80008000u : 0u, so = hf ? sm : 0u;
      bf16x8 Cx[2][2], Sx[2][2], Nx[2][2];
      #pragma unroll
      for (int s=0;s<2;s++) { Cx[0][s] = xor8(ldsr<bf16x8>(mt + s*2048), so); Sx[0][s] = xor8(ldsr<bf16x8>(mt + s*2048 + 1024), so);
        Cx[1][s] = xor8(Cx[0][s], sm); Sx[1][s] = xor8(Sx[0][s], sm); Nx[0][s] = neg8(Sx[0][s]); Nx[1][s] = neg8(Sx[1][s]); }
      unsigned zb[2][2];
      { const unsigned o = (4*h + q)*512 + (2*(gi&1) + (p >> 1))*128 + (p & 1)*8, sw = (2*q + (gi&1)) & 7;
        #pragma unroll
        for (int ri=0;ri<2;ri++) { unsigned t_ = o + (((col*2 + ri) ^ sw)*16); zb[0][ri] = t_ + (hf ? C_IN1 : C_IN0); zb[1][ri] = t_ + (hf ? C_IN0 : C_IN1); } }
      f32x16 Pr = zero16(), Pi = zero16();
      #pragma unroll
      for (int x=0;x<2;x++)
        #pragma unroll
        for (int s=0;s<2;s++) {
          bf16x8 zr = cat8(trrd(zb[x][0] + s*8192), trrd(zb[x][0] + s*8192 + 4096)), zi = cat8(trrd(zb[x][1] + s*8192), trrd(zb[x][1] + s*8192 + 4096));
          Pr = mfma32(zr, Cx[x][s], Pr); Pi = mfma32(zi, Cx[x][s], Pi);
          Pr = mfma32(zi, Sx[x][s], Pr); Pi = mfma32(zr, Nx[x][s], Pi);
        }
      f32x2 twc[8], tws[8];
      #pragma unroll
      for (int i=0;i<4;i++) { f32x4 c4 = ldsr<f32x4>(C_TW + i*1024 + lane*16), s4 = ldsr<f32x4>(C_TW + 4096 + i*1024 + lane*16);
        twc[2*i] = (f32x2){c4[0], c4[1]}; twc[2*i+1] = (f32x2){c4[2], c4[3]}; tws[2*i] = (f32x2){s4[0], s4[1]}; tws[2*i+1] = (f32x2){s4[2], s4[3]}; }
      unsigned wv[16];
      { const int u = l31 + 32*hf; const unsigned o = ((u >> 3) + 32*h)*128 + (u & 3)*4, ch = 4 + col*2 + ((u >> 2) & 1);
        const unsigned w0 = o + ((ch ^ (u >> 4))*16), w1_ = o + ((ch ^ (u >> 4) ^ 4)*16);
        #pragma unroll
        for (int r=0;r<16;r++) wv[r] = ldsr<unsigned>(((r & 1) ? w1_ : w0) + ((r >> 3) ? C_IN1 : C_IN0) + crow(r & 7, 0)*1024); }
      auto pr2 = [](const f32x16& v, int i) { return (f32x2){v[2*i], v[2*i+1]}; };
      auto pk4 = [](const f32x2* v) { u32x4 p_ = { cvtpk(v[0].x,v[0].y), cvtpk(v[1].x,v[1].y), cvtpk(v[2].x,v[2].y), cvtpk(v[3].x,v[3].y) }; return __builtin_bit_cast(bf16x8, p_); };
      f32x2 a_[8], b_[8];
      #pragma unroll
      for (int i=0;i<8;i++) { a_[i] = pr2(Pr,i)*twc[i] + pr2(Pi,i)*tws[i]; b_[i] = pr2(Pi,i)*twc[i] - pr2(Pr,i)*tws[i]; }
      f32x16 Ur = zero16(), Ui = zero16();
      #pragma unroll
      for (int s=0;s<2;s++) { bf16x8 tr_ = pk4(a_ + 4*s), ti_ = pk4(b_ + 4*s), CA = ldsr<bf16x8>(mt + 4096 + s*2048), SA = ldsr<bf16x8>(mt + 4096 + s*2048 + 1024);
        Ur = mfma32(CA, tr_, Ur); Ui = mfma32(CA, ti_, Ui);
        Ur = mfma32(SA, ti_, Ur); Ui = mfma32(neg8(SA), tr_, Ui); }
      #pragma unroll
      for (int i=0;i<8;i++) { f32x2 wre = {bflo(wv[2*i]), bflo(wv[2*i+1])}, wim = {bfhi(wv[2*i]), bfhi(wv[2*i+1])};
        a_[i] = pr2(Ur,i)*wre - pr2(Ui,i)*wim; b_[i] = pr2(Ur,i)*wim + pr2(Ui,i)*wre; }
      f32x16 Qr = zero16(), Qi = zero16();
      { const bf16x8 CB0 = ldsr<bf16x8>(mt + 8192), SB0 = ldsr<bf16x8>(mt + 9216);
        #pragma unroll
        for (int s=0;s<2;s++) { bf16x8 yr = pk4(a_ + 4*s), yi = pk4(b_ + 4*s), CB = s ? xor8(CB0, sm) : CB0, SB_ = s ? xor8(SB0, sm) : SB0;
          Qr = mfma32(yr, CB, Qr); Qi = mfma32(yi, CB, Qi);
          Qr = mfma32(yi, neg8(SB_), Qr); Qi = mfma32(yr, SB_, Qi); } }
      #pragma unroll
      for (int i=0;i<8;i++) { a_[i] = pr2(Qr,i)*twc[i] - pr2(Qi,i)*tws[i]; b_[i] = pr2(Qi,i)*twc[i] + pr2(Qr,i)*tws[i]; }
      f32x16 Zr = zero16(), Zi = zero16();
      { bf16x8 Rr[2], Ri[2];
        const unsigned ex = C_EXC + w*4096 + lane*16;
        #pragma unroll
        for (int s=0;s<2;s++) { Rr[s] = pk4(a_ + 4*s); Ri[s] = pk4(b_ + 4*s); ldsw<bf16x8>(ex + s*1024, Rr[s]); ldsw<bf16x8>(ex + 2048 + s*1024, Ri[s]); }
        asm volatile("" ::: "memory");
        if (lane == 0) *(volatile LAS unsigned*)(size_t)(L_FL + w*4) = 2u;
        #pragma unroll
        for (int s=0;s<2;s++) { Zr = mfma32(Rr[s], Cx[0][s], Zr); Zi = mfma32(Ri[s], Cx[0][s], Zi); Zr = mfma32(Ri[s], Nx[0][s], Zr); Zi = mfma32(Rr[s], Sx[0][s], Zi); }
        lwait(L_FL, lane, 2u, 15u);
        #pragma unroll
        for (int s=0;s<2;s++) { Rr[s] = ldsr<bf16x8>((ex ^ 4096) + s*1024); Ri[s] = ldsr<bf16x8>((ex ^ 4096) + 2048 + s*1024); }
        #pragma unroll
        for (int s=0;s<2;s++) { Zr = mfma32(Rr[s], Cx[1][s], Zr); Zi = mfma32(Ri[s], Cx[1][s], Zi); Zr = mfma32(Ri[s], Nx[1][s], Zr); Zi = mfma32(Rr[s], Sx[1][s], Zi); } }
      { const unsigned ob = C_OUT + (l31 + 32*hf)*256, c0 = (col*2 + h) ^ (l31 & 15);
        #pragma unroll
        for (int ga=0; ga<4; ga++)
          ldsw<u32x4>(ob + ((c0 ^ (4*ga))*16), (u32x4){cvtpk(Zr[4*ga], Zr[4*ga+1]), cvtpk(Zr[4*ga+2], Zr[4*ga+3]), cvtpk(Zi[4*ga], Zi[4*ga+1]), cvtpk(Zi[4*ga+2], Zi[4*ga+3])}); }
      lsync(L_FL, w, lane, 3u, w < 2 ? 0u : 15u);
      if (w >= 2) { const unsigned la = C_OUT + ((t ^ ((t >> 4) & 7))*16), ga_ = WS_Z2 + gid*16384 + t*16;
        #pragma unroll
        for (int i=0;i<8;i++) st16(rw_, ga_ + i*2048, ldsr<u32x4>((la ^ ((i&1)*128)) + i*2048)); }
    }
    TS(4);
    if (w < 2) {
      const i32x4 r2 = mkrsrc(w2, F_*D_*2);
      unsigned va[8], vb[8];
      #pragma unroll
      for (int i=0;i<8;i++) { int pc = w*8+i; va[i] = WS_H + (unsigned)(((tm*8 + (pc>>1))*112 + ks*56 + (pc&1))*1024 + lane*16);
        int r = pc*4 + (lane>>4); int c = (lane&15) ^ ((r&7)<<1); vb[i] = (unsigned)((ks*1792 + r)*D_*2 + tn2*256 + c*16); }
      auto issueB = [&](int k) { int kk = k + rot; if (kk >= 28) kk -= 28;
        #pragma unroll
        for (int i=0;i<8;i++) dma16(r2, vb[i], kk*64*D_*2, G2_B + (k & 3)*G2_SLOT + w*8192 + i*1024); };
      auto issueA = [&](int k) { int kk = k + rot; if (kk >= 28) kk -= 28;
        #pragma unroll
        for (int i=0;i<8;i++) dma16s(rz, va[i], kk*2048, G2_A + (k & 3)*G2_SLOT + w*8192 + i*1024); };
      __builtin_amdgcn_s_setprio(2);
      WAITV(0); LBAR();
      TS(5);
      if (ROT) {
      issueA(2); issueB(2);
      BAR();
      issueB(3); issueB(4);
      WAITV(16); BAR();
      poll1(rw_, WS_F3 + (unsigned)((tm*16 + ks*8 + (lane & 7))*128), true, tag);
      TS(12);
      issueA(3); issueA(4);
      WAITV(8); BAR();
      #pragma unroll 1
      for (int k = 5; k < 28; k++) { issueB(k); issueA(k); WAITV(16); BAR(); }
      } else {
      issueB(2); issueA(2); BAR(); issueB(3); issueA(3);
      #pragma unroll 1
      for (int k = 4; k < 28; k++) { issueB(k); issueA(k); WAITV(32); BAR(); }
      WAITV(16); BAR();
      }
      __builtin_amdgcn_s_setprio(0);
    } else {
      __builtin_amdgcn_s_setprio(1);
      pfp = (const char*)w2 + ((w & 1)*1792 + 1008)*2048 + b*6272;
      WAITL0(); PF(5);
      WAITV(8); BAR(); PF(6);
      BAR();
      WAITV(0); BAR();
      if (a == 0 && lane == 0) st8(rw_, WS_F2 + gid*8, tag);
      #pragma unroll 1
      for (int k = 0; k < 5; k++) { PF(0); pfp += 896; BAR(); BAR(); }
      #pragma unroll 1
      for (int k = 0; k < 8; k++) BAR();
      { u32x4 fa = ld16(rw_, WS_F2 + lane*16), fb = ld16(rw_, WS_F2 + 1024 + lane*16);
        BAR(); BAR();
        if (bad256(fa, fb, tag)) poll256(rw_, WS_F2, lane, tag); asm volatile("" ::: "memory"); }
      BAR(); BAR(); BAR(); BAR();
      #pragma unroll
      for (int i=0;i<8;i++) { int pc = a*8 + i; dma16s(rz, (unsigned)((lane >> 2)*16384 + (((lane & 3) ^ (pc & 3))*16)), WS_Z2 + pc*16*16384 + gid*64, E_Z + pc*1024); }
    }
    if (w < 2) WAITV(0);
    else {
      #pragma unroll
      for (int i=0;i<6;i++) dma16(rt, lane*16, (28 + a*6 + i)*1024, E_K + (a*6 + i)*1024); }
    BAR();
    WAITV(0);
    __builtin_amdgcn_s_setprio(0);
    BAR();
    TS(6);
    {
      const int rq = w >> 1; const bool hi2 = w & 1;
      const bf16x8 C16 = ldsr<bf16x8>(E_K + lane*16), S16 = ldsr<bf16x8>(E_K + 1024 + lane*16);
      bf16x8 C2[2], S2[2];
      C2[0] = ldsr<bf16x8>(E_K + 2048 + lane*16); S2[0] = ldsr<bf16x8>(E_K + 3072 + lane*16);
      { unsigned m = (l31 & 1) ? 0x80008000u : 0u; C2[1] = xor8(C2[0], m); S2[1] = xor8(S2[0], m); }
      float tc[16], ts_[16];
      #pragma unroll
      for (int i=0;i<4;i++) { f32x4 c4 = ldsr<f32x4>(E_K + 4096 + i*1024 + lane*16), s4 = ldsr<f32x4>(E_K + 8192 + i*1024 + lane*16);
        #pragma unroll
        for (int e=0;e<4;e++) { tc[4*i+e] = c4[e]; ts_[4*i+e] = s4[e]; } }
      float ra[2][16], rb_[2][16];
      #pragma unroll
      for (int T=0;T<4;T++) {
        s16x4 v[2][2];
        #pragma unroll
        for (int t=0;t<2;t++) { int k2 = 8*t + 4*(gi>>1) + q; int P = 4*(gi&1) + p; int cw = 16*k2 + 4*T + (P >> 1);
          #pragma unroll
          for (int ri=0;ri<2;ri++) v[ri][t] = trrd(E_Z + cw*64 + ((((P & 1)*2 + rq) ^ (k2 & 3))*16) + ri*8); }
        bf16x8 zr = cat8(v[0][0], v[0][1]), zi = cat8(v[1][0], v[1][1]);
        f32x16 Qr = mfma32(zr, C16, zero16()), Qi = mfma32(zi, C16, zero16());
        Qr = mfma32(neg8(zi), S16, Qr); Qi = mfma32(zr, S16, Qi);
        #pragma unroll
        for (int row=0; row<2; row++)
          #pragma unroll
          for (int gq=0; gq<4; gq++) { float qr = hi2 ? Qr[4*gq + 2 + row] : Qr[4*gq + row], qi = hi2 ? Qi[4*gq + 2 + row] : Qi[4*gq + row];
            if (T == 0 && gq == 0) { unsigned z0 = ldsr<unsigned>(E_Z + rq*16 + (w & 1)*4); float z = row ? bfhi(z0) : bflo(z0); qr += (h == 0) ? ((l31 & 1) ? -1.5f*z : -0.5f*z) : 0.f; }
            int e = 4*T + gq; ra[row][e] = qr*tc[e] - qi*ts_[e]; rb_[row][e] = qi*tc[e] + qr*ts_[e]; }
      }
      #pragma unroll
      for (int row=0; row<2; row++) {
        f32x16 Y = zero16();
        #pragma unroll
        for (int s=0;s<2;s++) { Y = mfma32(pack8(ra[row] + 8*s), C2[s], Y); Y = mfma32(pack8(rb_[row] + 8*s), S2[s], Y); }
        #pragma unroll
        for (int ga=0; ga<4; ga++) ldsw<u32x2>(E_Y + (2*w + row)*2048 + l31*64 + ((ga ^ ((l31 >> 1) & 3))*16) + h*8, (u32x2){cvtpk(Y[4*ga], Y[4*ga+1]), cvtpk(Y[4*ga+2], Y[4*ga+3])});
      }
    }
    TS(13);
    if (w == 3) poll1(rw_, WS_F4 + (unsigned)(((tm*16 + (lane & 15))*2 + (tn >> 3))*128), true, tag);
  }
  {
    const int t = threadIdx.x;
    const rsrc_t ro = __builtin_amdgcn_make_buffer_rsrc(out, 0, S_*D_*2, 0x00020000);
    u32x4 xv[2];
    #pragma unroll
    for (int i=0;i<2;i++) xv[i] = *(const u32x4*)((const char*)x + gid*16384 + (t + 512*i)*16);
    LBAR();
    TS(7);
    u32x4 p0[2], p1[2], yv[2];
    #pragma unroll
    for (int i=0;i<2;i++) { unsigned o = WS_PART + gid*16384 + (t + 512*i)*16; p0[i] = ld16(rw_, o); p1[i] = ld16(rw_, o + (4u<<20)); }
    WAITV(0); TS(11);
    #pragma unroll
    for (int i=0;i<2;i++) { int ch = t + 512*i; yv[i] = ldsr<u32x4>(E_Y + ((ch & ~3) | ((ch & 3) ^ ((ch >> 3) & 3)))*16); }
    #pragma unroll
    for (int i=0;i<2;i++) { u32x4 o;
      #pragma unroll
      for (int e=0;e<4;e++) { unsigned xe = xv[i][e], ye = yv[i][e], pe = p0[i][e], qe = p1[i][e];
        o[e] = cvtpk(fmaf(bflo(pe) + bflo(qe), 0.6931471805599453f, bflo(xe) + bflo(ye)), fmaf(bfhi(pe) + bfhi(qe), 0.6931471805599453f, bfhi(xe) + bfhi(ye))); }
      st16(ro, gid*16384 + (t + 512*i)*16, o); }
    TS(1);
  }
}

extern "C" void kernel_launch(void* const* d_in, const int* in_sizes, int n_in, void* d_out, int out_size, void* d_ws, size_t ws_size, hipStream_t stream) {
  (void)hipFuncSetAttribute((const void*)k_all, hipFuncAttributeMaxDynamicSharedMemorySize, L_TOT);
  k_all<<<256,512,L_TOT,stream>>>((const u16*)d_in[0], (const u16*)d_in[1], (const u16*)d_in[2], (const u16*)d_in[4], (const u16*)d_in[5], (const u16*)d_in[6], (char*)d_ws, (u16*)d_out);
}
```
